```python
import torch

def _softmax(x, axis=-1):
    return torch.softmax(x, dim=axis)
def _gelu(x):
    return torch.nn.functional.gelu(x, approximate="tanh")
def _silu(x):
    return torch.nn.functional.silu(x)
def _sigmoid(x):
    return torch.sigmoid(x)
def _layernorm(x, axis=-1, eps=1e-5):
    m = x.mean(dim=axis, keepdim=True)
    v = x.var(dim=axis, keepdim=True, unbiased=False)
    return (x - m) / torch.sqrt(v + eps)
def _scatter_add(tgt, idx, src, axis=0):
    return tgt.clone().index_add_(axis, idx, src)
def _segment_sum(src, seg, n):
    out = torch.zeros((n,) + src.shape[1:], dtype=src.dtype)
    return out.index_add_(0, seg, src)
def _pad(x, pw):
    flat = tuple(v for pair in reversed(pw) for v in pair)
    return torch.nn.functional.pad(x, flat)

kernel_name = 'sequential_preLN__sliding_block__glu_gated_bilinear__prompt'

def setup_inputs():

    _w = (lambda a: (lambda t: t.to(torch.bfloat16) if t.is_floating_point() else t)(torch.as_tensor(a)))
    def _A_setup():
        B, S, D = 1, 2048, 640
        return {
            "x": _w((lambda *s: torch.randn(*s, dtype=torch.bfloat16) * 0.05)(B, S, D)),
            "wqkv": _w((lambda *s: torch.randn(*s, dtype=torch.bfloat16) * 0.05)(D, 3 * D)),
            "wo": _w((lambda *s: torch.randn(*s, dtype=torch.bfloat16) * 0.05)(D, D)),
        }
    _A_in = _A_setup()

    def _B_setup():
        B, S, D, F = 1, 2048, 640, 1536
        return {
            "x": _w((lambda *s: torch.randn(*s, dtype=torch.bfloat16) * 0.05)(B, S, D)),
            "w_gate": _w((lambda *s: torch.randn(*s, dtype=torch.bfloat16) * 0.8)(D, F) * (D ** -0.5)),
            "w_up": _w((lambda *s: torch.randn(*s, dtype=torch.bfloat16) * 0.8)(D, F) * (D ** -0.5)),
            "w_down": _w((lambda *s: torch.randn(*s, dtype=torch.bfloat16) * 0.8)(F, D) * (F ** -0.5)),
        }
    _B_in = _B_setup()

    return {
        "A_x": _A_in["x"],
        "A_wqkv": _A_in["wqkv"],
        "A_wo": _A_in["wo"],
        "B_x": _B_in["x"],
        "B_w_gate": _B_in["w_gate"],
        "B_w_up": _B_in["w_up"],
        "B_w_down": _B_in["w_down"],
    }

def reference(A_x, A_wqkv, A_wo, B_x, B_w_gate, B_w_up, B_w_down):
    def _A_ref(x, wqkv, wo):
        B, S, D = x.shape
        H = 32; Dh = D // H; W = 256
        qkv = (lambda x, *d: x.permute(*d))((x @ wqkv).reshape(B, S, 3, H, Dh), 2, 0, 3, 1, 4)
        q, k, v = qkv[0], qkv[1], qkv[2]
        scores = torch.einsum("bhqd,bhkd->bhqk", q, k) * (Dh ** -0.5)
        idx = torch.arange(S)
        mask = (idx[:, None] - idx[None, :] >= 0) & (idx[:, None] - idx[None, :] < W)
        scores = torch.where(mask[None, None], scores, -1e9)
        p = _softmax(scores, axis=-1)
        o = torch.einsum("bhqk,bhkd->bhqd", p, v)
        return (lambda x, *d: x.permute(*d))(o, 0, 2, 1, 3).reshape(B, S, D) @ wo

    def _B_ref(x, w_gate, w_up, w_down):
        g = torch.einsum("bsd,df->bsf", x, w_gate)
        u = torch.einsum("bsd,df->bsf", x, w_up)
        h = g * u
        return torch.einsum("bsf,fd->bsd", h, w_down)

    x = A_x
    h = x + _A_ref(_layernorm(x, axis=-1), A_wqkv, A_wo)
    h2 = h + _B_ref(_layernorm(h, axis=-1), B_w_gate, B_w_up, B_w_down)
    return h2
```

```cpp
#include <hip/hip_runtime.h>
#include <hip/hip_bf16.h>
typedef unsigned short u16;
__device__ __forceinline__ float bf2f(u16 b){ return __uint_as_float(((unsigned)b)<<16); }
__device__ __forceinline__ u16 f2bf(float f){ unsigned u=__float_as_uint(f); u += 0x7fffu + ((u>>16)&1u); return (u16)(u>>16); }
constexpr int S=2048,D=640,H=32,DH=20,W=256,F=1536;
__global__ void ln_k(const u16* x, u16* y){
  int r=blockIdx.x, l=threadIdx.x; float v[10]; float s=0;
  for(int i=0;i<10;i++){ v[i]=bf2f(x[r*D+l+64*i]); s+=v[i]; }
  for(int o=32;o;o>>=1) s+=__shfl_xor(s,o);
  float m=s/D; float q=0; for(int i=0;i<10;i++){ float d=v[i]-m; q+=d*d; }
  for(int o=32;o;o>>=1) q+=__shfl_xor(q,o);
  float rs=rsqrtf(q/D+1e-5f);
  for(int i=0;i<10;i++) y[r*D+l+64*i]=f2bf((v[i]-m)*rs);
}
__global__ void gemm_k(const u16* A,const u16* B,const u16* B2,const u16* res,u16* C,int N,int K,int mode){
  int n=blockIdx.x*64+threadIdx.x; int r=blockIdx.y; if(n>=N) return;
  float a=0,a2=0;
  for(int k=0;k<K;k++){ float x=bf2f(A[r*K+k]); a+=x*bf2f(B[k*N+n]); if(mode==2) a2+=x*bf2f(B2[k*N+n]); }
  if(mode==1) a+=bf2f(res[r*N+n]); if(mode==2) a*=a2;
  C[r*N+n]=f2bf(a);
}
__global__ void attn_k(const u16* qkv,u16* o){
  int q=blockIdx.x*64+threadIdx.x, h=blockIdx.y; float qv[DH];
  #pragma unroll
  for(int d=0;d<DH;d++) qv[d]=bf2f(qkv[q*3*D+h*DH+d])*0.22360679775f;
  int k0=q-W+1<0?0:q-W+1; float m=-1e30f;
  for(int k=k0;k<=q;k++){ float s=0;
    #pragma unroll
    for(int d=0;d<DH;d++) s+=qv[d]*bf2f(qkv[k*3*D+D+h*DH+d]); m=fmaxf(m,s);}
  float l=0, acc[DH];
  #pragma unroll
  for(int d=0;d<DH;d++) acc[d]=0;
  for(int k=k0;k<=q;k++){ float s=0;
    #pragma unroll
    for(int d=0;d<DH;d++) s+=qv[d]*bf2f(qkv[k*3*D+D+h*DH+d]); float p=__expf(s-m); l+=p;
    #pragma unroll
    for(int d=0;d<DH;d++) acc[d]+=p*bf2f(qkv[k*3*D+2*D+h*DH+d]);}
  #pragma unroll
  for(int d=0;d<DH;d++) o[q*D+h*DH+d]=f2bf(acc[d]/l);
}
extern "C" void kernel_launch(void* const* d_in, const int* in_sizes, int n_in, void* d_out, int out_size, void* d_ws, size_t ws_size, hipStream_t stream) {
  const u16* x=(const u16*)d_in[0]; const u16* wqkv=(const u16*)d_in[1]; const u16* wo=(const u16*)d_in[2];
  const u16* wg=(const u16*)d_in[4]; const u16* wu=(const u16*)d_in[5]; const u16* wd=(const u16*)d_in[6];
  u16* ws=(u16*)d_ws; u16* ln=ws; u16* qkv=ln+S*D; u16* o=qkv+S*3*D; u16* h=o+S*D; u16* hh=h+S*D;
  ln_k<<<S,64,0,stream>>>(x,ln);
  gemm_k<<<dim3(3*D/64,S),64,0,stream>>>(ln,wqkv,nullptr,nullptr,qkv,3*D,D,0);
  attn_k<<<dim3(S/64,H),64,0,stream>>>(qkv,o);
  gemm_k<<<dim3(D/64,S),64,0,stream>>>(o,wo,nullptr,x,h,D,D,1);
  ln_k<<<S,64,0,stream>>>(h,ln);
  gemm_k<<<dim3(F/64,S),64,0,stream>>>(ln,wg,wu,nullptr,hh,F,D,2);
  gemm_k<<<dim3(D/64,S),64,0,stream>>>(hh,wd,nullptr,h,(u16*)d_out,D,F,1);
}
```

```cpp
#include <hip/hip_runtime.h>
#include <stdint.h>

typedef unsigned short u16;
typedef unsigned int u32;
typedef unsigned long long u64;
typedef __bf16 bf16x8 __attribute__((ext_vector_type(8)));
typedef __bf16 bf16x2 __attribute__((ext_vector_type(2)));
typedef short s16x4 __attribute__((ext_vector_type(4)));
typedef float f32x4 __attribute__((ext_vector_type(4)));
typedef float f32x2 __attribute__((ext_vector_type(2)));
typedef u32 u32x4 __attribute__((ext_vector_type(4)));
typedef u32 u32x2 __attribute__((ext_vector_type(2)));
#define LAS __attribute__((address_space(3)))
#define GAS __attribute__((address_space(1)))
#define DI __device__ __forceinline__
#define RLX_AGENT __ATOMIC_RELAXED, __HIP_MEMORY_SCOPE_AGENT

#define STAMPS 0

constexpr u32 WS_EPOCH = 0;
constexpr u32 WS_FLAG = 4096;
constexpr u32 WS_QKV = WS_FLAG + 4 * 256 * 256;
constexpr u32 WS_O = WS_QKV + 16 * 240 * 2048;
constexpr u32 WS_H = WS_O + 16 * 80 * 2048;
constexpr u32 WS_HH = WS_H + 16 * 80 * 2048;
constexpr u32 WS_ST = WS_HH + 16 * 192 * 2048;
constexpr u32 WS_END = WS_ST + 16 * 8 * 1024;
constexpr u32 WS_STAMP = WS_END;
constexpr u64 TAG_STEP = 0x9E3779B97F4A7C15ull;

constexpr u32 LDS_BYTES = 163840;
constexpr u32 L_MISC = 161792;
constexpr u32 L_K = 0, KCH = 6144;
constexpr u32 L_V = 6 * KCH, VCH = 6272;
constexpr u32 L_WOB = L_MISC - 102400;
constexpr u32 L_GB = 0, GB_SLOT = 24576;
constexpr u32 L_GA = 4 * GB_SLOT, GA_SLOT = 16384;
constexpr u32 L_ST = L_GA + 3 * GA_SLOT;
constexpr u32 L_RED = L_GA;
constexpr float LN_EPS = 1e-5f;

extern __shared__ __attribute__((aligned(16))) char smem[];

DI u32x4 lds128(u32 off) { return *(LAS const u32x4*)(smem + off); }
DI u32x2 ldstr(u32 off) { return __builtin_bit_cast(u32x2, __builtin_amdgcn_ds_read_tr16_b64_v4i16((LAS s16x4*)(smem + off))); }
DI u32x4 ldstr2(u32 lo, u32 hi) { const u32x2 a = ldstr(lo), b = ldstr(hi); return u32x4{a[0], a[1], b[0], b[1]}; }
DI f32x4 mfma(u32x4 a, u32x4 b, f32x4 c) { return __builtin_amdgcn_mfma_f32_16x16x32_bf16(__builtin_bit_cast(bf16x8, a), __builtin_bit_cast(bf16x8, b), c, 0, 0, 0); }
DI f32x4 mfma16(u32x2 a, u32x2 b, f32x4 c) { return __builtin_amdgcn_mfma_f32_16x16x16bf16_1k(__builtin_bit_cast(s16x4, a), __builtin_bit_cast(s16x4, b), c, 0, 0, 0); }
DI float dot2(u32 a, u32 b, float c) { return __builtin_amdgcn_fdot2_f32_bf16(__builtin_bit_cast(bf16x2, a), __builtin_bit_cast(bf16x2, b), c, false); }
DI u32 pack2(float lo, float hi) { const f32x2 v = {lo, hi}; return __builtin_bit_cast(u32, __builtin_convertvector(v, bf16x2)); }
DI float bflo(u32 v) { return __uint_as_float(v << 16); }
DI float bfhi(u32 v) { return __uint_as_float(v & 0xffff0000u); }
constexpr u32 ONES2 = 0x3f803f80u;
DI float sum8(u32x4 a, float s) { const u32 a0 = a[0], a1 = a[1], a2 = a[2], a3 = a[3]; return dot2(a3, ONES2, dot2(a2, ONES2, dot2(a1, ONES2, dot2(a0, ONES2, s)))); }
DI float sq8(u32x4 a, float s) { const u32 a0 = a[0], a1 = a[1], a2 = a[2], a3 = a[3]; return dot2(a3, a3, dot2(a2, a2, dot2(a1, a1, dot2(a0, a0, s)))); }
DI float gsum(float x) {
  auto a = __builtin_amdgcn_permlane16_swap(__float_as_uint(x), __float_as_uint(x), false, false);
  x = __uint_as_float(a[0]) + __uint_as_float(a[1]);
  auto b = __builtin_amdgcn_permlane32_swap(__float_as_uint(x), __float_as_uint(x), false, false);
  return __uint_as_float(b[0]) + __uint_as_float(b[1]);
}
DI float gmax(float x) {
  auto a = __builtin_amdgcn_permlane16_swap(__float_as_uint(x), __float_as_uint(x), false, false);
  x = __builtin_fmaxf(__uint_as_float(a[0]), __uint_as_float(a[1]));
  auto b = __builtin_amdgcn_permlane32_swap(__float_as_uint(x), __float_as_uint(x), false, false);
  return __builtin_fmaxf(__uint_as_float(b[0]), __uint_as_float(b[1]));
}
DI u32x4 quad2chunk(u32x2 A, u32x2 B) {
  const u32 a0 = A[0], a1 = A[1], b0 = B[0], b1 = B[1];
  auto s0 = __builtin_amdgcn_permlane16_swap(a0, b0, false, false);
  auto s1 = __builtin_amdgcn_permlane16_swap(a1, b1, false, false);
  return u32x4{s0[0], s1[0], s0[1], s1[1]};
}
DI void dma(const void* sbase, u32 voff, u32 lds) {
  asm volatile("s_mov_b32 m0, %2\n\ts_nop 0\n\tglobal_load_lds_dwordx4 %0, %1" ::"v"(voff), "s"(sbase), "s"(lds));
}
DI void dma_sc1(const void* sbase, u32 voff, u32 lds) {
  asm volatile("s_mov_b32 m0, %2\n\ts_nop 0\n\tglobal_load_lds_dwordx4 %0, %1 sc1" ::"v"(voff), "s"(sbase), "s"(lds));
}
DI void gld_sc1(u32x4& dst, const void* sbase, u32 voff) {
  asm volatile("global_load_dwordx4 %0, %1, %2 sc1" : "=v"(dst) : "v"(voff), "s"(sbase));
}
template <int N> DI void vm_wait() { asm volatile("s_waitcnt vmcnt(%0)" ::"n"(N) : "memory"); }
template <int N> DI void vm_wait_use(u32x4& r) { asm volatile("s_waitcnt vmcnt(%1)" : "+v"(r) : "n"(N)); }
DI void bar() { asm volatile("s_waitcnt lgkmcnt(0)\n\ts_barrier" ::: "memory"); }
DI void st128_sc1(__amdgpu_buffer_rsrc_t rs, u32 off, u32x4 v) { __builtin_amdgcn_raw_buffer_store_b128(v, rs, (int)off, 0, 16); }
DI void st64_sc1(__amdgpu_buffer_rsrc_t rs, u32 off, u32x2 v) { __builtin_amdgcn_raw_buffer_store_b64(v, rs, (int)off, 0, 16); }

DI void poll_flags(const char* ws, u32 off, u64 tag) {
  GAS u64* a = (GAS u64*)(ws + off);
  for (u32 spins = 0; spins < (1u << 20); ++spins) {
    const u64 v = __hip_atomic_load(a, RLX_AGENT);
    if (__all(v == tag)) break;
  }
  __builtin_amdgcn_fence(__ATOMIC_ACQUIRE, "wavefront");
}
DI u32 flag_off(int phase, int p, int j) { return WS_FLAG + (u32)((phase * 256 + p * 16 + j) * 256); }

#define STAMP(i) do {} while (0)

__global__ __launch_bounds__(512, 2) void fused_block(const char* __restrict__ x, const char* __restrict__ wqkv, const char* __restrict__ wo,
                                                      const char* __restrict__ wg, const char* __restrict__ wu, const char* __restrict__ wd,
                                                      char* __restrict__ out, char* ws) {
  const int tid = threadIdx.x, lane = tid & 63, w = __builtin_amdgcn_readfirstlane(tid >> 6);
  const int r16 = lane & 15, g = lane >> 4, q4 = r16 >> 2, pq = r16 & 3;
  const int bx = blockIdx.x, xcd = bx & 7, rr = bx >> 3;
  const int p = (xcd >> 1) * 4 + (rr >> 3), j = (xcd & 1) * 8 + (rr & 7);
  const __amdgpu_buffer_rsrc_t rs = __builtin_amdgcn_make_buffer_rsrc(ws, 0, WS_END, 0x00020000);
  STAMP(0);
  const u64 epoch_v = __hip_atomic_load((GAS u64*)(ws + WS_EPOCH), RLX_AGENT);

  if (j < 15) {
    const int wm = w >> 2, wn = w & 3;
    u32 voffA[2], voffB[2];
#pragma unroll
    for (int i = 0; i < 2; ++i) {
      const int ra = 16 * w + 8 * i + (lane >> 3);
      voffA[i] = ra * 1280 + (((lane & 7) ^ ((ra >> 1) & 7)) << 4);
      const int rb = 8 * w + 4 * i + g, f = (rb & 3) | (((rb >> 3) & 1) << 2);
      voffB[i] = rb * 3840 + (((((r16 >> 1) ^ f) << 1) | (r16 & 1)) << 4);
    }
    const char* xa = x + (size_t)p * 128 * 1280;
    const char* wb = wqkv + j * 256;
    const int rot = ((rr & 7) + ((rr & 7) >> 2) + ((5 * (rr >> 3)) >> 1)) % 10;
    auto stage = [&](int c) {
      int kc = c + rot; kc = kc >= 10 ? kc - 10 : kc;
      const u32 slot = (c & 3) * 32768;
      const char* a = xa + kc * 128; const char* b = wb + (size_t)kc * 64 * 3840;
      dma(a, voffA[0], slot + w * 2048); dma(a, voffA[1], slot + w * 2048 + 1024);
      dma(b, voffB[0], slot + 16384 + w * 2048); dma(b, voffB[1], slot + 16384 + w * 2048 + 1024);
    };
    stage(0); stage(1); stage(2);
    u32 adA[4][2], adB[2];
#pragma unroll
    for (int m = 0; m < 4; ++m)
#pragma unroll
      for (int ks = 0; ks < 2; ++ks) adA[m][ks] = (64 * wm + 16 * ((m + wn) & 3) + r16) * 128 + (((4 * ks + g) ^ ((r16 >> 1) & 7)) << 4);
#pragma unroll
    for (int n = 0; n < 2; ++n) adB[n] = 16384 + (8 * g + q4) * 256 + (((2 * wn + (n ^ wm)) ^ (q4 | ((g & 1) << 2))) << 5) + pq * 8;
    f32x4 acc[4][2];
#pragma unroll
    for (int m = 0; m < 4; ++m) { acc[m][0] = f32x4{0, 0, 0, 0}; acc[m][1] = f32x4{0, 0, 0, 0}; }
    float rsum = 0.f, rsq = 0.f, csum = 0.f;
#pragma unroll
    for (int c = 0; c < 10; ++c) {
      if (c <= 7) vm_wait<8>(); else if (c == 8) vm_wait<4>(); else vm_wait<0>();
      bar();
      if (c + 3 < 10) stage(c + 3);
      const u32 slot = (c & 3) * 32768;
#pragma unroll
      for (int ks = 0; ks < 2; ++ks) {
        u32x4 a[4], b[2];
#pragma unroll
        for (int n = 0; n < 2; ++n) b[n] = ldstr2(slot + adB[n] + ks * 8192, slot + adB[n] + ks * 8192 + 1024);
#pragma unroll
        for (int m = 0; m < 4; ++m) a[m] = lds128(slot + adA[m][ks]);
#pragma unroll
        for (int m = 0; m < 4; ++m)
#pragma unroll
          for (int n = 0; n < 2; ++n) acc[m][n] = mfma(b[n], a[m], acc[m][n]);
        rsum = sum8(a[0], rsum); rsq = sq8(a[0], rsq); csum = sum8(b[0], csum);
      }
    }
    STAMP(1);
    rsum = gsum(rsum); rsq = gsum(rsq); csum = gsum(csum);
    if (g == 0) {
      const float mean = rsum * (1.f / 640.f), var = __builtin_fmaxf(rsq * (1.f / 640.f) - mean * mean, 0.f);
      *(LAS f32x2*)(smem + L_MISC + (64 * wm + 16 * wn + r16) * 8) = f32x2{mean, __builtin_amdgcn_rsqf(var + LN_EPS)};
      *(LAS float*)(smem + L_MISC + 1024 + (32 * wn + 16 * wm + r16) * 4) = csum;
    }
    bar();
    const float qs = j < 5 ? 0.22360679774997896f * 1.4426950408889634f : 1.f;
    f32x4 cs[2];
#pragma unroll
    for (int n = 0; n < 2; ++n) cs[n] = *(LAS const f32x4*)(smem + L_MISC + 1024 + (32 * wn + 16 * (n ^ wm) + 4 * g) * 4);
#pragma unroll
    for (int m = 0; m < 4; ++m) {
      const int row = 64 * wm + 16 * ((m + wn) & 3) + r16;
      const f32x2 st = *(LAS const f32x2*)(smem + L_MISC + row * 8);
      const float sc = st[1] * qs;
      u32x2 pk[2];
#pragma unroll
      for (int n = 0; n < 2; ++n) {
        const f32x4 v = (acc[m][n] - st[0] * cs[n]) * sc;
        pk[n] = u32x2{pack2(v[0], v[1]), pack2(v[2], v[3])};
      }
      const u32x4 o = quad2chunk(pk[0], pk[1]);
      const int chunk = 16 * j + 4 * wn + 2 * ((g & 1) ^ wm) + (g >> 1);
      st128_sc1(rs, WS_QKV + (p * 240 + chunk) * 2048 + row * 16, o);
    }
  }
  const u64 tag = (((u64)(u32)__builtin_amdgcn_readfirstlane((u32)(epoch_v >> 32)) << 32) | (u32)__builtin_amdgcn_readfirstlane((u32)epoch_v)) + TAG_STEP;
  vm_wait<0>();
  bar();
  STAMP(2);
  if (w == 7) {
    if (lane == 0) __hip_atomic_store((GAS u64*)(ws + flag_off(0, p, j)), tag, RLX_AGENT);
    const int e = r16 < 14 ? r16 : 0, sec = e < 2 ? 0 : (e < 8 ? 1 : 2), i = e < 2 ? e : (e - 2) % 6;
    int pp = e < 2 ? p : p - (i >> 1); pp = pp < 0 ? 0 : pp;
    poll_flags(ws, flag_off(0, pp, (80 * sec + 5 * j + 4 * (i & 1)) >> 4), tag);
  } else if (tid < 384) {
    const bool valid = 128 * (p - 2) + tid >= 0;
    *(LAS u32x4*)(smem + L_K + 5 * KCH + tid * 16) = u32x4{valid ? 0u : 0xf149u, 0u, ONES2, ONES2};
  }
  bar();
  STAMP(3);

  u32 voffW[5];
#pragma unroll
  for (int i = 0; i < 5; ++i) {
    const int s = 64 * i + lane, rw = s / 10, c = s - rw * 10;
    voffW[i] = (rw ^ (((rw >> 3) & 1) << 2)) * 1280 + c * 16;
  }
  const int half = j >> 3, slice = j & 7;

  f32x4 hacc[5];
  {
    u32x4 qf[2];
    const char* qkv = ws + WS_QKV;
#pragma unroll
    for (int h = 0; h < 2; ++h) {
      const int ch = g == 3 ? 0 : 2 * h + g;
      gld_sc1(qf[h], qkv, (p * 240 + 5 * j + ch) * 2048 + (16 * w + r16) * 16);
    }
#pragma unroll
    for (int i = 0; i < 8; ++i) {
      const int id = w + 8 * i;
      if (id < 60) {
        const int kv = id >= 30, r = id - 30 * kv, c = r / 6, r2 = r - 6 * c;
        int pp = p - 2 + (r2 >> 1); pp = pp < 0 ? 0 : pp;
        dma_sc1(qkv + (size_t)((pp * 240 + 80 * (1 + kv) + 5 * j + c) * 2048 + (r2 & 1) * 1024), lane * 16, (kv ? L_V + c * VCH : L_K + c * KCH) + r2 * 1024);
      }
    }
    vm_wait<0>();
    asm volatile("" : "+v"(qf[0]), "+v"(qf[1]));
    bar();
    STAMP(4);
    if (g == 2) { qf[0][2] = 0; qf[0][3] = 0; }
    if (g == 0) { qf[1][0] = 0; qf[1][1] = 0; }
    if (g == 3) { qf[0] = u32x4{0x3f80u, 0, 0, 0}; qf[1] = qf[0]; }
    u32x2 dmask;
    dmask[0] = (4 * g <= r16 ? 0xffffu : 0u) | (4 * g + 1 <= r16 ? 0xffff0000u : 0u);
    dmask[1] = (4 * g + 2 <= r16 ? 0xffffu : 0u) | (4 * g + 3 <= r16 ? 0xffff0000u : 0u);
    u32x2 o0p[2], o1p[2];
    const char* wob = wo + slice * 160;
#pragma unroll
    for (int h = 0; h < 2; ++h) {
      const u32 adK = L_K + (g == 3 ? 5 : 2 * h + g) * KCH + (16 * w + r16) * 16;
      const u32 vrow = 16 * w + 4 * g + q4;
      const u32 adV0 = L_V + (3 * h + (pq >> 1)) * VCH + vrow * 16 + (pq & 1) * 8;
      const u32 adV1 = pq == 0 ? L_V + 2 * VCH + vrow * 16 + 8 * h : L_K + 5 * KCH + (vrow ^ 8) * 16 + 8;
      const int blk = 4 + w + 8 * h;
      f32x4 s[17];
#pragma unroll
      for (int i = 0; i < 17; ++i) s[i] = mfma(lds128(adK + 256 * i), qf[h], f32x4{0, 0, 0, 0});
      dma(wob + blk * 32 * 1280, voffW[0], L_WOB + blk * 5120);
#pragma unroll
      for (int r = 0; r < 4; ++r) s[16][r] = (4 * g + r <= r16) ? s[16][r] : s[0][r];
      float mx = __builtin_fmaxf(s[16][0], s[16][1]);
      mx = __builtin_fmaxf(__builtin_fmaxf(mx, s[16][2]), s[16][3]);
#pragma unroll
      for (int i = 1; i < 16; ++i) {
        mx = __builtin_fmaxf(__builtin_fmaxf(mx, s[i][0]), s[i][1]);
        mx = __builtin_fmaxf(__builtin_fmaxf(mx, s[i][2]), s[i][3]);
      }
      mx = gmax(mx);
      dma(wob + blk * 32 * 1280, voffW[1], L_WOB + blk * 5120 + 1024);
      u32x2 pk[17];
#pragma unroll
      for (int i = 1; i < 17; ++i) {
        const float e0 = __builtin_amdgcn_exp2f(s[i][0] - mx), e1 = __builtin_amdgcn_exp2f(s[i][1] - mx);
        const float e2 = __builtin_amdgcn_exp2f(s[i][2] - mx), e3 = __builtin_amdgcn_exp2f(s[i][3] - mx);
        pk[i] = u32x2{pack2(e0, e1), pack2(e2, e3)};
        if (i == 5) dma(wob + blk * 32 * 1280, voffW[2], L_WOB + blk * 5120 + 2048);
        if (i == 11) dma(wob + blk * 32 * 1280, voffW[3], L_WOB + blk * 5120 + 3072);
      }
      pk[0] = pk[16] & ~dmask; pk[16] = pk[16] & dmask;
      dma(wob + blk * 32 * 1280, voffW[4], L_WOB + blk * 5120 + 4096);
      f32x4 o0 = mfma16(ldstr(adV0), pk[0], f32x4{0, 0, 0, 0});
      f32x4 o1 = mfma16(ldstr(adV1), pk[0], f32x4{0, 0, 0, 0});
#pragma unroll
      for (int u = 0; u < 8; ++u) {
        const int ta = 2 * u + 1, tb = 2 * u + 2;
        const u32x4 pf = u32x4{pk[ta][0], pk[ta][1], pk[tb][0], pk[tb][1]};
        o0 = mfma(ldstr2(adV0 + 256 * ta, adV0 + 256 * tb), pf, o0);
        o1 = mfma(ldstr2(adV1 + 256 * ta, adV1 + 256 * tb), pf, o1);
      }
      auto ls = __builtin_amdgcn_permlane16_swap(__float_as_uint(o1[0]), __float_as_uint(o1[0]), false, false);
      const float inv = __builtin_amdgcn_rcpf(__uint_as_float(ls[1]));
      o0p[h] = u32x2{pack2(o0[0] * inv, o0[1] * inv), pack2(o0[2] * inv, o0[3] * inv)};
      o1p[h] = u32x2{pack2(o1[0] * inv, o1[1] * inv), pack2(o1[2] * inv, o1[3] * inv)};
    }
    u32x2 xr[5];
#pragma unroll
    for (int t = 0; t < 5; ++t) xr[t] = *(GAS const u32x2*)(x + (size_t)(128 * p + 64 * half + 16 * (w & 3) + r16) * 1280 + slice * 160 + t * 32 + g * 8);
    const u32x4 o = quad2chunk(o0p[0], o0p[1]);
    const u32 ob = WS_O + (p * 80 + 5 * j) * 2048 + (16 * w + r16) * 16;
    st128_sc1(rs, ob + (3 * (g & 1) + (g >> 1)) * 2048, o);
    if (g == 0) st128_sc1(rs, ob + 2 * 2048, u32x4{o1p[0][0], o1p[0][1], o1p[1][0], o1p[1][1]});
    STAMP(5);
    vm_wait<0>();
#pragma unroll
    for (int t = 0; t < 5; ++t) {
      const u32 a = xr[t][0], b = xr[t][1];
      hacc[t] = w < 4 ? f32x4{bflo(a), bfhi(a), bflo(b), bfhi(b)} : f32x4{0, 0, 0, 0};
      asm volatile("" : "+v"(hacc[t]));
    }
  }
  bar();
  STAMP(6);

  u32 voffG[3];
#pragma unroll
  for (int i = 0; i < 3; ++i) {
    const int s = 64 * i + lane, rw = s / 12, cp = s - rw * 12;
    int c = cp - (w & 1) - 2 * ((rw >> 3) & 1); c = c < 0 ? c + 12 : c;
    voffG[i] = (16 * (w >> 1) + rw) * 3072 + c * 16;
  }
  const char* gsrc = ((w & 1) ? wu : wg) + j * 192;
  const int rotg = (slice + (slice >> 2) + ((5 * (rr >> 3)) >> 1)) % 10;
  auto stageGB = [&](int c) {
    int kc = c + rotg; kc = kc >= 10 ? kc - 10 : kc;
    const char* b = gsrc + (size_t)kc * 64 * 3072;
    const u32 dst = L_GB + (c & 3) * GB_SLOT + (w & 1) * 12288 + (w >> 1) * 3072;
    dma(b, voffG[0], dst); dma(b, voffG[1], dst + 1024); dma(b, voffG[2], dst + 2048);
  };
  if (w == 7) {
    if (lane == 0) __hip_atomic_store((GAS u64*)(ws + flag_off(1, p, j)), tag, RLX_AGENT);
    poll_flags(ws, flag_off(1, p, r16), tag);
  } else {
    if (w < 4) {
#pragma unroll
      for (int i = 0; i < 5; ++i) dma(wo + slice * 160 + w * 32 * 1280, voffW[i], L_WOB + w * 5120 + i * 1024);
    }
    stageGB(0); stageGB(1);
    vm_wait<6>();
  }
  bar();
  STAMP(7);

  const int wm4 = w & 3, wk = w >> 2;
  const u32 adW_lo = (8 * g + q4 + 4 * (g & 1)) * 160 + pq * 8, adW_hi = (8 * g + q4 + 4 * ((g & 1) ^ 1)) * 160 + pq * 8;
  const u32 voffR = g * 2048 + (64 * half + 16 * wm4 + r16) * 16;
  u32x2 hp[5];
  {
    if (w == 7) { stageGB(0); stageGB(1); }
    u32x4 a[10];
    const char* osrc = ws + WS_O + (p * 80 + 40 * wk) * 2048;
#pragma unroll
    for (int s = 0; s < 10; ++s) gld_sc1(a[s], osrc + s * 8192, voffR);
    const u32 bb = L_WOB + wk * 10 * 5120;
#pragma unroll
    for (int s = 0; s < 10; ++s) {
      u32x4 b[5];
#pragma unroll
      for (int t = 0; t < 5; ++t) b[t] = ldstr2(bb + adW_lo + s * 5120 + t * 32, bb + adW_hi + s * 5120 + t * 32);
      switch (s) {
        case 0: vm_wait_use<9>(a[0]); break; case 1: vm_wait_use<8>(a[1]); break; case 2: vm_wait_use<7>(a[2]); break;
        case 3: vm_wait_use<6>(a[3]); break; case 4: vm_wait_use<5>(a[4]); break; case 5: vm_wait_use<4>(a[5]); break;
        case 6: vm_wait_use<3>(a[6]); break; case 7: vm_wait_use<2>(a[7]); break; case 8: vm_wait_use<1>(a[8]); break;
        default: vm_wait_use<0>(a[9]); break;
      }
#pragma unroll
      for (int t = 0; t < 5; ++t) hacc[t] = mfma(b[t], a[s], hacc[t]);
    }
    STAMP(8);
    bar();
    if (wk == 1) {
#pragma unroll
      for (int t = 0; t < 5; ++t) *(LAS f32x4*)(smem + L_RED + (wm4 * 5 + t) * 1024 + lane * 16) = hacc[t];
    }
    bar();
    if (wk == 0) {
      float s1 = 0.f, s2 = 0.f;
#pragma unroll
      for (int t = 0; t < 5; ++t) {
        const f32x4 v = hacc[t] + *(LAS const f32x4*)(smem + L_RED + (wm4 * 5 + t) * 1024 + lane * 16);
        const u32 lo = pack2(v[0], v[1]), hi = pack2(v[2], v[3]);
        hp[t] = u32x2{lo, hi};
        s1 = dot2(hi, ONES2, dot2(lo, ONES2, s1)); s2 = dot2(hi, hi, dot2(lo, lo, s2));
      }
      const u32 hb = WS_H + (p * 80 + 10 * slice + 2 * (g & 1) + (g >> 1)) * 2048 + (64 * half + 16 * wm4 + r16) * 16;
      st128_sc1(rs, hb, quad2chunk(hp[0], hp[1]));
      st128_sc1(rs, hb + 4 * 2048, quad2chunk(hp[2], hp[3]));
      const u32x4 last = quad2chunk(hp[4], hp[4]);
      if ((g & 1) == 0) st128_sc1(rs, hb + 8 * 2048, last);
      s1 = gsum(s1); s2 = gsum(s2);
      if (g == 0) st64_sc1(rs, WS_ST + (p * 8 + slice) * 1024 + (64 * half + 16 * wm4 + r16) * 8, u32x2{__float_as_uint(s1), __float_as_uint(s2)});
      vm_wait<0>();
    } else {
#pragma unroll
      for (int t = 0; t < 5; ++t) hp[t] = u32x2{0, 0};
      if (w == 7 && bx == 0) {
#pragma unroll
        for (int i = 0; i < 4; ++i) poll_flags(ws, WS_FLAG + (64 * i + lane) * 256, tag);
        if (lane == 0) __hip_atomic_store((GAS u64*)(ws + WS_EPOCH), tag, RLX_AGENT);
      }
    }
  }
  bar();
  STAMP(9);
  if (w == 7) {
    if (lane == 0) __hip_atomic_store((GAS u64*)(ws + flag_off(2, p, j)), tag, RLX_AGENT);
    poll_flags(ws, flag_off(2, p, r16), tag);
  } else {
    stageGB(2); stageGB(3);
  }
  bar();
  STAMP(10);

  {
    const int wm = w >> 2, wn = w & 3;
    if (w == 7) { stageGB(2); stageGB(3); }
    const char* hsrc = ws + WS_H + (p * 80 + w) * 2048;
    auto stageGA = [&](int c) {
      int kc = c + rotg; kc = kc >= 10 ? kc - 10 : kc;
      const u32 dst = L_GA + (c % 3) * GA_SLOT + w * 2048;
      dma_sc1(hsrc + kc * 16384, lane * 16, dst); dma_sc1(hsrc + kc * 16384 + 1024, lane * 16, dst + 1024);
    };
    dma_sc1(ws + WS_ST + (p * 8 + w) * 1024, lane * 16, L_ST + w * 1024);
    stageGA(0); stageGA(1);
    u32 adA[2], adB[2][3];
#pragma unroll
    for (int s = 0; s < 2; ++s) {
      const int ks = s ^ wm;
      adA[s] = L_GA + (4 * ks + g) * 2048 + (64 * wm + r16) * 16;
#pragma unroll
      for (int t = 0; t < 3; ++t) {
        const int mat = t == 0 ? 0 : (t == 1 ? 1 : (pq >> 1)), c = t < 2 ? 3 * wn + (pq >> 1) : 3 * wn + 2;
        int cp = c + mat + 2 * (g & 1); cp = cp >= 12 ? cp - 12 : cp;
        adB[s][t] = L_GB + mat * 12288 + (32 * ks + 8 * g + q4) * 192 + cp * 16 + (pq & 1) * 8;
      }
    }
    f32x4 acc[4][3];
#pragma unroll
    for (int m = 0; m < 4; ++m)
#pragma unroll
      for (int t = 0; t < 3; ++t) acc[m][t] = f32x4{0, 0, 0, 0};
    float csum[3] = {0.f, 0.f, 0.f};
#pragma unroll
    for (int c = 0; c < 10; ++c) {
      if (c == 0) vm_wait<2>(); else if (c == 1) vm_wait<2>(); else if (c == 2) vm_wait<5>(); else if (c <= 7) vm_wait<8>(); else if (c == 8) vm_wait<5>(); else vm_wait<0>();
      bar();
      if (c + 2 < 10) stageGA(c + 2);
      if (c >= 1 && c + 3 < 10) stageGB(c + 3);
      if (c == 1) {
        const f32x2 u0 = *(LAS const f32x2*)(smem + L_ST + (2 * g) * 1024 + (16 * w + r16) * 8), u1 = *(LAS const f32x2*)(smem + L_ST + (2 * g + 1) * 1024 + (16 * w + r16) * 8);
        const float s1 = gsum(u0[0] + u1[0]), s2 = gsum(u0[1] + u1[1]);
        const float mean = s1 * (1.f / 640.f), var = __builtin_fmaxf(s2 * (1.f / 640.f) - mean * mean, 0.f);
        if (g == 0) *(LAS f32x2*)(smem + L_MISC + (16 * w + r16) * 8) = f32x2{mean, __builtin_amdgcn_rsqf(var + LN_EPS)};
      }
      const u32 sa = (c % 3) * GA_SLOT, sb = (c & 3) * GB_SLOT;
#pragma unroll
      for (int s = 0; s < 2; ++s) {
        u32x4 a[4], b[3];
#pragma unroll
        for (int t = 0; t < 3; ++t) b[t] = ldstr2(sb + adB[s][t], sb + adB[s][t] + 768);
#pragma unroll
        for (int m = 0; m < 4; ++m) a[m] = lds128(sa + adA[s] + m * 256);
#pragma unroll
        for (int m = 0; m < 4; ++m)
#pragma unroll
          for (int t = 0; t < 3; ++t) acc[m][t] = mfma(b[t], a[m], acc[m][t]);
        if (s == 0) {
#pragma unroll
          for (int t = 0; t < 3; ++t) csum[t] = sum8(b[t], csum[t]);
        }
      }
    }
    STAMP(11);
#pragma unroll
    for (int t = 0; t < 3; ++t) {
      const float v = gsum(csum[t]);
      if (g == 0) *(LAS float*)(smem + L_ST + (wm * 192 + wn * 48 + t * 16 + r16) * 4) = v;
    }
    bar();
    f32x4 cs[3];
#pragma unroll
    for (int t = 0; t < 3; ++t) cs[t] = *(LAS const f32x4*)(smem + L_ST + (wn * 48 + t * 16 + 4 * g) * 4) + *(LAS const f32x4*)(smem + L_ST + (192 + wn * 48 + t * 16 + 4 * g) * 4);
    u32x2 p01[4]; f32x4 t2[4];
#pragma unroll
    for (int m = 0; m < 4; ++m) {
      const f32x2 st = *(LAS const f32x2*)(smem + L_MISC + (64 * wm + 16 * m + r16) * 8);
      const f32x4 gv = (acc[m][0] - st[0] * cs[0]) * st[1], uv = (acc[m][1] - st[0] * cs[1]) * st[1];
      const f32x4 v = gv * uv;
      p01[m] = u32x2{pack2(v[0], v[1]), pack2(v[2], v[3])};
      t2[m] = (acc[m][2] - st[0] * cs[2]) * st[1];
    }
    u32x2 p2[2];
#pragma unroll
    for (int mm = 0; mm < 2; ++mm) {
      f32x4 v;
#pragma unroll
      for (int r = 0; r < 4; ++r) {
        auto sw = __builtin_amdgcn_permlane32_swap(__float_as_uint(t2[2 * mm][r]), __float_as_uint(t2[2 * mm + 1][r]), false, false);
        v[r] = __uint_as_float(sw[0]) * __uint_as_float(sw[1]);
      }
      p2[mm] = u32x2{pack2(v[0], v[1]), pack2(v[2], v[3])};
    }
    const u32 hb = WS_HH + (p * 192 + 12 * j + 3 * wn) * 2048 + (64 * wm + r16) * 16;
    st128_sc1(rs, hb + (g >> 1) * 2048 + (g & 1) * 256, quad2chunk(p01[0], p01[1]));
    st128_sc1(rs, hb + (g >> 1) * 2048 + (2 + (g & 1)) * 256, quad2chunk(p01[2], p01[3]));
    st128_sc1(rs, hb + 2 * 2048 + (2 * (g & 1) + (g >> 1)) * 256, quad2chunk(p2[0], p2[1]));
    vm_wait<0>();
  }
  bar();
  STAMP(12);

  {
    const int rotd = (slice * 6) >> 3;
    auto stageD = [&](int c) {
      int kc = c + rotd; kc = kc >= 6 ? kc - 6 : kc;
      const char* b = wd + slice * 160 + (size_t)(kc * 8 + w) * 32 * 1280;
      const u32 dst = (c & 3) * 40960 + w * 5120;
#pragma unroll
      for (int i = 0; i < 5; ++i) dma(b, voffW[i], dst + i * 1024);
    };
    if (w == 7) {
      if (lane == 0) __hip_atomic_store((GAS u64*)(ws + flag_off(3, p, j)), tag, RLX_AGENT);
      poll_flags(ws, flag_off(3, p, r16), tag);
    } else {
      stageD(0); stageD(1);
    }
    bar();
    STAMP(13);
    if (w == 7) { stageD(0); stageD(1); }
    f32x4 acc[5];
#pragma unroll
    for (int t = 0; t < 5; ++t) { const u32 a = hp[t][0], b = hp[t][1]; acc[t] = f32x4{bflo(a), bfhi(a), bflo(b), bfhi(b)}; }
    u32x4 a[6][4];
    const char* asrc = ws + WS_HH + (p * 192 + 16 * wk) * 2048;
    auto loadA = [&](int c) {
      int kc = c + rotd; kc = kc >= 6 ? kc - 6 : kc;
#pragma unroll
      for (int s = 0; s < 4; ++s) gld_sc1(a[c][s], asrc + kc * 65536 + s * 8192, voffR);
    };
    loadA(0); loadA(1);
#pragma unroll
    for (int c = 0; c < 6; ++c) {
      if (c == 0) vm_wait<4>(); else if (c <= 4) vm_wait<9>(); else vm_wait<0>();
      asm volatile("" : "+v"(a[c][0]), "+v"(a[c][1]), "+v"(a[c][2]), "+v"(a[c][3]));
      bar();
      if (c + 2 < 6) { stageD(c + 2); loadA(c + 2); }
      const u32 bb = (c & 3) * 40960 + wk * 4 * 5120;
#pragma unroll
      for (int s = 0; s < 4; ++s) {
        u32x4 b[5];
#pragma unroll
        for (int t = 0; t < 5; ++t) b[t] = ldstr2(bb + adW_lo + s * 5120 + t * 32, bb + adW_hi + s * 5120 + t * 32);
#pragma unroll
        for (int t = 0; t < 5; ++t) acc[t] = mfma(b[t], a[c][s], acc[t]);
      }
    }
    STAMP(14);
    constexpr u32 L_RED2 = 2 * 40960;
    if (wk == 1) {
#pragma unroll
      for (int t = 0; t < 5; ++t) *(LAS f32x4*)(smem + L_RED2 + (wm4 * 5 + t) * 1024 + lane * 16) = acc[t];
    }
    bar();
    if (wk == 0) {
      u32x2 op[5];
#pragma unroll
      for (int t = 0; t < 5; ++t) {
        const f32x4 v = acc[t] + *(LAS const f32x4*)(smem + L_RED2 + (wm4 * 5 + t) * 1024 + lane * 16);
        op[t] = u32x2{pack2(v[0], v[1]), pack2(v[2], v[3])};
      }
      char* orow = out + (size_t)(128 * p + 64 * half + 16 * wm4 + r16) * 1280 + slice * 160 + (2 * (g & 1) + (g >> 1)) * 16;
      *(GAS u32x4*)(orow) = quad2chunk(op[0], op[1]);
      *(GAS u32x4*)(orow + 64) = quad2chunk(op[2], op[3]);
      const u32x4 last = quad2chunk(op[4], op[4]);
      if ((g & 1) == 0) *(GAS u32x4*)(orow + 128) = last;
    }
    STAMP(15);
  }
}

extern "C" void kernel_launch(void* const* d_in, const int* in_sizes, int n_in, void* d_out, int out_size, void* d_ws, size_t ws_size, hipStream_t stream) {
  (void)in_sizes; (void)n_in; (void)out_size; (void)ws_size;
  (void)hipFuncSetAttribute((const void*)fused_block, hipFuncAttributeMaxDynamicSharedMemorySize, LDS_BYTES);
  fused_block<<<256, 512, LDS_BYTES, stream>>>((const char*)d_in[0], (const char*)d_in[1], (const char*)d_in[2], (const char*)d_in[4], (const char*)d_in[5],
                                               (const char*)d_in[6], (char*)d_out, (char*)d_ws);
}
```

```cpp
#include <hip/hip_runtime.h>
#include <stdint.h>

typedef unsigned short u16;
typedef unsigned int u32;
typedef unsigned long long u64;
typedef __bf16 bf16x8 __attribute__((ext_vector_type(8)));
typedef __bf16 bf16x2 __attribute__((ext_vector_type(2)));
typedef short s16x4 __attribute__((ext_vector_type(4)));
typedef float f32x4 __attribute__((ext_vector_type(4)));
typedef float f32x2 __attribute__((ext_vector_type(2)));
typedef u32 u32x4 __attribute__((ext_vector_type(4)));
typedef u32 u32x2 __attribute__((ext_vector_type(2)));
#define LAS __attribute__((address_space(3)))
#define GAS __attribute__((address_space(1)))
#define DI __device__ __forceinline__
#define RLX_AGENT __ATOMIC_RELAXED, __HIP_MEMORY_SCOPE_AGENT

#define STAMPS 0

constexpr u32 WS_EPOCH = 0;
constexpr u32 WS_FLAG = 4096;
constexpr u32 WS_QKV = WS_FLAG + 4 * 256 * 256;
constexpr u32 WS_O = WS_QKV + 16 * 240 * 2048;
constexpr u32 WS_H = WS_O + 16 * 80 * 2048;
constexpr u32 WS_HH = WS_H + 16 * 80 * 2048;
constexpr u32 WS_ST = WS_HH + 16 * 192 * 2048;
constexpr u32 WS_END = WS_ST + 16 * 8 * 1024;
constexpr u32 WS_STAMP = WS_END;
constexpr u64 TAG_STEP = 0x9E3779B97F4A7C15ull;

constexpr u32 LDS_BYTES = 163840;
constexpr u32 L_MISC = 161792;
constexpr u32 L_K = 0, KCH = 6144;
constexpr u32 L_V = 6 * KCH, VCH = 6272;
constexpr u32 L_WOB = L_MISC - 102400;
constexpr u32 L_GB = 0, GB_SLOT = 24576;
constexpr u32 L_GA = 4 * GB_SLOT, GA_SLOT = 16384;
constexpr u32 L_ST = L_GA + 3 * GA_SLOT;
constexpr u32 L_RED = L_GA;
constexpr u32 L_WARM = L_MISC + 1792;
constexpr float LN_EPS = 1e-5f;

extern __shared__ __attribute__((aligned(16))) char smem[];

DI u32x4 lds128(u32 off) { return *(LAS const u32x4*)(smem + off); }
DI u32x2 ldstr(u32 off) { return __builtin_bit_cast(u32x2, __builtin_amdgcn_ds_read_tr16_b64_v4i16((LAS s16x4*)(smem + off))); }
DI u32x4 ldstr2(u32 lo, u32 hi) { const u32x2 a = ldstr(lo), b = ldstr(hi); return u32x4{a[0], a[1], b[0], b[1]}; }
DI f32x4 mfma(u32x4 a, u32x4 b, f32x4 c) { return __builtin_amdgcn_mfma_f32_16x16x32_bf16(__builtin_bit_cast(bf16x8, a), __builtin_bit_cast(bf16x8, b), c, 0, 0, 0); }
DI f32x4 mfma16(u32x2 a, u32x2 b, f32x4 c) { return __builtin_amdgcn_mfma_f32_16x16x16bf16_1k(__builtin_bit_cast(s16x4, a), __builtin_bit_cast(s16x4, b), c, 0, 0, 0); }
DI float dot2(u32 a, u32 b, float c) { return __builtin_amdgcn_fdot2_f32_bf16(__builtin_bit_cast(bf16x2, a), __builtin_bit_cast(bf16x2, b), c, false); }
DI u32 pack2(float lo, float hi) { const f32x2 v = {lo, hi}; return __builtin_bit_cast(u32, __builtin_convertvector(v, bf16x2)); }
DI float bflo(u32 v) { return __uint_as_float(v << 16); }
DI float bfhi(u32 v) { return __uint_as_float(v & 0xffff0000u); }
constexpr u32 ONES2 = 0x3f803f80u;
DI float sum8(u32x4 a, float s) { const u32 a0 = a[0], a1 = a[1], a2 = a[2], a3 = a[3]; return dot2(a3, ONES2, dot2(a2, ONES2, dot2(a1, ONES2, dot2(a0, ONES2, s)))); }
DI float sq8(u32x4 a, float s) { const u32 a0 = a[0], a1 = a[1], a2 = a[2], a3 = a[3]; return dot2(a3, a3, dot2(a2, a2, dot2(a1, a1, dot2(a0, a0, s)))); }
DI float gsum(float x) {
  auto a = __builtin_amdgcn_permlane16_swap(__float_as_uint(x), __float_as_uint(x), false, false);
  x = __uint_as_float(a[0]) + __uint_as_float(a[1]);
  auto b = __builtin_amdgcn_permlane32_swap(__float_as_uint(x), __float_as_uint(x), false, false);
  return __uint_as_float(b[0]) + __uint_as_float(b[1]);
}
DI float gmax(float x) {
  auto a = __builtin_amdgcn_permlane16_swap(__float_as_uint(x), __float_as_uint(x), false, false);
  x = __builtin_fmaxf(__uint_as_float(a[0]), __uint_as_float(a[1]));
  auto b = __builtin_amdgcn_permlane32_swap(__float_as_uint(x), __float_as_uint(x), false, false);
  return __builtin_fmaxf(__uint_as_float(b[0]), __uint_as_float(b[1]));
}
DI u32x4 quad2chunk(u32x2 A, u32x2 B) {
  const u32 a0 = A[0], a1 = A[1], b0 = B[0], b1 = B[1];
  auto s0 = __builtin_amdgcn_permlane16_swap(a0, b0, false, false);
  auto s1 = __builtin_amdgcn_permlane16_swap(a1, b1, false, false);
  return u32x4{s0[0], s1[0], s0[1], s1[1]};
}
DI void dma(const void* sbase, u32 voff, u32 lds) {
  asm volatile("s_mov_b32 m0, %2\n\ts_nop 0\n\tglobal_load_lds_dwordx4 %0, %1" ::"v"(voff), "s"(sbase), "s"(lds));
}
DI void dma_sc1(const void* sbase, u32 voff, u32 lds) {
  asm volatile("s_mov_b32 m0, %2\n\ts_nop 0\n\tglobal_load_lds_dwordx4 %0, %1 sc1" ::"v"(voff), "s"(sbase), "s"(lds));
}
DI void gld_sc1(u32x4& dst, const void* sbase, u32 voff) {
  asm volatile("global_load_dwordx4 %0, %1, %2 sc1" : "=v"(dst) : "v"(voff), "s"(sbase));
}
template <int N> DI void vm_wait() { asm volatile("s_waitcnt vmcnt(%0)" ::"n"(N) : "memory"); }
template <int N> DI void vm_wait_use(u32x4& r) { asm volatile("s_waitcnt vmcnt(%1)" : "+v"(r) : "n"(N)); }
#define SB() __builtin_amdgcn_sched_barrier(0)
DI void warm(const void* sbase, u32 voff, u32 lds) {
  asm volatile("s_mov_b32 m0, %2\n\ts_nop 0\n\tglobal_load_lds_dword %0, %1" ::"v"(voff), "s"(sbase), "s"(lds));
}
DI void bar0() { asm volatile("s_barrier" ::: "memory"); }
DI u32x4 cat(u32x2 a, u32x2 b) { return u32x4{a[0], a[1], b[0], b[1]}; }
DI void bar() { asm volatile("s_waitcnt lgkmcnt(0)\n\ts_barrier" ::: "memory"); }
DI void st128_sc1(__amdgpu_buffer_rsrc_t rs, u32 off, u32x4 v) { __builtin_amdgcn_raw_buffer_store_b128(v, rs, (int)off, 0, 16); }
DI void st64_sc1(__amdgpu_buffer_rsrc_t rs, u32 off, u32x2 v) { __builtin_amdgcn_raw_buffer_store_b64(v, rs, (int)off, 0, 16); }

DI void poll_flags(const char* ws, u32 off, u64 tag) {
  GAS u64* a = (GAS u64*)(ws + off);
  for (u32 spins = 0; spins < (1u << 20); ++spins) {
    const u64 v = __hip_atomic_load(a, RLX_AGENT);
    if (__all(v == tag)) break;
  }
  __builtin_amdgcn_fence(__ATOMIC_ACQUIRE, "wavefront");
}
DI u32 flag_off(int phase, int p, int j) { return WS_FLAG + (u32)((phase * 256 + p * 16 + j) * 256); }

#define STAMP(i) do {} while (0)

__global__ __launch_bounds__(512, 2) void fused_block(const char* __restrict__ x, const char* __restrict__ wqkv, const char* __restrict__ wo,
                                                      const char* __restrict__ wg, const char* __restrict__ wu, const char* __restrict__ wd,
                                                      char* __restrict__ out, char* ws) {
  const int tid = threadIdx.x, lane = tid & 63, w = __builtin_amdgcn_readfirstlane(tid >> 6);
  const int r16 = lane & 15, g = lane >> 4, q4 = r16 >> 2, pq = r16 & 3;
  const int bx = blockIdx.x, xcd = bx & 7, rr = bx >> 3;
  const int p = (xcd >> 1) * 4 + (rr >> 3), j = (xcd & 1) * 8 + (rr & 7);
  const __amdgpu_buffer_rsrc_t rs = __builtin_amdgcn_make_buffer_rsrc(ws, 0, WS_END, 0x00020000);
  STAMP(0);
  const u64 epoch_v = __hip_atomic_load((GAS u64*)(ws + WS_EPOCH), RLX_AGENT);

  {
    u64 pc; asm volatile("s_getpc_b64 %0" : "=s"(pc));
    if (w == 0) warm((const char*)(pc & ~127ull) + rr * 1152, (lane < 9 ? lane : 8) * 128, L_WARM);
  }
  const u32x4 ones8 = {ONES2, ONES2, ONES2, ONES2};
  if (j < 15) {
    const int wm = w >> 2, wn = w & 3;
    u32 voffA[2], voffB[2];
#pragma unroll
    for (int i = 0; i < 2; ++i) {
      const int ra = 16 * w + 8 * i + (lane >> 3);
      voffA[i] = ra * 1280 + (((lane & 7) ^ ((ra >> 1) & 7)) << 4);
      const int rb = 8 * w + 4 * i + g, f = (rb & 3) | (((rb >> 3) & 1) << 2);
      voffB[i] = rb * 3840 + (((((r16 >> 1) ^ f) << 1) | (r16 & 1)) << 4);
    }
    const char* xa = x + (size_t)p * 128 * 1280;
    const char* wb = wqkv + j * 256;
    const int rot = ((rr & 7) + ((rr & 7) >> 2) + ((5 * (rr >> 3)) >> 1)) % 10;
    auto stage = [&](int c, int piece) {
      int kc = c + rot; kc = kc >= 10 ? kc - 10 : kc;
      const u32 slot = (c & 3) * 32768;
      const char* a = xa + kc * 128; const char* b = wb + (size_t)kc * 64 * 3840;
      if (piece < 0 || piece == 0) dma(a, voffA[0], slot + w * 2048);
      if (piece < 0 || piece == 1) dma(a, voffA[1], slot + w * 2048 + 1024);
      if (piece < 0 || piece == 2) dma(b, voffB[0], slot + 16384 + w * 2048);
      if (piece < 0 || piece == 3) dma(b, voffB[1], slot + 16384 + w * 2048 + 1024);
    };
    stage(0, -1); stage(1, -1); stage(2, -1);
    u32 adA[4][2], adB[2];
#pragma unroll
    for (int m = 0; m < 4; ++m)
#pragma unroll
      for (int ks = 0; ks < 2; ++ks) adA[m][ks] = (64 * wm + 16 * ((m + wn) & 3) + r16) * 128 + (((4 * ks + g) ^ ((r16 >> 1) & 7)) << 4);
#pragma unroll
    for (int n = 0; n < 2; ++n) adB[n] = 16384 + (8 * g + q4) * 256 + (((2 * wn + (n ^ wm)) ^ (q4 | ((g & 1) << 2))) << 5) + pq * 8;
    f32x4 acc[4][2], rsum = {0, 0, 0, 0}, rsq = {0, 0, 0, 0}, csum = {0, 0, 0, 0};
#pragma unroll
    for (int m = 0; m < 4; ++m) { acc[m][0] = f32x4{0, 0, 0, 0}; acc[m][1] = f32x4{0, 0, 0, 0}; }
    u32x4 fa[2][4]; u32x2 fb[2][2][2];
    auto rdp = [&](int c, int ks, int i) {
      const u32 slot = (c & 3) * 32768;
      if (i == 0 || i == 1) fb[ks][0][i] = ldstr(slot + adB[0] + ks * 8192 + i * 1024);
      else if (i == 2) fa[ks][0] = lds128(slot + adA[0][ks]);
      else if (i == 3 || i == 4) fb[ks][1][i - 3] = ldstr(slot + adB[1] + ks * 8192 + (i - 3) * 1024);
      else if (i < 8) fa[ks][i - 4] = lds128(slot + adA[i - 4][ks]);
    };
    auto mm = [&](int ks, int cr, int cn) {
      const u32x4 b0 = cat(fb[ks][0][0], fb[ks][0][1]), b1 = cat(fb[ks][1][0], fb[ks][1][1]);
#pragma unroll
      for (int i = 0; i < 11; ++i) {
        const int m = i / 3, e = i % 3;
        if (e == 0) acc[m][0] = mfma(b0, fa[ks][m], acc[m][0]);
        else if (e == 1) acc[m][1] = mfma(b1, fa[ks][m], acc[m][1]);
        else if (m == 0) rsum = mfma(ones8, fa[ks][0], rsum);
        else if (m == 1) rsq = mfma(fa[ks][0], fa[ks][0], rsq);
        else csum = mfma(b0, ones8, csum);
        if (cr < 10) rdp(cr, ks ^ 1, i);
        if (cn < 10 && i == 8) stage(cn, 2 * (ks ^ 1));
        if (cn < 10 && i == 10) stage(cn, 2 * (ks ^ 1) + 1);
        SB();
      }
    };
    vm_wait<8>();
    bar();
    STAMP(16);
#pragma unroll
    for (int i = 0; i < 8; ++i) rdp(0, 0, i);
    SB();
#pragma unroll
    for (int c = 0; c < 10; ++c) {
      mm(0, c, c >= 1 ? c + 2 : 10);
      if (c < 9) {
        if (c <= 7) vm_wait<4>(); else vm_wait<0>();
        bar0();
      }
      mm(1, c + 1, c + 3);
    }
    STAMP(1);
    {
      const float dq = pq == 0 ? rsq[0] : (pq == 1 ? rsq[1] : (pq == 2 ? rsq[2] : rsq[3]));
      const float mean = rsum[0] * (1.f / 640.f), var = __builtin_fmaxf(dq * (1.f / 640.f) - mean * mean, 0.f);
      if (g == q4) *(LAS f32x2*)(smem + L_MISC + (64 * wm + 16 * wn + r16) * 8) = f32x2{mean, __builtin_amdgcn_rsqf(var + LN_EPS)};
      if (r16 == 0) *(LAS f32x4*)(smem + L_MISC + 1024 + (32 * wn + 16 * wm + 4 * g) * 4) = csum;
    }
    bar();
    STAMP(17);
    const float qs = j < 5 ? 0.22360679774997896f * 1.4426950408889634f : 1.f;
    f32x4 cs[2];
#pragma unroll
    for (int n = 0; n < 2; ++n) cs[n] = *(LAS const f32x4*)(smem + L_MISC + 1024 + (32 * wn + 16 * (n ^ wm) + 4 * g) * 4);
#pragma unroll
    for (int m = 0; m < 4; ++m) {
      const int row = 64 * wm + 16 * ((m + wn) & 3) + r16;
      const f32x2 st = *(LAS const f32x2*)(smem + L_MISC + row * 8);
      const float sc = st[1] * qs;
      u32x2 pk[2];
#pragma unroll
      for (int n = 0; n < 2; ++n) {
        const f32x4 v = (acc[m][n] - st[0] * cs[n]) * sc;
        pk[n] = u32x2{pack2(v[0], v[1]), pack2(v[2], v[3])};
      }
      const u32x4 o = quad2chunk(pk[0], pk[1]);
      const int chunk = 16 * j + 4 * wn + 2 * ((g & 1) ^ wm) + (g >> 1);
      st128_sc1(rs, WS_QKV + (p * 240 + chunk) * 2048 + row * 16, o);
    }
  }
  const u64 tag = (((u64)(u32)__builtin_amdgcn_readfirstlane((u32)(epoch_v >> 32)) << 32) | (u32)__builtin_amdgcn_readfirstlane((u32)epoch_v)) + TAG_STEP;
  STAMP(18);
  vm_wait<0>();
  bar();
  STAMP(2);
  if (w == 7) {
    if (lane == 0) __hip_atomic_store((GAS u64*)(ws + flag_off(0, p, j)), tag, RLX_AGENT);
    const int e = r16 < 14 ? r16 : 0, sec = e < 2 ? 0 : (e < 8 ? 1 : 2), i = e < 2 ? e : (e - 2) % 6;
    int pp = e < 2 ? p : p - (i >> 1); pp = pp < 0 ? 0 : pp;
    poll_flags(ws, flag_off(0, pp, (80 * sec + 5 * j + 4 * (i & 1)) >> 4), tag);
  } else {
    if (w < 4) warm(wo + rr * 25600 + w * 6400, (lane < 50 ? lane : 49) * 128, L_WARM);
    if (tid < 384) {
      const bool valid = 128 * (p - 2) + tid >= 0;
      *(LAS u32x4*)(smem + L_K + 5 * KCH + tid * 16) = u32x4{valid ? 0u : 0xf149u, 0u, ONES2, ONES2};
    }
  }
  bar();
  STAMP(3);

  u32 voffW[5];
#pragma unroll
  for (int i = 0; i < 5; ++i) {
    const int s = 64 * i + lane, rw = s / 10, c = s - rw * 10;
    voffW[i] = (rw ^ (((rw >> 3) & 1) << 2)) * 1280 + c * 16;
  }
  const int half = j >> 3, slice = j & 7;

  f32x4 hacc[5];
  {
    u32x4 qf[2];
    const char* qkv = ws + WS_QKV;
#pragma unroll
    for (int h = 0; h < 2; ++h) {
      const int ch = g == 3 ? 0 : 2 * h + g;
      gld_sc1(qf[h], qkv, (p * 240 + 5 * j + ch) * 2048 + (16 * w + r16) * 16);
    }
#pragma unroll
    for (int i = 0; i < 8; ++i) {
      const int id = w + 8 * i;
      if (id < 60) {
        const int kv = id >= 30, r = id - 30 * kv, c = r / 6, r2 = r - 6 * c;
        int pp = p - 2 + (r2 >> 1); pp = pp < 0 ? 0 : pp;
        dma_sc1(qkv + (size_t)((pp * 240 + 80 * (1 + kv) + 5 * j + c) * 2048 + (r2 & 1) * 1024), lane * 16, (kv ? L_V + c * VCH : L_K + c * KCH) + r2 * 1024);
      }
    }
    vm_wait<0>();
    asm volatile("" : "+v"(qf[0]), "+v"(qf[1]));
    bar();
    STAMP(4);
    if (g == 2) { qf[0][2] = 0; qf[0][3] = 0; }
    if (g == 0) { qf[1][0] = 0; qf[1][1] = 0; }
    if (g == 3) { qf[0] = u32x4{0x3f80u, 0, 0, 0}; qf[1] = qf[0]; }
    u32x2 dmask;
    dmask[0] = (4 * g <= r16 ? 0xffffu : 0u) | (4 * g + 1 <= r16 ? 0xffff0000u : 0u);
    dmask[1] = (4 * g + 2 <= r16 ? 0xffffu : 0u) | (4 * g + 3 <= r16 ? 0xffff0000u : 0u);
    u32x2 o0p[2], o1p[2];
    const char* wob = wo + slice * 160;
    const u32 vrow = 16 * w + 4 * g + q4;
    u32 adK[2], adV0[2], adV1[2];
#pragma unroll
    for (int h = 0; h < 2; ++h) {
      adK[h] = L_K + (g == 3 ? 5 : 2 * h + g) * KCH + (16 * w + r16) * 16;
      adV0[h] = L_V + (3 * h + (pq >> 1)) * VCH + vrow * 16 + (pq & 1) * 8;
      adV1[h] = pq == 0 ? L_V + 2 * VCH + vrow * 16 + 8 * h : L_K + 5 * KCH + (vrow ^ 8) * 16 + 8;
    }
    const f32x4 zero4 = {0, 0, 0, 0};
    f32x4 s[2][17];
    {
      u32x4 kf[17];
#pragma unroll
      for (int i = 0; i < 17; ++i) kf[i] = lds128(adK[0] + 256 * i);
      SB();
      { const int l12 = lane < 60 ? lane : 59, rw = l12 / 12;
        warm(((w & 1) ? wu : wg) + (size_t)(rr * 20 + (w >> 1) * 5) * 3072 + half * 1536, rw * 3072 + (l12 - rw * 12) * 128, L_WARM); }
      SB();
#pragma unroll
      for (int i = 0; i < 17; ++i) s[0][i] = mfma(kf[i], qf[0], zero4);
      SB();
      STAMP(26);
    }
#pragma unroll
    for (int h = 0; h < 2; ++h) {
      const int blk = 4 + w + 8 * h;
      u32x4 kf[2][2], v0[2], v1[2];
      u32x2 v0f, v1f;
      if (h == 0) { kf[0][0] = lds128(adK[1]); kf[0][1] = lds128(adK[1] + 256); }
      v0[0] = ldstr2(adV0[h] + 256, adV0[h] + 512); v1[0] = ldstr2(adV1[h] + 256, adV1[h] + 512);
      v0f = ldstr(adV0[h]); v1f = ldstr(adV1[h]);
      SB();
#pragma unroll
      for (int r = 0; r < 4; ++r) s[h][16][r] = (4 * g + r <= r16) ? s[h][16][r] : s[h][0][r];
      float m4[4];
#pragma unroll
      for (int c = 0; c < 4; ++c) {
        m4[c] = __builtin_fmaxf(__builtin_fmaxf(s[h][1 + c][0], s[h][1 + c][1]), s[h][1 + c][2]);
        m4[c] = __builtin_fmaxf(__builtin_fmaxf(m4[c], s[h][1 + c][3]), s[h][5 + c][0]);
        m4[c] = __builtin_fmaxf(__builtin_fmaxf(m4[c], s[h][5 + c][1]), s[h][5 + c][2]);
        m4[c] = __builtin_fmaxf(__builtin_fmaxf(m4[c], s[h][5 + c][3]), s[h][9 + c][0]);
        m4[c] = __builtin_fmaxf(__builtin_fmaxf(m4[c], s[h][9 + c][1]), s[h][9 + c][2]);
        m4[c] = __builtin_fmaxf(__builtin_fmaxf(m4[c], s[h][9 + c][3]), s[h][13 + c][0]);
        m4[c] = __builtin_fmaxf(__builtin_fmaxf(m4[c], s[h][13 + c][1]), s[h][13 + c][2]);
      }
      float mx = __builtin_fmaxf(__builtin_fmaxf(m4[0], m4[1]), s[h][13][3]);
      float my = __builtin_fmaxf(__builtin_fmaxf(m4[2], m4[3]), s[h][14][3]);
      mx = __builtin_fmaxf(__builtin_fmaxf(mx, s[h][15][3]), s[h][16][3]);
      mx = gmax(__builtin_fmaxf(mx, my));
      const f32x2 nm = {-mx, -mx};
      f32x4 o0 = zero4, o1 = zero4;
      SB();
#pragma unroll
      for (int u = 0; u < 8; ++u) {
        if (u < 7) { v0[(u + 1) & 1] = ldstr2(adV0[h] + 256 * (2 * u + 3), adV0[h] + 256 * (2 * u + 4)); v1[(u + 1) & 1] = ldstr2(adV1[h] + 256 * (2 * u + 3), adV1[h] + 256 * (2 * u + 4)); }
        if (h == 0) { kf[(u + 1) & 1][0] = lds128(adK[1] + 256 * (2 * u + 2)); if (u < 7) kf[(u + 1) & 1][1] = lds128(adK[1] + 256 * (2 * u + 3)); }
        if (u < 5) dma(wob + blk * 32 * 1280, voffW[u], L_WOB + blk * 5120 + u * 1024);
        SB();
        u32 pw[4];
#pragma unroll
        for (int e = 0; e < 4; ++e) {
          const f32x4 sv = s[h][2 * u + 1 + (e >> 1)];
          const f32x2 d = ((e & 1) ? f32x2{sv[2], sv[3]} : f32x2{sv[0], sv[1]}) + nm;
          pw[e] = pack2(__builtin_amdgcn_exp2f(d[0]), __builtin_amdgcn_exp2f(d[1]));
          if (h == 0 && (e & 1) == 0) s[1][2 * u + (e >> 1)] = mfma(kf[u & 1][e >> 1], qf[1], zero4);
        }
        if (u == 7) {
          const u32x2 pf = u32x2{pw[2], pw[3]} & ~dmask;
          o0 = mfma16(v0f, pf, o0); o1 = mfma16(v1f, pf, o1);
          pw[2] &= dmask[0]; pw[3] &= dmask[1];
          if (h == 0) s[1][16] = mfma(kf[0][0], qf[1], zero4);
        }
        const u32x4 pf = u32x4{pw[0], pw[1], pw[2], pw[3]};
        o0 = mfma(v0[u & 1], pf, o0);
        o1 = mfma(v1[u & 1], pf, o1);
        SB();
      }
      if (h == 0) STAMP(27);
      auto ls = __builtin_amdgcn_permlane16_swap(__float_as_uint(o1[0]), __float_as_uint(o1[0]), false, false);
      const float inv = __builtin_amdgcn_rcpf(__uint_as_float(ls[1]));
      o0p[h] = u32x2{pack2(o0[0] * inv, o0[1] * inv), pack2(o0[2] * inv, o0[3] * inv)};
      o1p[h] = u32x2{pack2(o1[0] * inv, o1[1] * inv), pack2(o1[2] * inv, o1[3] * inv)};
    }
    u32x2 xr[5];
#pragma unroll
    for (int t = 0; t < 5; ++t) xr[t] = *(GAS const u32x2*)(x + (size_t)(128 * p + 64 * half + 16 * (w & 3) + r16) * 1280 + slice * 160 + t * 32 + g * 8);
    const u32x4 o = quad2chunk(o0p[0], o0p[1]);
    const u32 ob = WS_O + (p * 80 + 5 * j) * 2048 + (16 * w + r16) * 16;
    st128_sc1(rs, ob + (3 * (g & 1) + (g >> 1)) * 2048, o);
    if (g == 0) st128_sc1(rs, ob + 2 * 2048, u32x4{o1p[0][0], o1p[0][1], o1p[1][0], o1p[1][1]});
    STAMP(5);
    vm_wait<0>();
#pragma unroll
    for (int t = 0; t < 5; ++t) {
      const u32 a = xr[t][0], b = xr[t][1];
      hacc[t] = w < 4 ? f32x4{bflo(a), bfhi(a), bflo(b), bfhi(b)} : f32x4{0, 0, 0, 0};
      asm volatile("" : "+v"(hacc[t]));
    }
  }
  bar();
  STAMP(6);

  u32 voffG[3];
#pragma unroll
  for (int i = 0; i < 3; ++i) {
    const int s = 64 * i + lane, rw = s / 12, cp = s - rw * 12;
    int c = cp - (w & 1) - 2 * ((rw >> 3) & 1); c = c < 0 ? c + 12 : c;
    voffG[i] = (16 * (w >> 1) + rw) * 3072 + c * 16;
  }
  const char* gsrc = ((w & 1) ? wu : wg) + j * 192;
  const int rotg = (slice + (slice >> 2) + ((5 * (rr >> 3)) >> 1)) % 10;
  auto stageGBp = [&](int c, int i) {
    int kc = c + rotg; kc = kc >= 10 ? kc - 10 : kc;
    dma(gsrc + (size_t)kc * 64 * 3072, voffG[i], L_GB + (c & 3) * GB_SLOT + (w & 1) * 12288 + (w >> 1) * 3072 + i * 1024);
  };
  auto stageGB = [&](int c) { stageGBp(c, 0); stageGBp(c, 1); stageGBp(c, 2); };
  if (w == 7) {
    if (lane == 0) __hip_atomic_store((GAS u64*)(ws + flag_off(1, p, j)), tag, RLX_AGENT);
    poll_flags(ws, flag_off(1, p, r16), tag);
  } else {
    if (w < 4) {
#pragma unroll
      for (int i = 0; i < 5; ++i) dma(wo + slice * 160 + w * 32 * 1280, voffW[i], L_WOB + w * 5120 + i * 1024);
    }
    stageGB(0); stageGB(1);
    vm_wait<6>();
  }
  bar();
  STAMP(7);

  const int wm4 = w & 3, wk = w >> 2;
  const u32 adW_lo = (8 * g + q4 + 4 * (g & 1)) * 160 + pq * 8, adW_hi = (8 * g + q4 + 4 * ((g & 1) ^ 1)) * 160 + pq * 8;
  const u32 voffR = g * 2048 + (64 * half + 16 * wm4 + r16) * 16;
  u32x2 hp[5];
  {
    if (w == 7) { stageGB(0); stageGB(1); }
    u32x4 a[10];
    const char* osrc = ws + WS_O + (p * 80 + 40 * wk) * 2048;
#pragma unroll
    for (int s = 0; s < 10; ++s) gld_sc1(a[s], osrc + s * 8192, voffR);
    const u32 bb = L_WOB + wk * 10 * 5120;
    u32x4 b[2][5];
    auto rdB = [&](int s) {
#pragma unroll
      for (int t = 0; t < 5; ++t) b[s & 1][t] = ldstr2(bb + adW_lo + s * 5120 + t * 32, bb + adW_hi + s * 5120 + t * 32);
      SB();
    };
    rdB(0);
#pragma unroll
    for (int s = 0; s < 10; ++s) {
      if (s < 9) rdB(s + 1);
      switch (s) {
        case 0: vm_wait_use<9>(a[0]); break; case 1: vm_wait_use<8>(a[1]); break; case 2: vm_wait_use<7>(a[2]); break;
        case 3: vm_wait_use<6>(a[3]); break; case 4: vm_wait_use<5>(a[4]); break; case 5: vm_wait_use<4>(a[5]); break;
        case 6: vm_wait_use<3>(a[6]); break; case 7: vm_wait_use<2>(a[7]); break; case 8: vm_wait_use<1>(a[8]); break;
        default: vm_wait_use<0>(a[9]); break;
      }
      if (s == 0) STAMP(28);
#pragma unroll
      for (int t = 0; t < 5; ++t) hacc[t] = mfma(b[s & 1][t], a[s], hacc[t]);
      SB();
    }
    STAMP(8);
    bar();
    if (wk == 1) {
#pragma unroll
      for (int t = 0; t < 5; ++t) *(LAS f32x4*)(smem + L_RED + (wm4 * 5 + t) * 1024 + lane * 16) = hacc[t];
    }
    bar();
    if (wk == 0) {
      float s1 = 0.f, s2 = 0.f;
#pragma unroll
      for (int t = 0; t < 5; ++t) {
        const f32x4 v = hacc[t] + *(LAS const f32x4*)(smem + L_RED + (wm4 * 5 + t) * 1024 + lane * 16);
        const u32 lo = pack2(v[0], v[1]), hi = pack2(v[2], v[3]);
        hp[t] = u32x2{lo, hi};
        s1 = dot2(hi, ONES2, dot2(lo, ONES2, s1)); s2 = dot2(hi, hi, dot2(lo, lo, s2));
      }
      const u32 hb = WS_H + (p * 80 + 10 * slice + 2 * (g & 1) + (g >> 1)) * 2048 + (64 * half + 16 * wm4 + r16) * 16;
      st128_sc1(rs, hb, quad2chunk(hp[0], hp[1]));
      st128_sc1(rs, hb + 4 * 2048, quad2chunk(hp[2], hp[3]));
      const u32x4 last = quad2chunk(hp[4], hp[4]);
      if ((g & 1) == 0) st128_sc1(rs, hb + 8 * 2048, last);
      s1 = gsum(s1); s2 = gsum(s2);
      if (g == 0) st64_sc1(rs, WS_ST + (p * 8 + slice) * 1024 + (64 * half + 16 * wm4 + r16) * 8, u32x2{__float_as_uint(s1), __float_as_uint(s2)});
      vm_wait<0>();
    } else {
#pragma unroll
      for (int t = 0; t < 5; ++t) hp[t] = u32x2{0, 0};
      if (w == 7 && bx == 0) {
#pragma unroll
        for (int i = 0; i < 4; ++i) poll_flags(ws, WS_FLAG + (64 * i + lane) * 256, tag);
        if (lane == 0) __hip_atomic_store((GAS u64*)(ws + WS_EPOCH), tag, RLX_AGENT);
      }
    }
  }
  bar();
  STAMP(9);
  if (w == 7) {
    if (lane == 0) __hip_atomic_store((GAS u64*)(ws + flag_off(2, p, j)), tag, RLX_AGENT);
    poll_flags(ws, flag_off(2, p, r16), tag);
  } else {
    stageGB(2); stageGB(3);
  }
  bar();
  STAMP(10);

  {
    const int wm = w >> 2, wn = w & 3;
    if (w == 7) { stageGB(2); stageGB(3); }
    const char* hsrc = ws + WS_H + (p * 80 + w) * 2048;
    auto stageGA = [&](int c) {
      int kc = c + rotg; kc = kc >= 10 ? kc - 10 : kc;
      const u32 dst = L_GA + (c % 3) * GA_SLOT + w * 2048;
      dma_sc1(hsrc + kc * 16384, lane * 16, dst); dma_sc1(hsrc + kc * 16384 + 1024, lane * 16, dst + 1024);
    };
    dma_sc1(ws + WS_ST + (p * 8 + w) * 1024, lane * 16, L_ST + w * 1024);
    stageGA(0); stageGA(1); stageGA(2);
    u32 adA[2], adB[2][3];
#pragma unroll
    for (int s = 0; s < 2; ++s) {
      const int ks = s ^ wm;
      adA[s] = L_GA + (4 * ks + g) * 2048 + (64 * wm + r16) * 16;
#pragma unroll
      for (int t = 0; t < 3; ++t) {
        const int mat = t == 0 ? 0 : (t == 1 ? 1 : (pq >> 1)), c = t < 2 ? 3 * wn + (pq >> 1) : 3 * wn + 2;
        int cp = c + mat + 2 * (g & 1); cp = cp >= 12 ? cp - 12 : cp;
        adB[s][t] = L_GB + mat * 12288 + (32 * ks + 8 * g + q4) * 192 + cp * 16 + (pq & 1) * 8;
      }
    }
    f32x4 acc[4][3];
#pragma unroll
    for (int m = 0; m < 4; ++m)
#pragma unroll
      for (int t = 0; t < 3; ++t) acc[m][t] = f32x4{0, 0, 0, 0};
    f32x4 csum[3] = {{0, 0, 0, 0}, {0, 0, 0, 0}, {0, 0, 0, 0}};
    u32x4 fa[2][4]; u32x2 fb[2][3][2];
    auto rdp = [&](int c, int s, int i) {
      const u32 sa = (c % 3) * GA_SLOT, sb = (c & 3) * GB_SLOT;
      if (i < 2) fb[s][0][i] = ldstr(sb + adB[s][0] + i * 768);
      else if (i == 2) fa[s][0] = lds128(sa + adA[s]);
      else if (i < 7) fb[s][1 + ((i - 3) >> 1)][(i - 3) & 1] = ldstr(sb + adB[s][1 + ((i - 3) >> 1)] + ((i - 3) & 1) * 768);
      else if (i < 10) fa[s][i - 6] = lds128(sa + adA[s] + (i - 6) * 256);
    };
    auto mm = [&](int s, int c) {
      u32x4 b[3];
#pragma unroll
      for (int t = 0; t < 3; ++t) b[t] = cat(fb[s][t][0], fb[s][t][1]);
      const int cr = s == 0 ? c : c + 1;
#pragma unroll
      for (int i = 0; i < 12; ++i) {
        acc[i / 3][i % 3] = mfma(b[i % 3], fa[s][i / 3], acc[i / 3][i % 3]);
        if (cr < 10) rdp(cr, s ^ 1, i);
        if (s == 1 && i == 10 && c + 3 < 10) stageGA(c + 3);
        if (s == 1 && i == 11 && c == 3) warm(wd + rr * 61440 + w * 7680, (lane < 60 ? lane : 59) * 128, L_WARM);
        SB();
      }
      if (s == 0) {
#pragma unroll
        for (int t = 0; t < 3; ++t) {
          csum[t] = mfma(b[t], ones8, csum[t]);
          if (c >= 1 && c + 3 < 10) stageGBp(c + 3, t);
          SB();
        }
      }
    };
    vm_wait<4>();
    bar();
    STAMP(19);
#pragma unroll
    for (int i = 0; i < 10; ++i) rdp(0, 0, i);
    SB();
#pragma unroll
    for (int c = 0; c < 10; ++c) {
      if (c == 1) {
        const f32x2 u0 = *(LAS const f32x2*)(smem + L_ST + (2 * g) * 1024 + (16 * w + r16) * 8), u1 = *(LAS const f32x2*)(smem + L_ST + (2 * g + 1) * 1024 + (16 * w + r16) * 8);
        const float s1 = gsum(u0[0] + u1[0]), s2 = gsum(u0[1] + u1[1]);
        const float mean = s1 * (1.f / 640.f), var = __builtin_fmaxf(s2 * (1.f / 640.f) - mean * mean, 0.f);
        if (g == 0) *(LAS f32x2*)(smem + L_MISC + (16 * w + r16) * 8) = f32x2{mean, __builtin_amdgcn_rsqf(var + LN_EPS)};
      }
      mm(0, c);
      if (c < 9) {
        if (c == 0) vm_wait<2>(); else if (c == 1) vm_wait<5>(); else if (c <= 6) vm_wait<8>(); else if (c == 7) vm_wait<5>(); else vm_wait<0>();
        bar();
        if (c == 1) STAMP(29);
        if (c == 4) STAMP(20);
        if (c == 7) STAMP(30);
      }
      mm(1, c);
    }
    STAMP(11);
    if (r16 == 0) {
#pragma unroll
      for (int t = 0; t < 3; ++t) *(LAS f32x4*)(smem + L_ST + (wm * 192 + wn * 48 + t * 16 + 4 * g) * 4) = csum[t];
    }
    bar();
    f32x4 cs[3];
#pragma unroll
    for (int t = 0; t < 3; ++t) cs[t] = *(LAS const f32x4*)(smem + L_ST + (wn * 48 + t * 16 + 4 * g) * 4) + *(LAS const f32x4*)(smem + L_ST + (192 + wn * 48 + t * 16 + 4 * g) * 4);
    u32x2 p01[4]; f32x4 t2[4];
#pragma unroll
    for (int m = 0; m < 4; ++m) {
      const f32x2 st = *(LAS const f32x2*)(smem + L_MISC + (64 * wm + 16 * m + r16) * 8);
      const f32x4 gv = (acc[m][0] - st[0] * cs[0]) * st[1], uv = (acc[m][1] - st[0] * cs[1]) * st[1];
      const f32x4 v = gv * uv;
      p01[m] = u32x2{pack2(v[0], v[1]), pack2(v[2], v[3])};
      t2[m] = (acc[m][2] - st[0] * cs[2]) * st[1];
    }
    u32x2 p2[2];
#pragma unroll
    for (int mm = 0; mm < 2; ++mm) {
      f32x4 v;
#pragma unroll
      for (int r = 0; r < 4; ++r) {
        auto sw = __builtin_amdgcn_permlane32_swap(__float_as_uint(t2[2 * mm][r]), __float_as_uint(t2[2 * mm + 1][r]), false, false);
        v[r] = __uint_as_float(sw[0]) * __uint_as_float(sw[1]);
      }
      p2[mm] = u32x2{pack2(v[0], v[1]), pack2(v[2], v[3])};
    }
    const u32 hb = WS_HH + (p * 192 + 12 * j + 3 * wn) * 2048 + (64 * wm + r16) * 16;
    st128_sc1(rs, hb + (g >> 1) * 2048 + (g & 1) * 256, quad2chunk(p01[0], p01[1]));
    st128_sc1(rs, hb + (g >> 1) * 2048 + (2 + (g & 1)) * 256, quad2chunk(p01[2], p01[3]));
    st128_sc1(rs, hb + 2 * 2048 + (2 * (g & 1) + (g >> 1)) * 256, quad2chunk(p2[0], p2[1]));
    vm_wait<0>();
  }
  bar();
  STAMP(12);

  {
    const int rotd = (slice * 6) >> 3;
    auto stageD = [&](int c) {
      int kc = c + rotd; kc = kc >= 6 ? kc - 6 : kc;
      const char* b = wd + slice * 160 + (size_t)(kc * 8 + w) * 32 * 1280;
      const u32 dst = (c & 3) * 40960 + w * 5120;
#pragma unroll
      for (int i = 0; i < 5; ++i) dma(b, voffW[i], dst + i * 1024);
    };
    if (w == 7) {
      if (lane == 0) __hip_atomic_store((GAS u64*)(ws + flag_off(3, p, j)), tag, RLX_AGENT);
      poll_flags(ws, flag_off(3, p, r16), tag);
    } else {
      stageD(0); stageD(1);
    }
    bar();
    STAMP(13);
    if (w == 7) { stageD(0); stageD(1); }
    f32x4 acc[5];
#pragma unroll
    for (int t = 0; t < 5; ++t) { const u32 a = hp[t][0], b = hp[t][1]; acc[t] = f32x4{bflo(a), bfhi(a), bflo(b), bfhi(b)}; }
    u32x4 a[6][4];
    const char* asrc = ws + WS_HH + (p * 192 + 16 * wk) * 2048;
    auto loadA = [&](int c) {
      int kc = c + rotd; kc = kc >= 6 ? kc - 6 : kc;
#pragma unroll
      for (int s = 0; s < 4; ++s) gld_sc1(a[c][s], asrc + kc * 65536 + s * 8192, voffR);
    };
    loadA(0); loadA(1); stageD(2);
    u32x4 b[2][5];
    auto rdB = [&](int c, int s) {
      const u32 bb = (c & 3) * 40960 + wk * 4 * 5120;
#pragma unroll
      for (int t = 0; t < 5; ++t) b[s & 1][t] = ldstr2(bb + adW_lo + s * 5120 + t * 32, bb + adW_hi + s * 5120 + t * 32);
      SB();
    };
    vm_wait<9>();
    asm volatile("" : "+v"(a[0][0]), "+v"(a[0][1]), "+v"(a[0][2]), "+v"(a[0][3]));
    bar();
    STAMP(21);
    rdB(0, 0);
#pragma unroll
    for (int c = 0; c < 6; ++c) {
#pragma unroll
      for (int s = 0; s < 4; ++s) {
        if (s < 3) rdB(c, s + 1);
        else if (c < 5) {
          if (c <= 3) vm_wait<5>(); else vm_wait<0>();
          asm volatile("" : "+v"(a[c + 1][0]), "+v"(a[c + 1][1]), "+v"(a[c + 1][2]), "+v"(a[c + 1][3]));
          bar();
          if (c == 0) STAMP(22); if (c == 1) STAMP(23); if (c == 2) STAMP(24); if (c == 3) STAMP(25);
          rdB(c + 1, 0);
        }
#pragma unroll
        for (int t = 0; t < 5; ++t) acc[t] = mfma(b[s & 1][t], a[c][s], acc[t]);
        SB();
        if (s == 3) { if (c + 2 < 6) loadA(c + 2); if (c + 3 < 6) stageD(c + 3); SB(); }
      }
    }
    STAMP(14);
    constexpr u32 L_RED2 = 2 * 40960;
    if (wk == 1) {
#pragma unroll
      for (int t = 0; t < 5; ++t) *(LAS f32x4*)(smem + L_RED2 + (wm4 * 5 + t) * 1024 + lane * 16) = acc[t];
    }
    bar();
    if (wk == 0) {
      u32x2 op[5];
#pragma unroll
      for (int t = 0; t < 5; ++t) {
        const f32x4 v = acc[t] + *(LAS const f32x4*)(smem + L_RED2 + (wm4 * 5 + t) * 1024 + lane * 16);
        op[t] = u32x2{pack2(v[0], v[1]), pack2(v[2], v[3])};
      }
      char* orow = out + (size_t)(128 * p + 64 * half + 16 * wm4 + r16) * 1280 + slice * 160 + (2 * (g & 1) + (g >> 1)) * 16;
      *(GAS u32x4*)(orow) = quad2chunk(op[0], op[1]);
      *(GAS u32x4*)(orow + 64) = quad2chunk(op[2], op[3]);
      const u32x4 last = quad2chunk(op[4], op[4]);
      if ((g & 1) == 0) *(GAS u32x4*)(orow + 128) = last;
    }
    STAMP(15);
  }
}

extern "C" void kernel_launch(void* const* d_in, const int* in_sizes, int n_in, void* d_out, int out_size, void* d_ws, size_t ws_size, hipStream_t stream) {
  (void)in_sizes; (void)n_in; (void)out_size; (void)ws_size;
  (void)hipFuncSetAttribute((const void*)fused_block, hipFuncAttributeMaxDynamicSharedMemorySize, LDS_BYTES);
  fused_block<<<256, 512, LDS_BYTES, stream>>>((const char*)d_in[0], (const char*)d_in[1], (const char*)d_in[2], (const char*)d_in[4], (const char*)d_in[5],
                                               (const char*)d_in[6], (char*)d_out, (char*)d_ws);
}
```

```cpp
#include <hip/hip_runtime.h>
#include <stdint.h>

typedef unsigned short u16;
typedef unsigned int u32;
typedef unsigned long long u64;
typedef __bf16 bf16x8 __attribute__((ext_vector_type(8)));
typedef __bf16 bf16x2 __attribute__((ext_vector_type(2)));
typedef short s16x4 __attribute__((ext_vector_type(4)));
typedef float f32x4 __attribute__((ext_vector_type(4)));
typedef float f32x2 __attribute__((ext_vector_type(2)));
typedef u32 u32x4 __attribute__((ext_vector_type(4)));
typedef u32 u32x2 __attribute__((ext_vector_type(2)));
#define LAS __attribute__((address_space(3)))
#define GAS __attribute__((address_space(1)))
#define DI __device__ __forceinline__
#define RLX_AGENT __ATOMIC_RELAXED, __HIP_MEMORY_SCOPE_AGENT

#define RG 5
#define OUT_AUX 16
#define ATT_PRIO 0
#define STAMPS 0

constexpr u32 WS_EPOCH = 0;
constexpr u32 WS_DENSE = 2048;
constexpr u32 WS_FLAG = 4096;
constexpr u32 WS_QKV = WS_FLAG + 4 * 256 * 256;
constexpr u32 WS_O = WS_QKV + 16 * 240 * 2048;
constexpr u32 WS_H = WS_O + 16 * 80 * 2048;
constexpr u32 WS_HH = WS_H + 16 * 80 * 2048;
constexpr u32 WS_ST = WS_HH + 16 * 192 * 2048;
constexpr u32 WS_RS = WS_ST + 16 * 8 * 1024;
constexpr u32 WS_WB = WS_RS + 2048 * 16;
constexpr u32 WT_SLICE = 68 * 5120;
constexpr u32 WS_WT = WS_WB + 3072;
constexpr u32 WS_END = WS_WT + 8 * WT_SLICE;
constexpr u32 WS_STAMP = WS_END;
constexpr u64 TAG_STEP = 0x9E3779B97F4A7C15ull;

constexpr u32 LDS_BYTES = 163840;
constexpr u32 L_MISC = 161792;
constexpr u32 L_K = 0, KCH = 6144;
constexpr u32 L_V = 6 * KCH, VCH = 6208;
constexpr u32 L_WOB = L_MISC - 102400;
__device__ constexpr u32 gb_off(int c) { return (c & 3) == 2 ? 0 : ((c & 3) == 3 ? 24576 : ((c & 3) == 0 ? 65536 : 106496)); }
__device__ constexpr u32 ga_off(int c) { return c % 3 == 1 ? 49152 : (c % 3 == 2 ? 90112 : 131072); }
constexpr u32 L_ST = 147456;
constexpr u32 L_WB = 49152;
constexpr u32 L_RED = 24576;
constexpr u32 L_WARM = L_MISC + 1792;
constexpr u32 L_RT = 131072;
constexpr u32 SPIN_CAP = 1u << 15;
constexpr float LN_EPS = 1e-5f;

extern __shared__ __attribute__((aligned(16))) char smem[];

DI u32x4 lds128(u32 off) { return *(LAS const u32x4*)(smem + off); }
DI u32x2 ldstr(u32 off) { return __builtin_bit_cast(u32x2, __builtin_amdgcn_ds_read_tr16_b64_v4i16((LAS s16x4*)(smem + off))); }
DI u32x4 ldstr2(u32 lo, u32 hi) { const u32x2 a = ldstr(lo), b = ldstr(hi); return u32x4{a[0], a[1], b[0], b[1]}; }
DI f32x4 mfma(u32x4 a, u32x4 b, f32x4 c) { return __builtin_amdgcn_mfma_f32_16x16x32_bf16(__builtin_bit_cast(bf16x8, a), __builtin_bit_cast(bf16x8, b), c, 0, 0, 0); }
typedef float f32x16 __attribute__((ext_vector_type(16)));
DI f32x16 mfma32(u32x4 a, u32x4 b, f32x16 c) { return __builtin_amdgcn_mfma_f32_32x32x16_bf16(__builtin_bit_cast(bf16x8, a), __builtin_bit_cast(bf16x8, b), c, 0, 0, 0); }
DI float max3(float a, float b, float c) { return __builtin_fmaxf(__builtin_fmaxf(a, b), c); }
DI f32x4 mfma16(u32x2 a, u32x2 b, f32x4 c) { return __builtin_amdgcn_mfma_f32_16x16x16bf16_1k(__builtin_bit_cast(s16x4, a), __builtin_bit_cast(s16x4, b), c, 0, 0, 0); }
DI float dot2(u32 a, u32 b, float c) { return __builtin_amdgcn_fdot2_f32_bf16(__builtin_bit_cast(bf16x2, a), __builtin_bit_cast(bf16x2, b), c, false); }
DI u32 pack2(float lo, float hi) { const f32x2 v = {lo, hi}; return __builtin_bit_cast(u32, __builtin_convertvector(v, bf16x2)); }
DI float bflo(u32 v) { return __uint_as_float(v << 16); }
DI float bfhi(u32 v) { return __uint_as_float(v & 0xffff0000u); }
constexpr u32 ONES2 = 0x3f803f80u;
DI float sum8(u32x4 a, float s) { const u32 a0 = a[0], a1 = a[1], a2 = a[2], a3 = a[3]; return dot2(a3, ONES2, dot2(a2, ONES2, dot2(a1, ONES2, dot2(a0, ONES2, s)))); }
DI float sq8(u32x4 a, float s) { const u32 a0 = a[0], a1 = a[1], a2 = a[2], a3 = a[3]; return dot2(a3, a3, dot2(a2, a2, dot2(a1, a1, dot2(a0, a0, s)))); }
DI float gsum(float x) {
  auto a = __builtin_amdgcn_permlane16_swap(__float_as_uint(x), __float_as_uint(x), false, false);
  x = __uint_as_float(a[0]) + __uint_as_float(a[1]);
  auto b = __builtin_amdgcn_permlane32_swap(__float_as_uint(x), __float_as_uint(x), false, false);
  return __uint_as_float(b[0]) + __uint_as_float(b[1]);
}
DI float gmax(float x) {
  auto a = __builtin_amdgcn_permlane16_swap(__float_as_uint(x), __float_as_uint(x), false, false);
  x = __builtin_fmaxf(__uint_as_float(a[0]), __uint_as_float(a[1]));
  auto b = __builtin_amdgcn_permlane32_swap(__float_as_uint(x), __float_as_uint(x), false, false);
  return __builtin_fmaxf(__uint_as_float(b[0]), __uint_as_float(b[1]));
}
DI u32x4 quad2chunk(u32x2 A, u32x2 B) {
  const u32 a0 = A[0], a1 = A[1], b0 = B[0], b1 = B[1];
  auto s0 = __builtin_amdgcn_permlane16_swap(a0, b0, false, false);
  auto s1 = __builtin_amdgcn_permlane16_swap(a1, b1, false, false);
  return u32x4{s0[0], s1[0], s0[1], s1[1]};
}
DI void dma(const void* sbase, u32 voff, u32 lds) {
  asm volatile("s_mov_b32 m0, %2\n\ts_nop 0\n\tglobal_load_lds_dwordx4 %0, %1" ::"v"(voff), "s"(sbase), "s"(lds));
}
DI void dma_sc1(const void* sbase, u32 voff, u32 lds) {
  asm volatile("s_mov_b32 m0, %2\n\ts_nop 0\n\tglobal_load_lds_dwordx4 %0, %1 sc1" ::"v"(voff), "s"(sbase), "s"(lds));
}
DI void gld_sc1(u32x4& dst, const void* sbase, u32 voff) {
  asm volatile("global_load_dwordx4 %0, %1, %2 sc1" : "=v"(dst) : "v"(voff), "s"(sbase));
}
DI void gld(u32x4& dst, const void* sbase, u32 voff) {
  asm volatile("global_load_dwordx4 %0, %1, %2" : "=v"(dst) : "v"(voff), "s"(sbase));
}
DI void gld64(u32x2& dst, const void* sbase, u32 voff) {
  asm volatile("global_load_dwordx2 %0, %1, %2" : "=v"(dst) : "v"(voff), "s"(sbase));
}
template <int N> DI void vm_wait() { asm volatile("s_waitcnt vmcnt(%0)" ::"n"(N) : "memory"); }
template <int N> DI void vm_wait_use(u32x4& r) { asm volatile("s_waitcnt vmcnt(%1)" : "+v"(r) : "n"(N)); }
#define SB() __builtin_amdgcn_sched_barrier(0)
DI void warm(const void* sbase, u32 voff, u32 lds) {
  asm volatile("s_mov_b32 m0, %2\n\ts_nop 0\n\tglobal_load_lds_dword %0, %1" ::"v"(voff), "s"(sbase), "s"(lds));
}
DI void bar0() { asm volatile("s_barrier" ::: "memory"); }
DI u32x4 cat(u32x2 a, u32x2 b) { return u32x4{a[0], a[1], b[0], b[1]}; }
DI void bar() { asm volatile("s_waitcnt lgkmcnt(0)\n\ts_barrier" ::: "memory"); }
DI void st128_sc1(__amdgpu_buffer_rsrc_t rs, u32 off, u32x4 v) { __builtin_amdgcn_raw_buffer_store_b128(v, rs, (int)off, 0, 16); }
DI void st64_sc1(__amdgpu_buffer_rsrc_t rs, u32 off, u32x2 v) { __builtin_amdgcn_raw_buffer_store_b64(v, rs, (int)off, 0, 16); }

DI void poll_flags(const char* ws, u32 off, u64 tag) {
  GAS u64* a = (GAS u64*)(ws + off);
  for (u32 spins = 0; spins < SPIN_CAP; ++spins) {
    const u64 v = __hip_atomic_load(a, RLX_AGENT);
    if (__all(v == tag)) break;
  }
  __builtin_amdgcn_fence(__ATOMIC_ACQUIRE, "wavefront");
}
DI u32 flag_off(int phase, int p, int j) { return WS_FLAG + (u32)((phase * 256 + p * 16 + j) * 256); }

#define STAMP(i) do {} while (0)

__global__ __launch_bounds__(512, 2) void fused_block(const char* __restrict__ x, const char* __restrict__ wqkv, const char* __restrict__ wo,
                                                      const char* __restrict__ wg, const char* __restrict__ wu, const char* __restrict__ wd,
                                                      char* __restrict__ out, char* ws) {
  const int tid = threadIdx.x, lane = tid & 63, w = __builtin_amdgcn_readfirstlane(tid >> 6);
  const int r16 = lane & 15, g = lane >> 4, q4 = r16 >> 2, pq = r16 & 3;
  const int bx = blockIdx.x, xcd = bx & 7, rr = bx >> 3;
  const int p = (xcd >> 1) * 4 + (rr >> 3), j = (xcd & 1) * 8 + (rr & 7);
  const int pa = rr >> 1, ja = 2 * xcd + (rr & 1);
  const int pg = 8 * (xcd >> 2) + (rr >> 2), jg = 4 * (xcd & 3) + (rr & 3);
  const int half = j >> 3, slice = j & 7;
  constexpr int PW = 3;
  const __amdgpu_buffer_rsrc_t rs = __builtin_amdgcn_make_buffer_rsrc(ws, 0, WS_END, 0x00020000);
  STAMP(0);
  u64 tag;
  auto load_epoch = [&](u32x2& e) { const u32 z = 0; asm volatile("global_load_dwordx2 %0, %1, %2 sc1" : "=v"(e) : "v"(z), "s"(ws + WS_EPOCH)); };
  auto make_tag = [&](u32x2& e) {
    asm volatile("" : "+v"(e));
    const u32 e0 = e[0], e1 = e[1];
    tag = (((u64)(u32)__builtin_amdgcn_readfirstlane(e1) << 32) | (u32)__builtin_amdgcn_readfirstlane(e0)) + TAG_STEP;
  };
  const char* xa = x + (size_t)p * 128 * 1280;
  const u32x4 ones8 = {ONES2, ONES2, ONES2, ONES2};
  const char* wt = ws + WS_WT + slice * WT_SLICE;
  auto rt_addr = [&](int piece, int gi, u32 slot, u32& l, u32& d) {
    const int tile = 4 * piece + g, sl = tile / 5, t = tile - sl * 5;
    l = slot + q4 * 1280 + tile * 32 + pq * 8;
    d = WS_WT + sl * WT_SLICE + (gi >> 2) * 5120 + t * 1024 + (gi & 3) * 256 + r16 * 16;
  };

  if (j < 15) {
    const int wm = w >> 2, wn = w & 3;
    u32 voffA[2], voffB[2];
#pragma unroll
    for (int i = 0; i < 2; ++i) {
      const int ra = 16 * w + 8 * i + (lane >> 3);
      voffA[i] = ra * 1280 + (((lane & 7) ^ ((ra >> 1) & 7)) << 4);
      const int rb = 8 * w + 4 * i + g, f = (rb & 3) | (((rb >> 3) & 1) << 2);
      voffB[i] = rb * 3840 + (((((r16 >> 1) ^ f) << 1) | (r16 & 1)) << 4);
    }
    const char* wb = wqkv + j * 256;
    const int rot = RG == 10 ? ((rr & 7) + ((rr & 7) >> 2) + ((5 * (rr >> 3)) >> 1)) % 10 : ((rr & 7) + (rr >> 3)) % RG;
    auto stage = [&](int c, int piece) {
      int kc = c % RG + rot; kc = (kc >= RG ? kc - RG : kc) + (c / RG) * RG;
      const u32 slot = (c & 3) * 32768;
      const char* a = xa + kc * 128; const char* b = wb + (size_t)kc * 64 * 3840;
      if (piece < 0 || piece == 0) dma(a, voffA[0], slot + w * 2048);
      if (piece < 0 || piece == 1) dma(a, voffA[1], slot + w * 2048 + 1024);
      if (piece < 0 || piece == 2) dma(b, voffB[0], slot + 16384 + w * 2048);
      if (piece < 0 || piece == 3) dma(b, voffB[1], slot + 16384 + w * 2048 + 1024);
    };
    stage(0, -1);
    u32x2 epoch;
    load_epoch(epoch);
    {
      u64 pc; asm volatile("s_getpc_b64 %0" : "=s"(pc));
      if (w == 0) warm((const char*)(pc & ~127ull) + rr * 1280, (lane < 10 ? lane : 9) * 128, L_WARM);
    }
    stage(1, -1);
    u32 adA[2], adB[2];
#pragma unroll
    for (int ks = 0; ks < 2; ++ks) adA[ks] = (64 * wm + r16) * 128 + (((4 * ks + g) ^ ((r16 >> 1) & 7)) << 4);
#pragma unroll
    for (int n = 0; n < 2; ++n) adB[n] = 16384 + (8 * g + q4) * 256 + (((2 * wn + (n ^ wm)) ^ (q4 | ((g & 1) << 2))) << 5) + pq * 8;
    f32x4 acc[4][2], csum = {0, 0, 0, 0};
#pragma unroll
    for (int m = 0; m < 4; ++m) { acc[m][0] = f32x4{0, 0, 0, 0}; acc[m][1] = f32x4{0, 0, 0, 0}; }
    u32x4 fa[2][4]; u32x2 fb[2][2][2];
    u32x4 rsg[4];
    auto ld_rs = [&]() {
#pragma unroll
      for (int m = 0; m < 4; ++m) gld_sc1(rsg[m], ws + WS_RS + (p * 128 + 64 * wm + 16 * m) * 16, r16 * 16);
    };
    auto rdp = [&](int c, int ks, int i) {
      const u32 slot = (c & 3) * 32768;
      if (i == 0 || i == 1) fb[ks][0][i] = ldstr(slot + adB[0] + ks * 8192 + i * 1024);
      else if (i == 2) fa[ks][0] = lds128(slot + adA[ks]);
      else if (i == 3 || i == 4) fb[ks][1][i - 3] = ldstr(slot + adB[1] + ks * 8192 + (i - 3) * 1024);
      else if (i < 8) fa[ks][i - 4] = lds128(slot + adA[ks] + (i - 4) * 2048);
    };
    auto mm = [&](int ks, int cr, int cn) {
      const u32x4 b0 = cat(fb[ks][0][0], fb[ks][0][1]), b1 = cat(fb[ks][1][0], fb[ks][1][1]);
#pragma unroll
      for (int i = 0; i < 9; ++i) {
        if (i < 8) acc[i >> 1][i & 1] = mfma((i & 1) ? b1 : b0, fa[ks][i >> 1], acc[i >> 1][i & 1]);
        else csum = mfma(b0, ones8, csum);
        if (cr < 10) rdp(cr, ks ^ 1, i);
        if (cn < 10 && i == 4) stage(cn, 2 * (ks ^ 1));
        if (cn < 10 && i == 8) stage(cn, 2 * (ks ^ 1) + 1);
        SB();
      }
    };
    vm_wait<4>();
    make_tag(epoch);
    bar();
    STAMP(16);
#pragma unroll
    for (int i = 0; i < 8; ++i) rdp(0, 0, i);
    SB();
    stage(2, 0); stage(2, 1);
    SB();
#pragma unroll
    for (int c = 0; c < 10; ++c) {
      mm(0, c, c + 2);
      if (c < 9) {
        if (c <= 7) vm_wait<4>(); else vm_wait<0>();
        bar0();
        if (c == 8) {
          ld_rs();
          if (j < 12 && w < 5) {
            dma(wd + (p * 12 + j) * 10240 + w * 2048, lane * 16, L_RT + w * 2048);
            dma(wd + (p * 12 + j) * 10240 + w * 2048 + 1024, lane * 16, L_RT + w * 2048 + 1024);
          }
          SB();
        }
      }
      mm(1, c + 1, c + 3);
    }
    STAMP(1);
    if (r16 == 0) *(LAS f32x4*)(smem + L_MISC + 1024 + (32 * wn + 16 * wm + 4 * g) * 4) = csum;
    if (j < 12 && w < 5) vm_wait<2>(); else vm_wait<0>();
    for (u32 spins = 0; spins < SPIN_CAP; ++spins) {
      asm volatile("" : "+v"(rsg[0]), "+v"(rsg[1]), "+v"(rsg[2]), "+v"(rsg[3]));
      bool ok = true;
#pragma unroll
      for (int m = 0; m < 4; ++m) ok = ok && rsg[m][1] == (u32)tag && rsg[m][3] == (u32)(tag >> 32);
      if (__all(ok)) break;
      ld_rs();
      vm_wait<0>();
    }
    asm volatile("" : "+v"(rsg[0]), "+v"(rsg[1]), "+v"(rsg[2]), "+v"(rsg[3]));
    bar();
    STAMP(17);
    const float qs = j < 5 ? 0.22360679774997896f * 1.4426950408889634f : 1.f;
    f32x4 cs[2];
#pragma unroll
    for (int n = 0; n < 2; ++n) cs[n] = *(LAS const f32x4*)(smem + L_MISC + 1024 + (32 * wn + 16 * (n ^ wm) + 4 * g) * 4);
    const u32 qb = WS_QKV + (p * 240 + 16 * j + 4 * wn + 2 * ((g & 1) ^ wm) + (g >> 1)) * 2048 + (64 * wm + r16) * 16;
#pragma unroll
    for (int m = 0; m < 4; ++m) {
      const u32 um = rsg[m][0], ur = rsg[m][2];
      const float mean = __uint_as_float(um), sc = __uint_as_float(ur) * qs;
      u32x2 pk[2];
#pragma unroll
      for (int n = 0; n < 2; ++n) {
        const f32x4 v = (acc[m][n] - mean * cs[n]) * sc;
        pk[n] = u32x2{pack2(v[0], v[1]), pack2(v[2], v[3])};
      }
      st128_sc1(rs, qb + m * 256, quad2chunk(pk[0], pk[1]));
    }
  } else {
    u32x2 epoch;
    load_epoch(epoch);
    u32x4 xv[2][10];
#pragma unroll
    for (int s2 = 0; s2 < 2; ++s2)
#pragma unroll
      for (int t = 0; t < 10; ++t) gld(xv[s2][t], xa + (16 * w + 8 * s2) * 1280 + t * 128, (lane >> 3) * 1280 + (lane & 7) * 16);
    if (w < 5) {
#pragma unroll
      for (int i = 0; i < 10; ++i) dma(wo + (p * 5 + w) * 10240 + i * 1024, lane * 16, w * 10240 + i * 1024);
    }
#pragma unroll
    for (int s2 = 0; s2 < 2; ++s2) {
      if (s2 == 0) { if (w < 5) vm_wait<20>(); else vm_wait<10>(); make_tag(epoch); } else { if (w < 5) vm_wait<11>(); else vm_wait<1>(); }
      float s1[2] = {0.f, 0.f}, sq[2] = {0.f, 0.f};
#pragma unroll
      for (int t = 0; t < 10; ++t) { asm volatile("" : "+v"(xv[s2][t])); s1[t & 1] = sum8(xv[s2][t], s1[t & 1]); sq[t & 1] = sq8(xv[s2][t], sq[t & 1]); }
      float a1 = s1[0] + s1[1], a2 = sq[0] + sq[1];
#pragma unroll
      for (int o = 1; o < 8; o <<= 1) { a1 += __shfl_xor(a1, o); a2 += __shfl_xor(a2, o); }
      const float mean = a1 * (1.f / 640.f), var = __builtin_fmaxf(a2 * (1.f / 640.f) - mean * mean, 0.f);
      if ((lane & 7) == 0)
        st128_sc1(rs, WS_RS + (p * 128 + 16 * w + 8 * s2 + (lane >> 3)) * 16, u32x4{__float_as_uint(mean), (u32)tag, __float_as_uint(__builtin_amdgcn_rsqf(var + LN_EPS)), (u32)(tag >> 32)});
    }
    STAMP(16);
    if (w < 5) {
      vm_wait<2>();
      const int gi = p * 5 + w;
#pragma unroll
      for (int i = 0; i < 10; ++i) { u32 l, d; rt_addr(i, gi, w * 10240, l, d); st128_sc1(rs, d, ldstr2(l, l + 5120)); }
      float a1 = 0.f;
#pragma unroll
      for (int t = 0; t < 10; ++t) a1 = sum8(lds128(w * 10240 + (lane >> 3) * 1280 + t * 128 + (lane & 7) * 16), a1);
#pragma unroll
      for (int o = 1; o < 8; o <<= 1) a1 += __shfl_xor(a1, o);
      a1 *= (1.f / 640.f);
      const u32 hi = pack2(a1, 0.f), lo = pack2(a1 - bflo(hi), 0.f);
      if ((lane & 7) < 2) __builtin_amdgcn_raw_buffer_store_b16((u16)((lane & 1) ? lo : hi), rs, (int)(WS_WB + (lane & 1) * 1280 + (gi * 8 + (lane >> 3)) * 2), 0, 16);
    }
  }
  STAMP(18);
  vm_wait<0>();
  bar();
  STAMP(2);
  if (w == PW && lane == 0) __hip_atomic_store((GAS u64*)(ws + flag_off(0, p, j)), tag, RLX_AGENT);
  asm volatile("" ::: "memory");
  u32 kvsrc[8], kvdst[8];
#pragma unroll
  for (int i = 0; i < 8; ++i) {
    const int id = w + 8 * i, kv = id >= 30, r = id - 30 * kv, c = r / 6, r2 = r - 6 * c;
    int pp = pa - 2 + (r2 >> 1); pp = pp < 0 ? 0 : pp;
    kvsrc[i] = WS_QKV + (pp * 240 + 80 * (1 + kv) + 5 * ja + c) * 2048 + (r2 & 1) * 1024;
    kvdst[i] = (kv ? L_V + c * VCH : L_K + c * KCH) + r2 * 1024;
    asm volatile("" : "+s"(kvsrc[i]), "+s"(kvdst[i]));
  }
  if (tid < 384) {
    const bool valid = 128 * (pa - 2) + tid >= 0;
    *(LAS u32x4*)(smem + L_K + 5 * KCH + tid * 16) = u32x4{valid ? 0u : 0xf149u, 0u, ONES2, ONES2};
  }
  u32 rtl[2], rtg[2];
#pragma unroll
  for (int i = 0; i < 2; ++i) { rt_addr(2 * w + i, 80 + p * 12 + j, L_RT, rtl[i], rtg[i]); asm volatile("" : "+v"(rtl[i]), "+v"(rtg[i])); }
  u32 fl_put, fl_get;
  {
    const int e = r16 < 14 ? r16 : 0, sec = e < 2 ? 0 : (e < 8 ? 1 : 2), i = e < 2 ? e : (e - 2) % 6;
    int pp = e < 2 ? pa : pa - (i >> 1); pp = pp < 0 ? 0 : pp;
    fl_get = (g & 1) ? flag_off(0, r16, 15) : flag_off(0, pp, (80 * sec + 5 * ja + 4 * (i & 1)) >> 4);
    fl_put = lane == 0 ? flag_off(1, pa, ja) : WS_DENSE + bx * 8;
    asm volatile("" : "+v"(fl_get), "+v"(fl_put));
  }
  if (w == PW) poll_flags(ws, fl_get, tag);
  bar();
  STAMP(3);

  u32x2 xr[6];
  u32 voffG[3];
  {
    const int h = w & 1, qt = w >> 1, l32 = lane & 31, hh = lane >> 5;
    u32x4 qf[2];
    gld_sc1(qf[0], ws + WS_QKV + (pa * 240 + 5 * ja + 2 * h) * 2048 + qt * 512, hh * 2048 + l32 * 16);
    gld_sc1(qf[1], ws + WS_QKV + (pa * 240 + 5 * ja + 2 * h + 2) * 2048 + qt * 512, l32 * 16);
#pragma unroll
    for (int i = 0; i < 5; ++i) dma_sc1(ws + kvsrc[i], lane * 16, kvdst[i]);
    u32 dm[8];
#pragma unroll
    for (int m = 0; m < 8; ++m) {
      const int k0 = ((2 * m) & 3) + 8 * (m >> 1) + 4 * hh;
      dm[m] = (k0 <= l32 ? 0xffffu : 0u) | (k0 + 1 <= l32 ? 0xffff0000u : 0u);
      asm volatile("" : "+v"(dm[m]));
    }
    u32 adK0 = L_K + (2 * h + hh) * KCH + (32 * qt + l32) * 16, adK1 = L_K + (hh ? 5 : 2 * h + 2) * KCH + (32 * qt + l32) * 16;
    const u32 vrow = 32 * qt + 4 * hh + q4;
    u32 adV = (g & 1) == 0 ? L_V + (3 * h + (pq >> 1)) * VCH + vrow * 16 + (pq & 1) * 8
                           : (pq == 0 ? L_V + 2 * VCH + vrow * 16 + 8 * h : L_K + 5 * KCH + ((4 * hh + (h ? 4 : 12)) & 15) * 16 + 8);
    u32 voffwarm;
    { const int l6 = lane < 30 ? lane : 29, rw = l6 / 6; voffwarm = rw * 3072 + (l6 - rw * 6) * 128; }
#pragma unroll
    for (int i = 0; i < 3; ++i) {
      const int s = 64 * i + lane, rw = s / 12, cp = s - rw * 12;
      int c = cp - (w & 1) - 2 * ((rw >> 3) & 1); c = c < 0 ? c + 12 : c;
      voffG[i] = rw * 3072 + c * 16;
    }
    asm volatile("" : "+v"(voffwarm), "+v"(adK0), "+v"(adK1), "+v"(adV), "+v"(voffG[0]), "+v"(voffG[1]), "+v"(voffG[2]));
    vm_wait<1>();
    asm volatile("" : "+v"(qf[0]), "+v"(qf[1]));
    bar();
    STAMP(4);
    const f32x16 zero16 = {0, 0, 0, 0, 0, 0, 0, 0, 0, 0, 0, 0, 0, 0, 0, 0};
    u32x4 kf[9][2];
    constexpr int ord[9] = {0, 8, 1, 2, 3, 4, 5, 6, 7};
#pragma unroll
    for (int n = 0; n < 4; ++n) { kf[ord[n]][0] = lds128(adK0 + 512 * ord[n]); kf[ord[n]][1] = lds128(adK1 + 512 * ord[n]); }
    SB();
#pragma unroll
    for (int i = 5; i < 8; ++i)
      if (i < 7 || w < 4) dma_sc1(ws + kvsrc[i], lane * 16, kvdst[i]);
    if (h == 1 && hh == 0) { qf[0][0] = 0; qf[0][1] = 0; }
    if (h == 0) { qf[1][2] = 0; qf[1][3] = 0; }
    if (hh == 1) qf[1] = u32x4{0x3f80u, 0, 0, 0};
    SB();
    f32x16 sc[9];
    float m4[4];
    auto qk = [&](int i, int st) { sc[i] = mfma32(kf[i][st], qf[st], st == 0 ? zero16 : sc[i]); };
    qk(0, 0); qk(8, 0); qk(0, 1); qk(8, 1);
    SB();
#pragma unroll
    for (int n = 2; n < 11; ++n) {
      if (n < 9) { qk(ord[n], 0); SB(); }
      if (n + 2 < 9) { kf[ord[n + 2]][0] = lds128(adK0 + 512 * ord[n + 2]); SB(); }
      if (n == 3) {
#pragma unroll
        for (int r = 6; r < 16; ++r) sc[0][r] = ((r & 3) + 8 * (r >> 2) + 4 * hh <= l32) ? sc[8][r] : sc[0][r];
      } else if (n > 3) {
#pragma unroll
        for (int c = 0; c < 4; ++c) m4[c] = max3(m4[c], sc[n - 3][2 * c], sc[n - 3][2 * c + 1]);
      }
      SB();
      if (n < 9) { qk(ord[n], 1); SB(); }
      if (n + 2 < 9) { kf[ord[n + 2]][1] = lds128(adK1 + 512 * ord[n + 2]); SB(); }
      if (n == 2) {
#pragma unroll
        for (int r = 0; r < 6; ++r) sc[0][r] = ((r & 3) + 8 * (r >> 2) + 4 * hh <= l32) ? sc[8][r] : sc[0][r];
      } else if (n == 3) {
#pragma unroll
        for (int c = 0; c < 4; ++c) m4[c] = max3(max3(sc[0][4 * c], sc[0][4 * c + 1], sc[0][4 * c + 2]), sc[0][4 * c + 3], sc[0][4 * c + 3]);
      } else {
#pragma unroll
        for (int c = 0; c < 4; ++c) m4[c] = max3(m4[c], sc[n - 3][8 + 2 * c], sc[n - 3][9 + 2 * c]);
      }
      SB();
    }
    float mx = __builtin_fmaxf(max3(m4[0], m4[1], m4[2]), m4[3]);
    { auto sw = __builtin_amdgcn_permlane32_swap(__float_as_uint(mx), __float_as_uint(mx), false, false); mx = __builtin_fmaxf(__uint_as_float(sw[0]), __uint_as_float(sw[1])); }
    u32x4 nmf;
    {
      const float nmx = -mx;
      const u32 p0 = __float_as_uint(nmx) & 0xffff0000u;
      const float r1 = nmx - __uint_as_float(p0);
      const u32 p1 = __float_as_uint(r1) & 0xffff0000u;
      const u32 p2 = __float_as_uint(r1 - __uint_as_float(p1));
      nmf = u32x4{hh ? p2 >> 16 : (p0 >> 16) | p1, 0, 0, 0};
    }
    auto submax = [&](int i) { sc[i] = mfma32(ones8, nmf, sc[i]); };
    submax(0); submax(1);
    SB();
    u32x4 rt0, rt1;
    if (j < 12 && w < 5) { rt0 = ldstr2(rtl[0], rtl[0] + 5120); rt1 = ldstr2(rtl[1], rtl[1] + 5120); }
    vm_wait<0>();
    bar();
    STAMP(26);
    if (w >= 4) {
#pragma unroll
      for (int t = 0; t < 5; ++t) gld64(xr[t], xa + (64 * half + 16 * (w - 4)) * 1280 + slice * 160 + t * 32, r16 * 1280 + g * 8);
      asm volatile("global_load_dwordx2 %0, %1, %2 sc1" : "=v"(xr[5]) : "v"(r16 * 16), "s"(ws + WS_RS + (p * 128 + 64 * half + 16 * (w - 4)) * 16));
    } else {
#pragma unroll
      for (int t = 0; t < 6; ++t) asm volatile("v_mov_b64 %0, 0" : "=v"(xr[t]));
    }
    if (j < 12 && w < 5) { st128_sc1(rs, rtg[0], rt0); st128_sc1(rs, rtg[1], rt1); }
    warm(((w & 1) ? wu : wg) + (size_t)(rr * 20 + (w >> 1) * 5) * 3072 + (xcd & 3) * 768, voffwarm, L_WARM);
    SB();
    f32x16 o = zero16;
    u32 pw[2][8];
    u32x2 vf[2][4], vx[4];
    auto rdV = [&](int i, int e) { return ldstr(adV + 512 * i + 128 * e); };
    auto pfrag = [&](const u32* q, int st) { return u32x4{q[4 * st], q[4 * st + 1], q[4 * st + 2], q[4 * st + 3]}; };
    u32 p8[8];
#pragma unroll
    for (int n = 0; n < 8; ++n) {
      float ex[16];
#pragma unroll
      for (int e = 0; e < 8; ++e) {
        ex[2 * e] = __builtin_amdgcn_exp2f(sc[n][2 * e]); SB();
        if (e < 4) { vf[n & 1][e] = rdV(n, e); SB(); }
        else if (n == 0) { vx[e - 4] = rdV(8, e - 4); SB(); }
        ex[2 * e + 1] = __builtin_amdgcn_exp2f(sc[n][2 * e + 1]); SB();
        if (e > 0) { pw[n & 1][e - 1] = pack2(ex[2 * e - 2], ex[2 * e - 1]); SB(); }
        if (n >= 1) {
          if (e == 0 && n < 7) { submax(n + 1); SB(); }
          if (e == 3) { o = mfma32(cat(vf[(n - 1) & 1][0], vf[(n - 1) & 1][1]), pfrag(pw[(n - 1) & 1], 0), o); SB(); }
          if (e == 6) { o = mfma32(cat(vf[(n - 1) & 1][2], vf[(n - 1) & 1][3]), pfrag(pw[(n - 1) & 1], 1), o); SB(); }
          if (n == 2 && e == 2) { o = mfma32(cat(vx[0], vx[1]), pfrag(p8, 0), o); SB(); }
          if (n == 2 && e == 5) { o = mfma32(cat(vx[2], vx[3]), pfrag(p8, 1), o); SB(); }
        }
      }
      pw[n & 1][7] = pack2(ex[14], ex[15]);
      if (n == 0) {
#pragma unroll
        for (int m = 0; m < 8; ++m) { p8[m] = pw[0][m] & dm[m]; pw[0][m] &= ~dm[m]; }
      }
      SB();
      if (n == 3) STAMP(27);
    }
    o = mfma32(cat(vf[1][0], vf[1][1]), pfrag(pw[1], 0), o);
    o = mfma32(cat(vf[1][2], vf[1][3]), pfrag(pw[1], 1), o);
    const float inv = __builtin_amdgcn_rcpf(o[12]);
    const u32 x0 = pack2(o[0] * inv, o[1] * inv), x1 = pack2(o[2] * inv, o[3] * inv), y0 = pack2(o[4] * inv, o[5] * inv), y1 = pack2(o[6] * inv, o[7] * inv);
    auto s0 = __builtin_amdgcn_permlane32_swap(x0, y0, false, false);
    auto s1 = __builtin_amdgcn_permlane32_swap(x1, y1, false, false);
    const u32 ob = WS_O + (pa * 80 + 5 * ja) * 2048 + (32 * qt + l32) * 16;
    st128_sc1(rs, ob + (3 * h + hh) * 2048, u32x4{s0[0], s1[0], s0[1], s1[1]});
    if (hh == 0) st64_sc1(rs, ob + 2 * 2048 + 8 * h, u32x2{pack2(o[8] * inv, o[9] * inv), pack2(o[10] * inv, o[11] * inv)});
    STAMP(5);
  }
  const char* gsrc = ((w & 1) ? wu : wg) + jg * 192;
  auto stageGBp = [&](int c, int rb, int i) {
    dma(gsrc + (size_t)(c * 64 + rb * 16) * 3072, voffG[i], gb_off(c) + (w & 1) * 12288 + rb * 3072 + i * 1024);
  };
  auto stageGB = [&](int c, int rb) { stageGBp(c, rb, 0); stageGBp(c, rb, 1); stageGBp(c, rb, 2); };
  const int wm4 = w & 3, wk = w >> 2;
  u32 voffR = g * 2048 + (64 * half + 16 * wm4 + r16) * 16;
  fl_get = flag_off(1, p, r16);
  u32 fl_get2 = flag_off(2, pg, r16);
  asm volatile("" : "+v"(voffR), "+v"(fl_get), "+v"(fl_get2));
  asm volatile("" ::: "memory");
  if (w < 4) {
#pragma unroll
    for (int i = 0; i < 20; ++i) dma_sc1(wt + 20480 * (1 + w) + i * 1024, lane * 16, L_WOB + 20480 * (1 + w) + i * 1024);
  }
  bar();
  if (w < 4) {
#pragma unroll
    for (int i = 0; i < 5; ++i) dma_sc1(wt + 5120 * w + i * 1024, lane * 16, L_WOB + 5120 * w + i * 1024);
    dma_sc1(ws + WS_WB + (w < 3 ? w : 2) * 1024, lane * 16, L_WB + (w < 3 ? w : 2) * 1024);
    vm_wait<6>();
  } else {
    stageGB(2, w >> 1); stageGB(2, (w >> 1) - 2);
    vm_wait<6>();
  }
  bar0();
  STAMP(6);
  u32x2 hp[5];
  {
    if (w == PW && lane < 2) __hip_atomic_store((GAS u64*)(ws + fl_put), tag, RLX_AGENT);
    u32x4 a[10], b[6][5];
    const u32 ad0 = L_WOB + wk * 10 * 5120 + lane * 16, adm = L_WB + (r16 & 1) * 1280 + wk * 640 + g * 16;
    auto rdB = [&](int s, int t) { b[s % 6][t] = lds128(ad0 + ((s + 4) % 10) * 5120 + t * 1024); };
#pragma unroll
    for (int s = 0; s < 6; ++s)
#pragma unroll
      for (int t = 0; t < 5; ++t) rdB(s, t);
    asm volatile("" : "+v"(xr[0]), "+v"(xr[1]), "+v"(xr[2]), "+v"(xr[3]), "+v"(xr[4]), "+v"(xr[5]));
    f32x4 hacc[5], macc;
    { const u32 xm = xr[5][0]; macc = f32x4{__uint_as_float(xm), 0.f, 0.f, 0.f}; }
#pragma unroll
    for (int t = 0; t < 5; ++t) { const u32 a0 = xr[t][0], a1 = xr[t][1]; hacc[t] = f32x4{bflo(a0), bfhi(a0), bflo(a1), bfhi(a1)}; }
    SB();
    if (w == PW) poll_flags(ws, fl_get, tag); else if (w < 4) vm_wait<0>();
    bar();
    STAMP(7);
    const char* osrc = ws + WS_O + (p * 80 + 40 * wk) * 2048;
#pragma unroll
    for (int s = 0; s < 10; ++s) gld_sc1(a[s], osrc + ((s + 4) % 10) * 8192, voffR);
    u32x4 bm[2];
    bm[0] = lds128(adm + 4 * 64);
    SB();
#pragma unroll
    for (int s = 0; s < 10; ++s) {
      switch (s) {
        case 0: vm_wait_use<9>(a[0]); break; case 1: vm_wait_use<8>(a[1]); break; case 2: vm_wait_use<7>(a[2]); break;
        case 3: vm_wait_use<6>(a[3]); break; case 4: vm_wait_use<5>(a[4]); break; case 5: vm_wait_use<4>(a[5]); break;
        case 6: vm_wait_use<3>(a[6]); break; case 7: vm_wait_use<2>(a[7]); break; case 8: vm_wait_use<1>(a[8]); break;
        default: vm_wait_use<0>(a[9]); break;
      }
      if (s == 0) STAMP(28);
#pragma unroll
      for (int t = 0; t < 5; ++t) {
        hacc[t] = mfma(b[s % 6][t], a[s], hacc[t]);
        SB();
        if (s + 6 < 10) { rdB(s + 6, t); SB(); }
        if (t == 0 && s < 9) { bm[(s + 1) & 1] = lds128(adm + ((s + 5) % 10) * 64); SB(); }
      }
      macc = mfma(bm[s & 1], a[s], macc);
      SB();
    }
    STAMP(8);
    if (wk == 0) {
#pragma unroll
      for (int t = 0; t < 5; ++t) *(LAS f32x4*)(smem + L_RED + (wm4 * 5 + t) * 1024 + lane * 16) = hacc[t];
      *(LAS float*)(smem + L_RED + 20480 + wm4 * 256 + lane * 4) = macc[0] + macc[1];
    }
    bar();
    if (wk == 1) {
      float s1 = 0.f, s2 = 0.f;
      const float mean = macc[0] + macc[1] + *(LAS const float*)(smem + L_RED + 20480 + wm4 * 256 + lane * 4);
      u32x2 hc[5];
#pragma unroll
      for (int t = 0; t < 5; ++t) {
        const f32x4 v = hacc[t] + *(LAS const f32x4*)(smem + L_RED + (wm4 * 5 + t) * 1024 + lane * 16);
        const u32 lo = pack2(v[0], v[1]), hi = pack2(v[2], v[3]);
        hp[t] = u32x2{lo, hi};
        hc[t] = u32x2{pack2(bflo(lo) - mean, bfhi(lo) - mean), pack2(bflo(hi) - mean, bfhi(hi) - mean)};
      }
      const u32 hb = WS_H + (p * 80 + 10 * slice + 2 * (g & 1) + (g >> 1)) * 2048 + (64 * half + 16 * wm4 + r16) * 16;
      st128_sc1(rs, hb, quad2chunk(hc[0], hc[1]));
      st128_sc1(rs, hb + 4 * 2048, quad2chunk(hc[2], hc[3]));
      const u32x4 last = quad2chunk(hc[4], hc[4]);
      if ((g & 1) == 0) st128_sc1(rs, hb + 8 * 2048, last);
#pragma unroll
      for (int t = 0; t < 5; ++t) { const u32 lo = hc[t][0], hi = hc[t][1]; s1 = dot2(hi, ONES2, dot2(lo, ONES2, s1)); s2 = dot2(hi, hi, dot2(lo, lo, s2)); }
      s1 = gsum(s1); s2 = gsum(s2);
      if (g == 0) st64_sc1(rs, WS_ST + (p * 8 + slice) * 1024 + (64 * half + 16 * wm4 + r16) * 8, u32x2{__float_as_uint(s1), __float_as_uint(s2)});
      vm_wait<0>();
    } else {
#pragma unroll
      for (int t = 0; t < 5; ++t) hp[t] = u32x2{0, 0};
      stageGB(0, w >> 1); stageGB(0, 2 + (w >> 1)); stageGB(1, w >> 1); stageGB(1, 2 + (w >> 1));
    }
  }
  bar();
  STAMP(9);
  if (w == PW) {
    if (lane == 0) __hip_atomic_store((GAS u64*)(ws + flag_off(2, p, j)), tag, RLX_AGENT);
    poll_flags(ws, fl_get2, tag);
  } else if (w == 2) {
    GAS u64* a = (GAS u64*)(ws + WS_DENSE + lane * 8);
    for (u32 spins = 0; spins < SPIN_CAP; ++spins) {
      const u64 v0 = __hip_atomic_load(a, RLX_AGENT), v1 = __hip_atomic_load(a + 64, RLX_AGENT), v2 = __hip_atomic_load(a + 128, RLX_AGENT), v3 = __hip_atomic_load(a + 192, RLX_AGENT);
      if (__all(v0 == tag && v1 == tag && v2 == tag && v3 == tag)) break;
    }
    if (bx == 0 && lane == 0) __hip_atomic_store((GAS u64*)(ws + WS_EPOCH), tag, RLX_AGENT);
  } else if (wk == 1) {
    stageGB(3, w >> 1); stageGB(3, (w >> 1) - 2);
  }
  bar();
  STAMP(10);

  const int rotd = (((slice * 6) >> 3) + ((3 * (rr >> 3)) >> 1)) % 6;
  auto stageDp = [&](int c, int i) {
    int kc = c + rotd; kc = kc >= 6 ? kc - 6 : kc;
    dma_sc1(wt + (20 + kc * 8 + w) * 5120 + i * 1024, lane * 16, (c & 3) * 40960 + w * 5120 + i * 1024);
  };
  auto stageD = [&](int c) {
#pragma unroll
    for (int i = 0; i < 5; ++i) stageDp(c, i);
  };

  {
    const int wm = w >> 2, wn = w & 3;
    const char* hsrc = ws + WS_H + (pg * 80 + w) * 2048;
    auto stageGAp = [&](int c, int i) { dma_sc1(hsrc + c * 16384 + i * 1024, lane * 16, ga_off(c) + w * 2048 + i * 1024); };
    auto stageGA = [&](int c) { stageGAp(c, 0); stageGAp(c, 1); };
    stageGA(0);
    dma_sc1(ws + WS_ST + (pg * 8 + w) * 1024, lane * 16, L_ST + w * 1024);
    stageGA(1);
    const u32 adA = g * 2048 + (64 * wm + r16) * 16;
    u32 adB[3];
#pragma unroll
    for (int t = 0; t < 3; ++t) {
      const int mat = t == 0 ? 0 : (t == 1 ? 1 : (pq >> 1)), c = t < 2 ? 3 * wn + (pq >> 1) : 3 * wn + 2;
      int cp = c + mat + 2 * (g & 1); cp = cp >= 12 ? cp - 12 : cp;
      adB[t] = mat * 12288 + (8 * g + q4) * 192 + cp * 16 + (pq & 1) * 8;
    }
    f32x4 acc[4][3];
#pragma unroll
    for (int m = 0; m < 4; ++m)
#pragma unroll
      for (int t = 0; t < 3; ++t) acc[m][t] = f32x4{0, 0, 0, 0};
    u32x4 fa[2][4]; u32x2 fb[2][3][2];
    auto rdp = [&](int c, int s, int i) {
      const u32 sa = ga_off(c) + s * 8192, sb = gb_off(c) + s * 6144;
      if (i < 2) fb[s][0][i] = ldstr(sb + adB[0] + i * 768);
      else if (i == 2) fa[s][0] = lds128(sa + adA);
      else if (i < 7) fb[s][1 + ((i - 3) >> 1)][(i - 3) & 1] = ldstr(sb + adB[1 + ((i - 3) >> 1)] + ((i - 3) & 1) * 768);
      else if (i < 10) fa[s][i - 6] = lds128(sa + adA + (i - 6) * 256);
    };
    auto mm = [&](int s, int c) {
      u32x4 b[3];
#pragma unroll
      for (int t = 0; t < 3; ++t) b[t] = cat(fb[s][t][0], fb[s][t][1]);
      const int cr = s == 0 ? c : c + 1;
#pragma unroll
      for (int i = 0; i < 12; ++i) {
        acc[i / 3][i % 3] = mfma(b[i % 3], fa[s][i / 3], acc[i / 3][i % 3]);
        if (cr < 10) rdp(cr, s ^ 1, i);
        if (s == 1 && (i == 4 || i == 10) && c + 3 < 10) stageGAp(c + 3, i == 10);
        if (s == 0 && i % 4 == 3 && c >= 1 && c + 3 < 10) stageGBp(c + 3, w >> 1, i >> 2);
        if (s == 1 && (c == 7 || c == 8) && i % 2 == 1 && i < 10) stageDp(c - 7, i >> 1);
        SB();
      }
    };
    vm_wait<3>();
    bar();
    STAMP(19);
#pragma unroll
    for (int i = 0; i < 10; ++i) rdp(0, 0, i);
    SB();
    stageGA(2);
    SB();
#pragma unroll
    for (int c = 0; c < 10; ++c) {
      if (c == 1) {
        const f32x2 u0 = *(LAS const f32x2*)(smem + L_ST + (2 * g) * 1024 + (16 * w + r16) * 8), u1 = *(LAS const f32x2*)(smem + L_ST + (2 * g + 1) * 1024 + (16 * w + r16) * 8);
        const float s1 = gsum(u0[0] + u1[0]), s2 = gsum(u0[1] + u1[1]);
        const float mean = s1 * (1.f / 640.f), var = __builtin_fmaxf(s2 * (1.f / 640.f) - mean * mean, 0.f);
        if (g == 0) *(LAS float*)(smem + L_MISC + (16 * w + r16) * 4) = __builtin_amdgcn_rcpf(var + LN_EPS);
      }
      mm(0, c);
      if (c < 9) {
        if (c == 0) vm_wait<2>(); else if (c == 1) vm_wait<5>(); else if (c <= 6) vm_wait<8>(); else vm_wait<5>();
        bar();
        if (c == 1) STAMP(29);
        if (c == 4) STAMP(20);
        if (c == 7) STAMP(30);
      }
      mm(1, c);
    }
    STAMP(11);
    u32x2 p01[4];
    float r2[4];
#pragma unroll
    for (int m = 0; m < 4; ++m) {
      r2[m] = *(LAS const float*)(smem + L_MISC + (64 * wm + 16 * m + r16) * 4);
      const f32x4 v = acc[m][0] * acc[m][1] * r2[m];
      p01[m] = u32x2{pack2(v[0], v[1]), pack2(v[2], v[3])};
    }
    const u32 hb = WS_HH + (pg * 192 + 12 * jg + 3 * wn) * 2048 + (64 * wm + r16) * 16;
    st128_sc1(rs, hb + (g >> 1) * 2048 + (g & 1) * 256, quad2chunk(p01[0], p01[1]));
    st128_sc1(rs, hb + (g >> 1) * 2048 + (2 + (g & 1)) * 256, quad2chunk(p01[2], p01[3]));
    u32x2 p2[2];
#pragma unroll
    for (int mm2 = 0; mm2 < 2; ++mm2) {
      f32x4 v;
#pragma unroll
      for (int r = 0; r < 4; ++r) {
        auto sw = __builtin_amdgcn_permlane32_swap(__float_as_uint(acc[2 * mm2][2][r]), __float_as_uint(acc[2 * mm2 + 1][2][r]), false, false);
        v[r] = __uint_as_float(sw[0]) * __uint_as_float(sw[1]) * (lane < 32 ? r2[2 * mm2] : r2[2 * mm2 + 1]);
      }
      p2[mm2] = u32x2{pack2(v[0], v[1]), pack2(v[2], v[3])};
    }
    st128_sc1(rs, hb + 2 * 2048 + (2 * (g & 1) + (g >> 1)) * 256, quad2chunk(p2[0], p2[1]));
    STAMP(31);
    vm_wait<8>();
    bar();
    stageD(2); stageD(3);
  }

  {
    u32x4 b[6][5];
    const u32 ad0 = wk * 4 * 5120 + lane * 16;
    auto rdB = [&](int s, int t) { b[s % 6][t] = lds128(ad0 + ((s >> 2) & 3) * 40960 + (s & 3) * 5120 + t * 1024); };
#pragma unroll
    for (int s = 0; s < 4; ++s)
#pragma unroll
      for (int t = 0; t < 5; ++t) rdB(s, t);
    SB();
    vm_wait<10>();
    bar();
    STAMP(12);
    if (w == PW && lane == 0) __hip_atomic_store((GAS u64*)(ws + flag_off(3, pg, jg)), tag, RLX_AGENT);
#pragma unroll
    for (int s = 4; s < 6; ++s)
#pragma unroll
      for (int t = 0; t < 5; ++t) rdB(s, t);
    f32x4 acc[5];
#pragma unroll
    for (int t = 0; t < 5; ++t) { const u32 a = hp[t][0], b2 = hp[t][1]; acc[t] = f32x4{bflo(a), bfhi(a), bflo(b2), bfhi(b2)}; }
    SB();
    if (w == PW) poll_flags(ws, fl_get + 2 * 65536, tag); else vm_wait<5>();
    bar();
    STAMP(13);
    u32x4 a[6][4];
    const char* asrc = ws + WS_HH + (p * 192 + 16 * wk) * 2048;
    auto loadAp = [&](int c, int s) {
      int kc = c + rotd; kc = kc >= 6 ? kc - 6 : kc;
      gld_sc1(a[c][s], asrc + kc * 65536 + s * 8192, voffR);
    };
    auto issue = [&](int n) {
      if (n < 4) loadAp(2, n); else if (n < 9) stageDp(4, n - 4); else if (n < 13) loadAp(3, n - 9); else if (n < 18) stageDp(5, n - 13);
      else if (n < 22) loadAp(4, n - 18); else if (n < 26) loadAp(5, n - 22);
    };
#pragma unroll
    for (int s = 0; s < 4; ++s) loadAp(0, s);
#pragma unroll
    for (int s = 0; s < 4; ++s) loadAp(1, s);
#pragma unroll
    for (int s = 0; s < 24; ++s) {
      if (s == 0) vm_wait<4>();
      if (s == 4) vm_wait<8>();
      if (s == 8) vm_wait<12>();
      if (s == 10) vm_wait<11>();
      if (s == 12) vm_wait<11>();
      if (s == 14) vm_wait<8>();
      if (s == 16) vm_wait<4>();
      if (s == 20) vm_wait<0>();
      if (s % 4 == 0) asm volatile("" : "+v"(a[s >> 2][0]), "+v"(a[s >> 2][1]), "+v"(a[s >> 2][2]), "+v"(a[s >> 2][3]));
      if (s == 6) bar();
      if (s == 10 || s == 14) bar0();
      if (s == 0) STAMP(21);
      if (s == 4) STAMP(22); if (s == 8) STAMP(23); if (s == 12) STAMP(24); if (s == 16) STAMP(25);
#pragma unroll
      for (int t = 0; t < 5; ++t) {
        acc[t] = mfma(b[s % 6][t], a[s >> 2][s & 3], acc[t]);
        SB();
        if (s + 6 < 24) { rdB(s + 6, t); SB(); }
        if (t == 1) { issue(2 * s); SB(); }
        if (t == 4) { issue(2 * s + 1); SB(); }
      }
    }
    STAMP(14);
    constexpr u32 L_RED2 = 2 * 40960;
    if (wk == 0) {
#pragma unroll
      for (int t = 0; t < 5; ++t) *(LAS f32x4*)(smem + L_RED2 + (wm4 * 5 + t) * 1024 + lane * 16) = acc[t];
    }
    bar();
    if (wk == 1) {
      u32x2 op[5];
#pragma unroll
      for (int t = 0; t < 5; ++t) {
        const f32x4 v = acc[t] + *(LAS const f32x4*)(smem + L_RED2 + (wm4 * 5 + t) * 1024 + lane * 16);
        op[t] = u32x2{pack2(v[0], v[1]), pack2(v[2], v[3])};
      }
      const __amdgpu_buffer_rsrc_t ro = __builtin_amdgcn_make_buffer_rsrc(out, 0, 2048 * 1280, 0x00020000);
      const u32 oo = (128 * p + 64 * half + 16 * wm4 + r16) * 1280 + slice * 160 + (2 * (g & 1) + (g >> 1)) * 16;
      __builtin_amdgcn_raw_buffer_store_b128(quad2chunk(op[0], op[1]), ro, (int)oo, 0, OUT_AUX);
      __builtin_amdgcn_raw_buffer_store_b128(quad2chunk(op[2], op[3]), ro, (int)oo + 64, 0, OUT_AUX);
      const u32x4 last = quad2chunk(op[4], op[4]);
      if ((g & 1) == 0) __builtin_amdgcn_raw_buffer_store_b128(last, ro, (int)oo + 128, 0, OUT_AUX);
    }
    STAMP(15);
  }
}

extern "C" void kernel_launch(void* const* d_in, const int* in_sizes, int n_in, void* d_out, int out_size, void* d_ws, size_t ws_size, hipStream_t stream) {
  (void)in_sizes; (void)n_in; (void)out_size; (void)ws_size;
  (void)hipFuncSetAttribute((const void*)fused_block, hipFuncAttributeMaxDynamicSharedMemorySize, LDS_BYTES);
  fused_block<<<256, 512, LDS_BYTES, stream>>>((const char*)d_in[0], (const char*)d_in[1], (const char*)d_in[2], (const char*)d_in[4], (const char*)d_in[5],
                                               (const char*)d_in[6], (char*)d_out, (char*)d_ws);
}
```

```cpp
#include <hip/hip_runtime.h>
#include <stdint.h>

typedef unsigned short u16;
typedef unsigned int u32;
typedef unsigned long long u64;
typedef __bf16 bf16x8 __attribute__((ext_vector_type(8)));
typedef __bf16 bf16x2 __attribute__((ext_vector_type(2)));
typedef short s16x4 __attribute__((ext_vector_type(4)));
typedef float f32x4 __attribute__((ext_vector_type(4)));
typedef float f32x2 __attribute__((ext_vector_type(2)));
typedef u32 u32x4 __attribute__((ext_vector_type(4)));
typedef u32 u32x2 __attribute__((ext_vector_type(2)));
#define LAS __attribute__((address_space(3)))
#define GAS __attribute__((address_space(1)))
#define DI __device__ __forceinline__
#define RLX_AGENT __ATOMIC_RELAXED, __HIP_MEMORY_SCOPE_AGENT

#define RG 5
#define OUT_AUX 0
#define HOLD_AT 15
#define ATT_PRIO 0
#define STAMPS 0

constexpr u32 WS_EPOCH = 0;
constexpr u32 WS_DENSE = 2048;
constexpr u32 WS_FLAG = 4096;
constexpr u32 WS_QKV = WS_FLAG + 4 * 256 * 256;
constexpr u32 WS_O = WS_QKV + 16 * 240 * 2048;
constexpr u32 WS_H = WS_O + 16 * 80 * 2048;
constexpr u32 WS_HH = WS_H + 16 * 80 * 2048;
constexpr u32 WS_ST = WS_HH + 16 * 192 * 2048;
constexpr u32 WS_RS = WS_ST + 16 * 8 * 1024;
constexpr u32 WS_WB = WS_RS + 2048 * 16;
constexpr u32 WT_SLICE = 68 * 5120;
constexpr u32 WS_WT = WS_WB + 3072;
constexpr u32 WS_END = WS_WT + 8 * WT_SLICE;
constexpr u32 WS_STAMP = WS_END;
constexpr u64 TAG_STEP = 0x9E3779B97F4A7C15ull;

constexpr u32 LDS_BYTES = 163840;
constexpr u32 L_MISC = 161792;
constexpr u32 L_K = 0, KCH = 6144;
constexpr u32 L_V = 6 * KCH, VCH = 6208;
constexpr u32 L_WOB = L_MISC - 102400;
__device__ constexpr u32 gb_off(int c) { return (c & 3) == 2 ? 0 : ((c & 3) == 3 ? 24576 : ((c & 3) == 0 ? 65536 : 106496)); }
__device__ constexpr u32 ga_off(int c) { return c % 3 == 1 ? 49152 : (c % 3 == 2 ? 90112 : 131072); }
constexpr u32 L_ST = 147456;
constexpr u32 L_WB = 49152;
constexpr u32 L_RED = 24576;
constexpr u32 L_WARM = L_MISC + 1792;
constexpr u32 L_RT = 131072;
constexpr u32 SPIN_CAP = 1u << 15;
constexpr float LN_EPS = 1e-5f;

extern __shared__ __attribute__((aligned(16))) char smem[];

DI u32x4 lds128(u32 off) { return *(LAS const u32x4*)(smem + off); }
DI u32x2 ldstr(u32 off) { return __builtin_bit_cast(u32x2, __builtin_amdgcn_ds_read_tr16_b64_v4i16((LAS s16x4*)(smem + off))); }
DI u32x4 ldstr2(u32 lo, u32 hi) { const u32x2 a = ldstr(lo), b = ldstr(hi); return u32x4{a[0], a[1], b[0], b[1]}; }
DI f32x4 mfma(u32x4 a, u32x4 b, f32x4 c) { return __builtin_amdgcn_mfma_f32_16x16x32_bf16(__builtin_bit_cast(bf16x8, a), __builtin_bit_cast(bf16x8, b), c, 0, 0, 0); }
typedef float f32x16 __attribute__((ext_vector_type(16)));
DI f32x16 mfma32(u32x4 a, u32x4 b, f32x16 c) { return __builtin_amdgcn_mfma_f32_32x32x16_bf16(__builtin_bit_cast(bf16x8, a), __builtin_bit_cast(bf16x8, b), c, 0, 0, 0); }
DI float max3(float a, float b, float c) { return __builtin_fmaxf(__builtin_fmaxf(a, b), c); }
DI f32x4 mfma16(u32x2 a, u32x2 b, f32x4 c) { return __builtin_amdgcn_mfma_f32_16x16x16bf16_1k(__builtin_bit_cast(s16x4, a), __builtin_bit_cast(s16x4, b), c, 0, 0, 0); }
DI float dot2(u32 a, u32 b, float c) { return __builtin_amdgcn_fdot2_f32_bf16(__builtin_bit_cast(bf16x2, a), __builtin_bit_cast(bf16x2, b), c, false); }
DI u32 pack2(float lo, float hi) { const f32x2 v = {lo, hi}; return __builtin_bit_cast(u32, __builtin_convertvector(v, bf16x2)); }
DI float bflo(u32 v) { return __uint_as_float(v << 16); }
DI float bfhi(u32 v) { return __uint_as_float(v & 0xffff0000u); }
constexpr u32 ONES2 = 0x3f803f80u;
DI float sum8(u32x4 a, float s) { const u32 a0 = a[0], a1 = a[1], a2 = a[2], a3 = a[3]; return dot2(a3, ONES2, dot2(a2, ONES2, dot2(a1, ONES2, dot2(a0, ONES2, s)))); }
DI float sq8(u32x4 a, float s) { const u32 a0 = a[0], a1 = a[1], a2 = a[2], a3 = a[3]; return dot2(a3, a3, dot2(a2, a2, dot2(a1, a1, dot2(a0, a0, s)))); }
DI float gsum(float x) {
  auto a = __builtin_amdgcn_permlane16_swap(__float_as_uint(x), __float_as_uint(x), false, false);
  x = __uint_as_float(a[0]) + __uint_as_float(a[1]);
  auto b = __builtin_amdgcn_permlane32_swap(__float_as_uint(x), __float_as_uint(x), false, false);
  return __uint_as_float(b[0]) + __uint_as_float(b[1]);
}
DI float gmax(float x) {
  auto a = __builtin_amdgcn_permlane16_swap(__float_as_uint(x), __float_as_uint(x), false, false);
  x = __builtin_fmaxf(__uint_as_float(a[0]), __uint_as_float(a[1]));
  auto b = __builtin_amdgcn_permlane32_swap(__float_as_uint(x), __float_as_uint(x), false, false);
  return __builtin_fmaxf(__uint_as_float(b[0]), __uint_as_float(b[1]));
}
DI u32x4 quad2chunk(u32x2 A, u32x2 B) {
  const u32 a0 = A[0], a1 = A[1], b0 = B[0], b1 = B[1];
  auto s0 = __builtin_amdgcn_permlane16_swap(a0, b0, false, false);
  auto s1 = __builtin_amdgcn_permlane16_swap(a1, b1, false, false);
  return u32x4{s0[0], s1[0], s0[1], s1[1]};
}
DI void dma(const void* sbase, u32 voff, u32 lds) {
  asm volatile("s_mov_b32 m0, %2\n\ts_nop 0\n\tglobal_load_lds_dwordx4 %0, %1" ::"v"(voff), "s"(sbase), "s"(lds));
}
DI void dma_sc1(const void* sbase, u32 voff, u32 lds) {
  asm volatile("s_mov_b32 m0, %2\n\ts_nop 0\n\tglobal_load_lds_dwordx4 %0, %1 sc1" ::"v"(voff), "s"(sbase), "s"(lds));
}
DI void gld_sc1(u32x4& dst, const void* sbase, u32 voff) {
  asm volatile("global_load_dwordx4 %0, %1, %2 sc1" : "=v"(dst) : "v"(voff), "s"(sbase));
}
DI void gld(u32x4& dst, const void* sbase, u32 voff) {
  asm volatile("global_load_dwordx4 %0, %1, %2" : "=v"(dst) : "v"(voff), "s"(sbase));
}
DI void gld64(u32x2& dst, const void* sbase, u32 voff) {
  asm volatile("global_load_dwordx2 %0, %1, %2" : "=v"(dst) : "v"(voff), "s"(sbase));
}
template <int N> DI void vm_wait() { asm volatile("s_waitcnt vmcnt(%0)" ::"n"(N) : "memory"); }
template <int N> DI void vm_wait_use(u32x4& r) { asm volatile("s_waitcnt vmcnt(%1)" : "+v"(r) : "n"(N)); }
#define SB() __builtin_amdgcn_sched_barrier(0)
DI void warm(const void* sbase, u32 voff, u32 lds) {
  asm volatile("s_mov_b32 m0, %2\n\ts_nop 0\n\tglobal_load_lds_dword %0, %1" ::"v"(voff), "s"(sbase), "s"(lds));
}
DI void bar0() { asm volatile("s_barrier" ::: "memory"); }
DI u32x4 cat(u32x2 a, u32x2 b) { return u32x4{a[0], a[1], b[0], b[1]}; }
DI void bar() { asm volatile("s_waitcnt lgkmcnt(0)\n\ts_barrier" ::: "memory"); }
DI void st128_sc1(__amdgpu_buffer_rsrc_t rs, u32 off, u32x4 v) { __builtin_amdgcn_raw_buffer_store_b128(v, rs, (int)off, 0, 16); }
DI void st64_sc1(__amdgpu_buffer_rsrc_t rs, u32 off, u32x2 v) { __builtin_amdgcn_raw_buffer_store_b64(v, rs, (int)off, 0, 16); }

DI void poll_flags(const char* ws, u32 off, u64 tag) {
  GAS u64* a = (GAS u64*)(ws + off);
  for (u32 spins = 0; spins < SPIN_CAP; ++spins) {
    const u64 v = __hip_atomic_load(a, RLX_AGENT);
    if (__all(v == tag)) break;
  }
  __builtin_amdgcn_fence(__ATOMIC_ACQUIRE, "wavefront");
}
DI u32 flag_off(int phase, int p, int j) { return WS_FLAG + (u32)((phase * 256 + p * 16 + j) * 256); }

#define STAMP(i) do { if ((i) == 0) hold0 = __builtin_amdgcn_s_memrealtime(); if ((i) == HOLD_AT) { while (__builtin_amdgcn_s_memrealtime() - hold0 < 5000) __builtin_amdgcn_s_sleep(2); } } while (0)

__global__ __launch_bounds__(512, 2) void fused_block(const char* __restrict__ x, const char* __restrict__ wqkv, const char* __restrict__ wo,
                                                      const char* __restrict__ wg, const char* __restrict__ wu, const char* __restrict__ wd,
                                                      char* __restrict__ out, char* ws) {
  const int tid = threadIdx.x, lane = tid & 63, w = __builtin_amdgcn_readfirstlane(tid >> 6);
  const int r16 = lane & 15, g = lane >> 4, q4 = r16 >> 2, pq = r16 & 3;
  const int bx = blockIdx.x, xcd = bx & 7, rr = bx >> 3;
  const int p = (xcd >> 1) * 4 + (rr >> 3), j = (xcd & 1) * 8 + (rr & 7);
  const int pa = rr >> 1, ja = 2 * xcd + (rr & 1);
  const int pg = 8 * (xcd >> 2) + (rr >> 2), jg = 4 * (xcd & 3) + (rr & 3);
  const int half = j >> 3, slice = j & 7;
  constexpr int PW = 3;
  const __amdgpu_buffer_rsrc_t rs = __builtin_amdgcn_make_buffer_rsrc(ws, 0, WS_END, 0x00020000);
  u64 hold0;
  STAMP(0);
  u64 tag;
  auto load_epoch = [&](u32x2& e) { const u32 z = 0; asm volatile("global_load_dwordx2 %0, %1, %2 sc1" : "=v"(e) : "v"(z), "s"(ws + WS_EPOCH)); };
  auto make_tag = [&](u32x2& e) {
    asm volatile("" : "+v"(e));
    const u32 e0 = e[0], e1 = e[1];
    tag = (((u64)(u32)__builtin_amdgcn_readfirstlane(e1) << 32) | (u32)__builtin_amdgcn_readfirstlane(e0)) + TAG_STEP;
  };
  const char* xa = x + (size_t)p * 128 * 1280;
  const u32x4 ones8 = {ONES2, ONES2, ONES2, ONES2};
  const char* wt = ws + WS_WT + slice * WT_SLICE;
  auto rt_addr = [&](int piece, int gi, u32 slot, u32& l, u32& d) {
    const int tile = 4 * piece + g, sl = tile / 5, t = tile - sl * 5;
    l = slot + q4 * 1280 + tile * 32 + pq * 8;
    d = WS_WT + sl * WT_SLICE + (gi >> 2) * 5120 + t * 1024 + (gi & 3) * 256 + r16 * 16;
  };

  if (j < 15) {
    const int wm = w >> 2, wn = w & 3;
    u32 voffA[2], voffB[2];
#pragma unroll
    for (int i = 0; i < 2; ++i) {
      const int ra = 16 * w + 8 * i + (lane >> 3);
      voffA[i] = ra * 1280 + (((lane & 7) ^ ((ra >> 1) & 7)) << 4);
      const int rb = 8 * w + 4 * i + g, f = (rb & 3) | (((rb >> 3) & 1) << 2);
      voffB[i] = rb * 3840 + (((((r16 >> 1) ^ f) << 1) | (r16 & 1)) << 4);
    }
    const char* wb = wqkv + j * 256;
    const int rot = RG == 10 ? ((rr & 7) + ((rr & 7) >> 2) + ((5 * (rr >> 3)) >> 1)) % 10 : ((rr & 7) + (rr >> 3)) % RG;
    auto stage = [&](int c, int piece) {
      int kc = c % RG + rot; kc = (kc >= RG ? kc - RG : kc) + (c / RG) * RG;
      const u32 slot = (c & 3) * 32768;
      const char* a = xa + kc * 128; const char* b = wb + (size_t)kc * 64 * 3840;
      if (piece < 0 || piece == 0) dma(a, voffA[0], slot + w * 2048);
      if (piece < 0 || piece == 1) dma(a, voffA[1], slot + w * 2048 + 1024);
      if (piece < 0 || piece == 2) dma(b, voffB[0], slot + 16384 + w * 2048);
      if (piece < 0 || piece == 3) dma(b, voffB[1], slot + 16384 + w * 2048 + 1024);
    };
    stage(0, -1);
    u32x2 epoch;
    load_epoch(epoch);
    {
      u64 pc; asm volatile("s_getpc_b64 %0" : "=s"(pc));
      if (w == 0) warm((const char*)(pc & ~127ull) + rr * 1280, (lane < 10 ? lane : 9) * 128, L_WARM);
    }
    stage(1, -1);
    u32 adA[2], adB[2];
#pragma unroll
    for (int ks = 0; ks < 2; ++ks) adA[ks] = (64 * wm + r16) * 128 + (((4 * ks + g) ^ ((r16 >> 1) & 7)) << 4);
#pragma unroll
    for (int n = 0; n < 2; ++n) adB[n] = 16384 + (8 * g + q4) * 256 + (((2 * wn + (n ^ wm)) ^ (q4 | ((g & 1) << 2))) << 5) + pq * 8;
    f32x4 acc[4][2], csum = {0, 0, 0, 0};
#pragma unroll
    for (int m = 0; m < 4; ++m) { acc[m][0] = f32x4{0, 0, 0, 0}; acc[m][1] = f32x4{0, 0, 0, 0}; }
    u32x4 fa[2][4]; u32x2 fb[2][2][2];
    u32x4 rsg[4];
    auto ld_rs = [&]() {
#pragma unroll
      for (int m = 0; m < 4; ++m) gld_sc1(rsg[m], ws + WS_RS + (p * 128 + 64 * wm + 16 * m) * 16, r16 * 16);
    };
    auto rdp = [&](int c, int ks, int i) {
      const u32 slot = (c & 3) * 32768;
      if (i == 0 || i == 1) fb[ks][0][i] = ldstr(slot + adB[0] + ks * 8192 + i * 1024);
      else if (i == 2) fa[ks][0] = lds128(slot + adA[ks]);
      else if (i == 3 || i == 4) fb[ks][1][i - 3] = ldstr(slot + adB[1] + ks * 8192 + (i - 3) * 1024);
      else if (i < 8) fa[ks][i - 4] = lds128(slot + adA[ks] + (i - 4) * 2048);
    };
    auto mm = [&](int ks, int cr, int cn) {
      const u32x4 b0 = cat(fb[ks][0][0], fb[ks][0][1]), b1 = cat(fb[ks][1][0], fb[ks][1][1]);
#pragma unroll
      for (int i = 0; i < 9; ++i) {
        if (i < 8) acc[i >> 1][i & 1] = mfma((i & 1) ? b1 : b0, fa[ks][i >> 1], acc[i >> 1][i & 1]);
        else csum = mfma(b0, ones8, csum);
        if (cr < 10) rdp(cr, ks ^ 1, i);
        if (cn < 10 && i == 4) stage(cn, 2 * (ks ^ 1));
        if (cn < 10 && i == 8) stage(cn, 2 * (ks ^ 1) + 1);
        SB();
      }
    };
    vm_wait<4>();
    make_tag(epoch);
    bar();
    STAMP(16);
#pragma unroll
    for (int i = 0; i < 8; ++i) rdp(0, 0, i);
    SB();
    stage(2, 0); stage(2, 1);
    SB();
#pragma unroll
    for (int c = 0; c < 10; ++c) {
      mm(0, c, c + 2);
      if (c < 9) {
        if (c <= 7) vm_wait<4>(); else vm_wait<0>();
        bar0();
        if (c == 8) {
          ld_rs();
          if (j < 12 && w < 5) {
            dma(wd + (p * 12 + j) * 10240 + w * 2048, lane * 16, L_RT + w * 2048);
            dma(wd + (p * 12 + j) * 10240 + w * 2048 + 1024, lane * 16, L_RT + w * 2048 + 1024);
          }
          SB();
        }
      }
      mm(1, c + 1, c + 3);
    }
    STAMP(1);
    if (r16 == 0) *(LAS f32x4*)(smem + L_MISC + 1024 + (32 * wn + 16 * wm + 4 * g) * 4) = csum;
    if (j < 12 && w < 5) vm_wait<2>(); else vm_wait<0>();
    for (u32 spins = 0; spins < SPIN_CAP; ++spins) {
      asm volatile("" : "+v"(rsg[0]), "+v"(rsg[1]), "+v"(rsg[2]), "+v"(rsg[3]));
      bool ok = true;
#pragma unroll
      for (int m = 0; m < 4; ++m) ok = ok && rsg[m][1] == (u32)tag && rsg[m][3] == (u32)(tag >> 32);
      if (__all(ok)) break;
      ld_rs();
      vm_wait<0>();
    }
    asm volatile("" : "+v"(rsg[0]), "+v"(rsg[1]), "+v"(rsg[2]), "+v"(rsg[3]));
    bar();
    STAMP(17);
    const float qs = j < 5 ? 0.22360679774997896f * 1.4426950408889634f : 1.f;
    f32x4 cs[2];
#pragma unroll
    for (int n = 0; n < 2; ++n) cs[n] = *(LAS const f32x4*)(smem + L_MISC + 1024 + (32 * wn + 16 * (n ^ wm) + 4 * g) * 4);
    const u32 qb = WS_QKV + (p * 240 + 16 * j + 4 * wn + 2 * ((g & 1) ^ wm) + (g >> 1)) * 2048 + (64 * wm + r16) * 16;
#pragma unroll
    for (int m = 0; m < 4; ++m) {
      const u32 um = rsg[m][0], ur = rsg[m][2];
      const float mean = __uint_as_float(um), sc = __uint_as_float(ur) * qs;
      u32x2 pk[2];
#pragma unroll
      for (int n = 0; n < 2; ++n) {
        const f32x4 v = (acc[m][n] - mean * cs[n]) * sc;
        pk[n] = u32x2{pack2(v[0], v[1]), pack2(v[2], v[3])};
      }
      st128_sc1(rs, qb + m * 256, quad2chunk(pk[0], pk[1]));
    }
  } else {
    u32x2 epoch;
    load_epoch(epoch);
    u32x4 xv[2][10];
#pragma unroll
    for (int s2 = 0; s2 < 2; ++s2)
#pragma unroll
      for (int t = 0; t < 10; ++t) gld(xv[s2][t], xa + (16 * w + 8 * s2) * 1280 + t * 128, (lane >> 3) * 1280 + (lane & 7) * 16);
    if (w < 5) {
#pragma unroll
      for (int i = 0; i < 10; ++i) dma(wo + (p * 5 + w) * 10240 + i * 1024, lane * 16, w * 10240 + i * 1024);
    }
#pragma unroll
    for (int s2 = 0; s2 < 2; ++s2) {
      if (s2 == 0) { if (w < 5) vm_wait<20>(); else vm_wait<10>(); make_tag(epoch); } else { if (w < 5) vm_wait<11>(); else vm_wait<1>(); }
      float s1[2] = {0.f, 0.f}, sq[2] = {0.f, 0.f};
#pragma unroll
      for (int t = 0; t < 10; ++t) { asm volatile("" : "+v"(xv[s2][t])); s1[t & 1] = sum8(xv[s2][t], s1[t & 1]); sq[t & 1] = sq8(xv[s2][t], sq[t & 1]); }
      float a1 = s1[0] + s1[1], a2 = sq[0] + sq[1];
#pragma unroll
      for (int o = 1; o < 8; o <<= 1) { a1 += __shfl_xor(a1, o); a2 += __shfl_xor(a2, o); }
      const float mean = a1 * (1.f / 640.f), var = __builtin_fmaxf(a2 * (1.f / 640.f) - mean * mean, 0.f);
      if ((lane & 7) == 0)
        st128_sc1(rs, WS_RS + (p * 128 + 16 * w + 8 * s2 + (lane >> 3)) * 16, u32x4{__float_as_uint(mean), (u32)tag, __float_as_uint(__builtin_amdgcn_rsqf(var + LN_EPS)), (u32)(tag >> 32)});
    }
    STAMP(16);
    if (w < 5) {
      vm_wait<2>();
      const int gi = p * 5 + w;
#pragma unroll
      for (int i = 0; i < 10; ++i) { u32 l, d; rt_addr(i, gi, w * 10240, l, d); st128_sc1(rs, d, ldstr2(l, l + 5120)); }
      float a1 = 0.f;
#pragma unroll
      for (int t = 0; t < 10; ++t) a1 = sum8(lds128(w * 10240 + (lane >> 3) * 1280 + t * 128 + (lane & 7) * 16), a1);
#pragma unroll
      for (int o = 1; o < 8; o <<= 1) a1 += __shfl_xor(a1, o);
      a1 *= (1.f / 640.f);
      const u32 hi = pack2(a1, 0.f), lo = pack2(a1 - bflo(hi), 0.f);
      if ((lane & 7) < 2) __builtin_amdgcn_raw_buffer_store_b16((u16)((lane & 1) ? lo : hi), rs, (int)(WS_WB + (lane & 1) * 1280 + (gi * 8 + (lane >> 3)) * 2), 0, 16);
    }
  }
  STAMP(18);
  vm_wait<0>();
  bar();
  STAMP(2);
  if (w == PW && lane == 0) __hip_atomic_store((GAS u64*)(ws + flag_off(0, p, j)), tag, RLX_AGENT);
  asm volatile("" ::: "memory");
  u32 kvsrc[8], kvdst[8];
#pragma unroll
  for (int i = 0; i < 8; ++i) {
    const int id = w + 8 * i, kv = id >= 30, r = id - 30 * kv, c = r / 6, r2 = r - 6 * c;
    int pp = pa - 2 + (r2 >> 1); pp = pp < 0 ? 0 : pp;
    kvsrc[i] = WS_QKV + (pp * 240 + 80 * (1 + kv) + 5 * ja + c) * 2048 + (r2 & 1) * 1024;
    kvdst[i] = (kv ? L_V + c * VCH : L_K + c * KCH) + r2 * 1024;
    asm volatile("" : "+s"(kvsrc[i]), "+s"(kvdst[i]));
  }
  if (tid < 384) {
    const bool valid = 128 * (pa - 2) + tid >= 0;
    *(LAS u32x4*)(smem + L_K + 5 * KCH + tid * 16) = u32x4{valid ? 0u : 0xf149u, 0u, ONES2, ONES2};
  }
  u32 rtl[2], rtg[2];
#pragma unroll
  for (int i = 0; i < 2; ++i) { rt_addr(2 * w + i, 80 + p * 12 + j, L_RT, rtl[i], rtg[i]); asm volatile("" : "+v"(rtl[i]), "+v"(rtg[i])); }
  u32 fl_put, fl_get;
  {
    const int e = r16 < 14 ? r16 : 0, sec = e < 2 ? 0 : (e < 8 ? 1 : 2), i = e < 2 ? e : (e - 2) % 6;
    int pp = e < 2 ? pa : pa - (i >> 1); pp = pp < 0 ? 0 : pp;
    fl_get = (g & 1) ? flag_off(0, r16, 15) : flag_off(0, pp, (80 * sec + 5 * ja + 4 * (i & 1)) >> 4);
    fl_put = lane == 0 ? flag_off(1, pa, ja) : WS_DENSE + bx * 8;
    asm volatile("" : "+v"(fl_get), "+v"(fl_put));
  }
  if (w == PW) poll_flags(ws, fl_get, tag);
  bar();
  STAMP(3);

  u32x2 xr[6];
  u32 voffG[3];
  {
    const int h = w & 1, qt = w >> 1, l32 = lane & 31, hh = lane >> 5;
    u32x4 qf[2];
    gld_sc1(qf[0], ws + WS_QKV + (pa * 240 + 5 * ja + 2 * h) * 2048 + qt * 512, hh * 2048 + l32 * 16);
    gld_sc1(qf[1], ws + WS_QKV + (pa * 240 + 5 * ja + 2 * h + 2) * 2048 + qt * 512, l32 * 16);
#pragma unroll
    for (int i = 0; i < 5; ++i) dma_sc1(ws + kvsrc[i], lane * 16, kvdst[i]);
    u32 dm[8];
#pragma unroll
    for (int m = 0; m < 8; ++m) {
      const int k0 = ((2 * m) & 3) + 8 * (m >> 1) + 4 * hh;
      dm[m] = (k0 <= l32 ? 0xffffu : 0u) | (k0 + 1 <= l32 ? 0xffff0000u : 0u);
      asm volatile("" : "+v"(dm[m]));
    }
    u32 adK0 = L_K + (2 * h + hh) * KCH + (32 * qt + l32) * 16, adK1 = L_K + (hh ? 5 : 2 * h + 2) * KCH + (32 * qt + l32) * 16;
    const u32 vrow = 32 * qt + 4 * hh + q4;
    u32 adV = (g & 1) == 0 ? L_V + (3 * h + (pq >> 1)) * VCH + vrow * 16 + (pq & 1) * 8
                           : (pq == 0 ? L_V + 2 * VCH + vrow * 16 + 8 * h : L_K + 5 * KCH + ((4 * hh + (h ? 4 : 12)) & 15) * 16 + 8);
    u32 voffwarm;
    { const int l6 = lane < 30 ? lane : 29, rw = l6 / 6; voffwarm = rw * 3072 + (l6 - rw * 6) * 128; }
#pragma unroll
    for (int i = 0; i < 3; ++i) {
      const int s = 64 * i + lane, rw = s / 12, cp = s - rw * 12;
      int c = cp - (w & 1) - 2 * ((rw >> 3) & 1); c = c < 0 ? c + 12 : c;
      voffG[i] = rw * 3072 + c * 16;
    }
    asm volatile("" : "+v"(voffwarm), "+v"(adK0), "+v"(adK1), "+v"(adV), "+v"(voffG[0]), "+v"(voffG[1]), "+v"(voffG[2]));
    vm_wait<1>();
    asm volatile("" : "+v"(qf[0]), "+v"(qf[1]));
    bar();
    STAMP(4);
    const f32x16 zero16 = {0, 0, 0, 0, 0, 0, 0, 0, 0, 0, 0, 0, 0, 0, 0, 0};
    u32x4 kf[9][2];
    constexpr int ord[9] = {0, 8, 1, 2, 3, 4, 5, 6, 7};
#pragma unroll
    for (int n = 0; n < 4; ++n) { kf[ord[n]][0] = lds128(adK0 + 512 * ord[n]); kf[ord[n]][1] = lds128(adK1 + 512 * ord[n]); }
    SB();
#pragma unroll
    for (int i = 5; i < 8; ++i)
      if (i < 7 || w < 4) dma_sc1(ws + kvsrc[i], lane * 16, kvdst[i]);
    if (h == 1 && hh == 0) { qf[0][0] = 0; qf[0][1] = 0; }
    if (h == 0) { qf[1][2] = 0; qf[1][3] = 0; }
    if (hh == 1) qf[1] = u32x4{0x3f80u, 0, 0, 0};
    SB();
    f32x16 sc[9];
    float m4[4];
    auto qk = [&](int i, int st) { sc[i] = mfma32(kf[i][st], qf[st], st == 0 ? zero16 : sc[i]); };
    qk(0, 0); qk(8, 0); qk(0, 1); qk(8, 1);
    SB();
#pragma unroll
    for (int n = 2; n < 11; ++n) {
      if (n < 9) { qk(ord[n], 0); SB(); }
      if (n + 2 < 9) { kf[ord[n + 2]][0] = lds128(adK0 + 512 * ord[n + 2]); SB(); }
      if (n == 3) {
#pragma unroll
        for (int r = 6; r < 16; ++r) sc[0][r] = ((r & 3) + 8 * (r >> 2) + 4 * hh <= l32) ? sc[8][r] : sc[0][r];
      } else if (n > 3) {
#pragma unroll
        for (int c = 0; c < 4; ++c) m4[c] = max3(m4[c], sc[n - 3][2 * c], sc[n - 3][2 * c + 1]);
      }
      SB();
      if (n < 9) { qk(ord[n], 1); SB(); }
      if (n + 2 < 9) { kf[ord[n + 2]][1] = lds128(adK1 + 512 * ord[n + 2]); SB(); }
      if (n == 2) {
#pragma unroll
        for (int r = 0; r < 6; ++r) sc[0][r] = ((r & 3) + 8 * (r >> 2) + 4 * hh <= l32) ? sc[8][r] : sc[0][r];
      } else if (n == 3) {
#pragma unroll
        for (int c = 0; c < 4; ++c) m4[c] = max3(max3(sc[0][4 * c], sc[0][4 * c + 1], sc[0][4 * c + 2]), sc[0][4 * c + 3], sc[0][4 * c + 3]);
      } else {
#pragma unroll
        for (int c = 0; c < 4; ++c) m4[c] = max3(m4[c], sc[n - 3][8 + 2 * c], sc[n - 3][9 + 2 * c]);
      }
      SB();
    }
    float mx = __builtin_fmaxf(max3(m4[0], m4[1], m4[2]), m4[3]);
    { auto sw = __builtin_amdgcn_permlane32_swap(__float_as_uint(mx), __float_as_uint(mx), false, false); mx = __builtin_fmaxf(__uint_as_float(sw[0]), __uint_as_float(sw[1])); }
    u32x4 nmf;
    {
      const float nmx = -mx;
      const u32 p0 = __float_as_uint(nmx) & 0xffff0000u;
      const float r1 = nmx - __uint_as_float(p0);
      const u32 p1 = __float_as_uint(r1) & 0xffff0000u;
      const u32 p2 = __float_as_uint(r1 - __uint_as_float(p1));
      nmf = u32x4{hh ? p2 >> 16 : (p0 >> 16) | p1, 0, 0, 0};
    }
    auto submax = [&](int i) { sc[i] = mfma32(ones8, nmf, sc[i]); };
    submax(0); submax(1);
    SB();
    u32x4 rt0, rt1;
    if (j < 12 && w < 5) { rt0 = ldstr2(rtl[0], rtl[0] + 5120); rt1 = ldstr2(rtl[1], rtl[1] + 5120); }
    vm_wait<0>();
    bar();
    STAMP(26);
    if (w >= 4) {
#pragma unroll
      for (int t = 0; t < 5; ++t) gld64(xr[t], xa + (64 * half + 16 * (w - 4)) * 1280 + slice * 160 + t * 32, r16 * 1280 + g * 8);
      asm volatile("global_load_dwordx2 %0, %1, %2 sc1" : "=v"(xr[5]) : "v"(r16 * 16), "s"(ws + WS_RS + (p * 128 + 64 * half + 16 * (w - 4)) * 16));
    } else {
#pragma unroll
      for (int t = 0; t < 6; ++t) asm volatile("v_mov_b64 %0, 0" : "=v"(xr[t]));
    }
    if (j < 12 && w < 5) { st128_sc1(rs, rtg[0], rt0); st128_sc1(rs, rtg[1], rt1); }
    warm(((w & 1) ? wu : wg) + (size_t)(rr * 20 + (w >> 1) * 5) * 3072 + (xcd & 3) * 768, voffwarm, L_WARM);
    SB();
    f32x16 o = zero16;
    u32 pw[2][8];
    u32x2 vf[2][4], vx[4];
    auto rdV = [&](int i, int e) { return ldstr(adV + 512 * i + 128 * e); };
    auto pfrag = [&](const u32* q, int st) { return u32x4{q[4 * st], q[4 * st + 1], q[4 * st + 2], q[4 * st + 3]}; };
    u32 p8[8];
#pragma unroll
    for (int n = 0; n < 8; ++n) {
      float ex[16];
#pragma unroll
      for (int e = 0; e < 8; ++e) {
        ex[2 * e] = __builtin_amdgcn_exp2f(sc[n][2 * e]); SB();
        if (e < 4) { vf[n & 1][e] = rdV(n, e); SB(); }
        else if (n == 0) { vx[e - 4] = rdV(8, e - 4); SB(); }
        ex[2 * e + 1] = __builtin_amdgcn_exp2f(sc[n][2 * e + 1]); SB();
        if (e > 0) { pw[n & 1][e - 1] = pack2(ex[2 * e - 2], ex[2 * e - 1]); SB(); }
        if (n >= 1) {
          if (e == 0 && n < 7) { submax(n + 1); SB(); }
          if (e == 3) { o = mfma32(cat(vf[(n - 1) & 1][0], vf[(n - 1) & 1][1]), pfrag(pw[(n - 1) & 1], 0), o); SB(); }
          if (e == 6) { o = mfma32(cat(vf[(n - 1) & 1][2], vf[(n - 1) & 1][3]), pfrag(pw[(n - 1) & 1], 1), o); SB(); }
          if (n == 2 && e == 2) { o = mfma32(cat(vx[0], vx[1]), pfrag(p8, 0), o); SB(); }
          if (n == 2 && e == 5) { o = mfma32(cat(vx[2], vx[3]), pfrag(p8, 1), o); SB(); }
        }
      }
      pw[n & 1][7] = pack2(ex[14], ex[15]);
      if (n == 0) {
#pragma unroll
        for (int m = 0; m < 8; ++m) { p8[m] = pw[0][m] & dm[m]; pw[0][m] &= ~dm[m]; }
      }
      SB();
      if (n == 3) STAMP(27);
    }
    o = mfma32(cat(vf[1][0], vf[1][1]), pfrag(pw[1], 0), o);
    o = mfma32(cat(vf[1][2], vf[1][3]), pfrag(pw[1], 1), o);
    const float inv = __builtin_amdgcn_rcpf(o[12]);
    const u32 x0 = pack2(o[0] * inv, o[1] * inv), x1 = pack2(o[2] * inv, o[3] * inv), y0 = pack2(o[4] * inv, o[5] * inv), y1 = pack2(o[6] * inv, o[7] * inv);
    auto s0 = __builtin_amdgcn_permlane32_swap(x0, y0, false, false);
    auto s1 = __builtin_amdgcn_permlane32_swap(x1, y1, false, false);
    const u32 ob = WS_O + (pa * 80 + 5 * ja) * 2048 + (32 * qt + l32) * 16;
    st128_sc1(rs, ob + (3 * h + hh) * 2048, u32x4{s0[0], s1[0], s0[1], s1[1]});
    if (hh == 0) st64_sc1(rs, ob + 2 * 2048 + 8 * h, u32x2{pack2(o[8] * inv, o[9] * inv), pack2(o[10] * inv, o[11] * inv)});
    STAMP(5);
  }
  const char* gsrc = ((w & 1) ? wu : wg) + jg * 192;
  auto stageGBp = [&](int c, int rb, int i) {
    dma(gsrc + (size_t)(c * 64 + rb * 16) * 3072, voffG[i], gb_off(c) + (w & 1) * 12288 + rb * 3072 + i * 1024);
  };
  auto stageGB = [&](int c, int rb) { stageGBp(c, rb, 0); stageGBp(c, rb, 1); stageGBp(c, rb, 2); };
  const int wm4 = w & 3, wk = w >> 2;
  u32 voffR = g * 2048 + (64 * half + 16 * wm4 + r16) * 16;
  fl_get = flag_off(1, p, r16);
  u32 fl_get2 = flag_off(2, pg, r16);
  asm volatile("" : "+v"(voffR), "+v"(fl_get), "+v"(fl_get2));
  asm volatile("" ::: "memory");
  if (w < 4) {
#pragma unroll
    for (int i = 0; i < 20; ++i) dma_sc1(wt + 20480 * (1 + w) + i * 1024, lane * 16, L_WOB + 20480 * (1 + w) + i * 1024);
  }
  bar();
  if (w < 4) {
#pragma unroll
    for (int i = 0; i < 5; ++i) dma_sc1(wt + 5120 * w + i * 1024, lane * 16, L_WOB + 5120 * w + i * 1024);
    dma_sc1(ws + WS_WB + (w < 3 ? w : 2) * 1024, lane * 16, L_WB + (w < 3 ? w : 2) * 1024);
    vm_wait<6>();
  } else {
    stageGB(2, w >> 1); stageGB(2, (w >> 1) - 2);
    vm_wait<6>();
  }
  bar0();
  STAMP(6);
  u32x2 hp[5];
  {
    if (w == PW && lane < 2) __hip_atomic_store((GAS u64*)(ws + fl_put), tag, RLX_AGENT);
    u32x4 a[10], b[6][5];
    const u32 ad0 = L_WOB + wk * 10 * 5120 + lane * 16, adm = L_WB + (r16 & 1) * 1280 + wk * 640 + g * 16;
    auto rdB = [&](int s, int t) { b[s % 6][t] = lds128(ad0 + ((s + 4) % 10) * 5120 + t * 1024); };
#pragma unroll
    for (int s = 0; s < 6; ++s)
#pragma unroll
      for (int t = 0; t < 5; ++t) rdB(s, t);
    asm volatile("" : "+v"(xr[0]), "+v"(xr[1]), "+v"(xr[2]), "+v"(xr[3]), "+v"(xr[4]), "+v"(xr[5]));
    f32x4 hacc[5], macc;
    { const u32 xm = xr[5][0]; macc = f32x4{__uint_as_float(xm), 0.f, 0.f, 0.f}; }
#pragma unroll
    for (int t = 0; t < 5; ++t) { const u32 a0 = xr[t][0], a1 = xr[t][1]; hacc[t] = f32x4{bflo(a0), bfhi(a0), bflo(a1), bfhi(a1)}; }
    SB();
    if (w == PW) poll_flags(ws, fl_get, tag); else if (w < 4) vm_wait<0>();
    bar();
    STAMP(7);
    const char* osrc = ws + WS_O + (p * 80 + 40 * wk) * 2048;
#pragma unroll
    for (int s = 0; s < 10; ++s) gld_sc1(a[s], osrc + ((s + 4) % 10) * 8192, voffR);
    u32x4 bm[2];
    bm[0] = lds128(adm + 4 * 64);
    SB();
#pragma unroll
    for (int s = 0; s < 10; ++s) {
      switch (s) {
        case 0: vm_wait_use<9>(a[0]); break; case 1: vm_wait_use<8>(a[1]); break; case 2: vm_wait_use<7>(a[2]); break;
        case 3: vm_wait_use<6>(a[3]); break; case 4: vm_wait_use<5>(a[4]); break; case 5: vm_wait_use<4>(a[5]); break;
        case 6: vm_wait_use<3>(a[6]); break; case 7: vm_wait_use<2>(a[7]); break; case 8: vm_wait_use<1>(a[8]); break;
        default: vm_wait_use<0>(a[9]); break;
      }
      if (s == 0) STAMP(28);
#pragma unroll
      for (int t = 0; t < 5; ++t) {
        hacc[t] = mfma(b[s % 6][t], a[s], hacc[t]);
        SB();
        if (s + 6 < 10) { rdB(s + 6, t); SB(); }
        if (t == 0 && s < 9) { bm[(s + 1) & 1] = lds128(adm + ((s + 5) % 10) * 64); SB(); }
      }
      macc = mfma(bm[s & 1], a[s], macc);
      SB();
    }
    STAMP(8);
    if (wk == 0) {
#pragma unroll
      for (int t = 0; t < 5; ++t) *(LAS f32x4*)(smem + L_RED + (wm4 * 5 + t) * 1024 + lane * 16) = hacc[t];
      *(LAS float*)(smem + L_RED + 20480 + wm4 * 256 + lane * 4) = macc[0] + macc[1];
    }
    bar();
    if (wk == 1) {
      float s1 = 0.f, s2 = 0.f;
      const float mean = macc[0] + macc[1] + *(LAS const float*)(smem + L_RED + 20480 + wm4 * 256 + lane * 4);
      u32x2 hc[5];
#pragma unroll
      for (int t = 0; t < 5; ++t) {
        const f32x4 v = hacc[t] + *(LAS const f32x4*)(smem + L_RED + (wm4 * 5 + t) * 1024 + lane * 16);
        const u32 lo = pack2(v[0], v[1]), hi = pack2(v[2], v[3]);
        hp[t] = u32x2{lo, hi};
        hc[t] = u32x2{pack2(bflo(lo) - mean, bfhi(lo) - mean), pack2(bflo(hi) - mean, bfhi(hi) - mean)};
      }
      const u32 hb = WS_H + (p * 80 + 10 * slice + 2 * (g & 1) + (g >> 1)) * 2048 + (64 * half + 16 * wm4 + r16) * 16;
      st128_sc1(rs, hb, quad2chunk(hc[0], hc[1]));
      st128_sc1(rs, hb + 4 * 2048, quad2chunk(hc[2], hc[3]));
      const u32x4 last = quad2chunk(hc[4], hc[4]);
      if ((g & 1) == 0) st128_sc1(rs, hb + 8 * 2048, last);
#pragma unroll
      for (int t = 0; t < 5; ++t) { const u32 lo = hc[t][0], hi = hc[t][1]; s1 = dot2(hi, ONES2, dot2(lo, ONES2, s1)); s2 = dot2(hi, hi, dot2(lo, lo, s2)); }
      s1 = gsum(s1); s2 = gsum(s2);
      if (g == 0) st64_sc1(rs, WS_ST + (p * 8 + slice) * 1024 + (64 * half + 16 * wm4 + r16) * 8, u32x2{__float_as_uint(s1), __float_as_uint(s2)});
      vm_wait<0>();
    } else {
#pragma unroll
      for (int t = 0; t < 5; ++t) hp[t] = u32x2{0, 0};
      stageGB(0, w >> 1); stageGB(0, 2 + (w >> 1)); stageGB(1, w >> 1); stageGB(1, 2 + (w >> 1));
    }
  }
  bar();
  STAMP(9);
  if (w == PW) {
    if (lane == 0) __hip_atomic_store((GAS u64*)(ws + flag_off(2, p, j)), tag, RLX_AGENT);
    poll_flags(ws, fl_get2, tag);
  } else if (w == 2) {
    GAS u64* a = (GAS u64*)(ws + WS_DENSE + lane * 8);
    for (u32 spins = 0; spins < SPIN_CAP; ++spins) {
      const u64 v0 = __hip_atomic_load(a, RLX_AGENT), v1 = __hip_atomic_load(a + 64, RLX_AGENT), v2 = __hip_atomic_load(a + 128, RLX_AGENT), v3 = __hip_atomic_load(a + 192, RLX_AGENT);
      if (__all(v0 == tag && v1 == tag && v2 == tag && v3 == tag)) break;
    }
    if (bx == 0 && lane == 0) __hip_atomic_store((GAS u64*)(ws + WS_EPOCH), tag, RLX_AGENT);
  } else if (wk == 1) {
    stageGB(3, w >> 1); stageGB(3, (w >> 1) - 2);
  }
  bar();
  STAMP(10);

  const int rotd = (((slice * 6) >> 3) + ((3 * (rr >> 3)) >> 1)) % 6;
  auto stageDp = [&](int c, int i) {
    int kc = c + rotd; kc = kc >= 6 ? kc - 6 : kc;
    dma_sc1(wt + (20 + kc * 8 + w) * 5120 + i * 1024, lane * 16, (c & 3) * 40960 + w * 5120 + i * 1024);
  };
  auto stageD = [&](int c) {
#pragma unroll
    for (int i = 0; i < 5; ++i) stageDp(c, i);
  };

  {
    const int wm = w >> 2, wn = w & 3;
    const char* hsrc = ws + WS_H + (pg * 80 + w) * 2048;
    auto stageGAp = [&](int c, int i) { dma_sc1(hsrc + c * 16384 + i * 1024, lane * 16, ga_off(c) + w * 2048 + i * 1024); };
    auto stageGA = [&](int c) { stageGAp(c, 0); stageGAp(c, 1); };
    stageGA(0);
    dma_sc1(ws + WS_ST + (pg * 8 + w) * 1024, lane * 16, L_ST + w * 1024);
    stageGA(1);
    const u32 adA = g * 2048 + (64 * wm + r16) * 16;
    u32 adB[3];
#pragma unroll
    for (int t = 0; t < 3; ++t) {
      const int mat = t == 0 ? 0 : (t == 1 ? 1 : (pq >> 1)), c = t < 2 ? 3 * wn + (pq >> 1) : 3 * wn + 2;
      int cp = c + mat + 2 * (g & 1); cp = cp >= 12 ? cp - 12 : cp;
      adB[t] = mat * 12288 + (8 * g + q4) * 192 + cp * 16 + (pq & 1) * 8;
    }
    f32x4 acc[4][3];
#pragma unroll
    for (int m = 0; m < 4; ++m)
#pragma unroll
      for (int t = 0; t < 3; ++t) acc[m][t] = f32x4{0, 0, 0, 0};
    u32x4 fa[2][4]; u32x2 fb[2][3][2];
    auto rdp = [&](int c, int s, int i) {
      const u32 sa = ga_off(c) + s * 8192, sb = gb_off(c) + s * 6144;
      if (i < 2) fb[s][0][i] = ldstr(sb + adB[0] + i * 768);
      else if (i == 2) fa[s][0] = lds128(sa + adA);
      else if (i < 7) fb[s][1 + ((i - 3) >> 1)][(i - 3) & 1] = ldstr(sb + adB[1 + ((i - 3) >> 1)] + ((i - 3) & 1) * 768);
      else if (i < 10) fa[s][i - 6] = lds128(sa + adA + (i - 6) * 256);
    };
    auto mm = [&](int s, int c) {
      u32x4 b[3];
#pragma unroll
      for (int t = 0; t < 3; ++t) b[t] = cat(fb[s][t][0], fb[s][t][1]);
      const int cr = s == 0 ? c : c + 1;
#pragma unroll
      for (int i = 0; i < 12; ++i) {
        acc[i / 3][i % 3] = mfma(b[i % 3], fa[s][i / 3], acc[i / 3][i % 3]);
        if (cr < 10) rdp(cr, s ^ 1, i);
        if (s == 1 && (i == 4 || i == 10) && c + 3 < 10) stageGAp(c + 3, i == 10);
        if (s == 0 && i % 4 == 3 && c >= 1 && c + 3 < 10) stageGBp(c + 3, w >> 1, i >> 2);
        if (s == 1 && (c == 7 || c == 8) && i % 2 == 1 && i < 10) stageDp(c - 7, i >> 1);
        SB();
      }
    };
    vm_wait<3>();
    bar();
    STAMP(19);
#pragma unroll
    for (int i = 0; i < 10; ++i) rdp(0, 0, i);
    SB();
    stageGA(2);
    SB();
#pragma unroll
    for (int c = 0; c < 10; ++c) {
      if (c == 1) {
        const f32x2 u0 = *(LAS const f32x2*)(smem + L_ST + (2 * g) * 1024 + (16 * w + r16) * 8), u1 = *(LAS const f32x2*)(smem + L_ST + (2 * g + 1) * 1024 + (16 * w + r16) * 8);
        const float s1 = gsum(u0[0] + u1[0]), s2 = gsum(u0[1] + u1[1]);
        const float mean = s1 * (1.f / 640.f), var = __builtin_fmaxf(s2 * (1.f / 640.f) - mean * mean, 0.f);
        if (g == 0) *(LAS float*)(smem + L_MISC + (16 * w + r16) * 4) = __builtin_amdgcn_rcpf(var + LN_EPS);
      }
      mm(0, c);
      if (c < 9) {
        if (c == 0) vm_wait<2>(); else if (c == 1) vm_wait<5>(); else if (c <= 6) vm_wait<8>(); else vm_wait<5>();
        bar();
        if (c == 1) STAMP(29);
        if (c == 4) STAMP(20);
        if (c == 7) STAMP(30);
      }
      mm(1, c);
    }
    STAMP(11);
    u32x2 p01[4];
    float r2[4];
#pragma unroll
    for (int m = 0; m < 4; ++m) {
      r2[m] = *(LAS const float*)(smem + L_MISC + (64 * wm + 16 * m + r16) * 4);
      const f32x4 v = acc[m][0] * acc[m][1] * r2[m];
      p01[m] = u32x2{pack2(v[0], v[1]), pack2(v[2], v[3])};
    }
    const u32 hb = WS_HH + (pg * 192 + 12 * jg + 3 * wn) * 2048 + (64 * wm + r16) * 16;
    st128_sc1(rs, hb + (g >> 1) * 2048 + (g & 1) * 256, quad2chunk(p01[0], p01[1]));
    st128_sc1(rs, hb + (g >> 1) * 2048 + (2 + (g & 1)) * 256, quad2chunk(p01[2], p01[3]));
    u32x2 p2[2];
#pragma unroll
    for (int mm2 = 0; mm2 < 2; ++mm2) {
      f32x4 v;
#pragma unroll
      for (int r = 0; r < 4; ++r) {
        auto sw = __builtin_amdgcn_permlane32_swap(__float_as_uint(acc[2 * mm2][2][r]), __float_as_uint(acc[2 * mm2 + 1][2][r]), false, false);
        v[r] = __uint_as_float(sw[0]) * __uint_as_float(sw[1]) * (lane < 32 ? r2[2 * mm2] : r2[2 * mm2 + 1]);
      }
      p2[mm2] = u32x2{pack2(v[0], v[1]), pack2(v[2], v[3])};
    }
    st128_sc1(rs, hb + 2 * 2048 + (2 * (g & 1) + (g >> 1)) * 256, quad2chunk(p2[0], p2[1]));
    STAMP(31);
    vm_wait<8>();
    bar();
    stageD(2); stageD(3);
  }

  {
    u32x4 b[6][5];
    const u32 ad0 = wk * 4 * 5120 + lane * 16;
    auto rdB = [&](int s, int t) { b[s % 6][t] = lds128(ad0 + ((s >> 2) & 3) * 40960 + (s & 3) * 5120 + t * 1024); };
#pragma unroll
    for (int s = 0; s < 4; ++s)
#pragma unroll
      for (int t = 0; t < 5; ++t) rdB(s, t);
    SB();
    vm_wait<10>();
    bar();
    STAMP(12);
    if (w == PW && lane == 0) __hip_atomic_store((GAS u64*)(ws + flag_off(3, pg, jg)), tag, RLX_AGENT);
#pragma unroll
    for (int s = 4; s < 6; ++s)
#pragma unroll
      for (int t = 0; t < 5; ++t) rdB(s, t);
    f32x4 acc[5];
#pragma unroll
    for (int t = 0; t < 5; ++t) { const u32 a = hp[t][0], b2 = hp[t][1]; acc[t] = f32x4{bflo(a), bfhi(a), bflo(b2), bfhi(b2)}; }
    SB();
    if (w == PW) poll_flags(ws, fl_get + 2 * 65536, tag); else vm_wait<5>();
    bar();
    STAMP(13);
    u32x4 a[6][4];
    const char* asrc = ws + WS_HH + (p * 192 + 16 * wk) * 2048;
    auto loadAp = [&](int c, int s) {
      int kc = c + rotd; kc = kc >= 6 ? kc - 6 : kc;
      gld_sc1(a[c][s], asrc + kc * 65536 + s * 8192, voffR);
    };
    auto issue = [&](int n) {
      if (n < 4) loadAp(2, n); else if (n < 9) stageDp(4, n - 4); else if (n < 13) loadAp(3, n - 9); else if (n < 18) stageDp(5, n - 13);
      else if (n < 22) loadAp(4, n - 18); else if (n < 26) loadAp(5, n - 22);
    };
#pragma unroll
    for (int s = 0; s < 4; ++s) loadAp(0, s);
#pragma unroll
    for (int s = 0; s < 4; ++s) loadAp(1, s);
#pragma unroll
    for (int s = 0; s < 24; ++s) {
      if (s == 0) vm_wait<4>();
      if (s == 4) vm_wait<8>();
      if (s == 8) vm_wait<12>();
      if (s == 10) vm_wait<11>();
      if (s == 12) vm_wait<11>();
      if (s == 14) vm_wait<8>();
      if (s == 16) vm_wait<4>();
      if (s == 20) vm_wait<0>();
      if (s % 4 == 0) asm volatile("" : "+v"(a[s >> 2][0]), "+v"(a[s >> 2][1]), "+v"(a[s >> 2][2]), "+v"(a[s >> 2][3]));
      if (s == 6) bar();
      if (s == 10 || s == 14) bar0();
      if (s == 0) STAMP(21);
      if (s == 4) STAMP(22); if (s == 8) STAMP(23); if (s == 12) STAMP(24); if (s == 16) STAMP(25);
#pragma unroll
      for (int t = 0; t < 5; ++t) {
        acc[t] = mfma(b[s % 6][t], a[s >> 2][s & 3], acc[t]);
        SB();
        if (s + 6 < 24) { rdB(s + 6, t); SB(); }
        if (t == 1) { issue(2 * s); SB(); }
        if (t == 4) { issue(2 * s + 1); SB(); }
      }
    }
    STAMP(14);
    constexpr u32 L_RED2 = 2 * 40960;
    if (wk == 0) {
#pragma unroll
      for (int t = 0; t < 5; ++t) *(LAS f32x4*)(smem + L_RED2 + (wm4 * 5 + t) * 1024 + lane * 16) = acc[t];
    }
    bar();
    if (wk == 1) {
      u32x2 op[5];
#pragma unroll
      for (int t = 0; t < 5; ++t) {
        const f32x4 v = acc[t] + *(LAS const f32x4*)(smem + L_RED2 + (wm4 * 5 + t) * 1024 + lane * 16);
        op[t] = u32x2{pack2(v[0], v[1]), pack2(v[2], v[3])};
      }
      const __amdgpu_buffer_rsrc_t ro = __builtin_amdgcn_make_buffer_rsrc(out, 0, 2048 * 1280, 0x00020000);
      const u32 oo = (128 * p + 64 * half + 16 * wm4 + r16) * 1280 + slice * 160 + (2 * (g & 1) + (g >> 1)) * 16;
      __builtin_amdgcn_raw_buffer_store_b128(quad2chunk(op[0], op[1]), ro, (int)oo, 0, OUT_AUX);
      __builtin_amdgcn_raw_buffer_store_b128(quad2chunk(op[2], op[3]), ro, (int)oo + 64, 0, OUT_AUX);
      const u32x4 last = quad2chunk(op[4], op[4]);
      if ((g & 1) == 0) __builtin_amdgcn_raw_buffer_store_b128(last, ro, (int)oo + 128, 0, OUT_AUX);
    }
    STAMP(15);
  }
}

extern "C" void kernel_launch(void* const* d_in, const int* in_sizes, int n_in, void* d_out, int out_size, void* d_ws, size_t ws_size, hipStream_t stream) {
  (void)in_sizes; (void)n_in; (void)out_size; (void)ws_size;
  (void)hipFuncSetAttribute((const void*)fused_block, hipFuncAttributeMaxDynamicSharedMemorySize, LDS_BYTES);
  fused_block<<<256, 512, LDS_BYTES, stream>>>((const char*)d_in[0], (const char*)d_in[1], (const char*)d_in[2], (const char*)d_in[4], (const char*)d_in[5],
                                               (const char*)d_in[6], (char*)d_out, (char*)d_ws);
}
```

```cpp
#include <hip/hip_runtime.h>
#include <stdint.h>

typedef unsigned short u16;
typedef unsigned int u32;
typedef unsigned long long u64;
typedef __bf16 bf16x8 __attribute__((ext_vector_type(8)));
typedef __bf16 bf16x2 __attribute__((ext_vector_type(2)));
typedef short s16x4 __attribute__((ext_vector_type(4)));
typedef float f32x4 __attribute__((ext_vector_type(4)));
typedef float f32x2 __attribute__((ext_vector_type(2)));
typedef u32 u32x4 __attribute__((ext_vector_type(4)));
typedef u32 u32x2 __attribute__((ext_vector_type(2)));
#define LAS __attribute__((address_space(3)))
#define GAS __attribute__((address_space(1)))
#define DI __device__ __forceinline__
#define RLX_AGENT __ATOMIC_RELAXED, __HIP_MEMORY_SCOPE_AGENT

#define RG 5
#define OUT_AUX 0
#define HOLD_AT 99
#define ATT_PRIO 0
#define STAMPS 0

constexpr u32 WS_EPOCH = 0;
constexpr u32 WS_DENSE = 2048;
constexpr u32 WS_FLAG = 4096;
constexpr u32 WS_QKV = WS_FLAG + 4 * 256 * 256;
constexpr u32 WS_O = WS_QKV + 16 * 240 * 2048;
constexpr u32 WS_H = WS_O + 16 * 80 * 2048;
constexpr u32 WS_HH = WS_H + 16 * 80 * 2048;
constexpr u32 WS_ST = WS_HH + 16 * 192 * 2048;
constexpr u32 WS_RS = WS_ST + 16 * 8 * 1024;
constexpr u32 WS_WB = WS_RS + 2048 * 16;
constexpr u32 WT_SLICE = 68 * 5120;
constexpr u32 WS_WT = WS_WB + 3072;
constexpr u32 WS_END = WS_WT + 8 * WT_SLICE;
constexpr u32 WS_STAMP = WS_END;
constexpr u64 TAG_STEP = 0x9E3779B97F4A7C15ull;

constexpr u32 LDS_BYTES = 163840;
constexpr u32 L_MISC = 161792;
constexpr u32 L_K = 0, KCH = 6144;
constexpr u32 L_V = 6 * KCH, VCH = 6208;
constexpr u32 L_WOB = L_MISC - 102400;
__device__ constexpr u32 gb_off(int c) { return (c & 3) == 2 ? 0 : ((c & 3) == 3 ? 24576 : ((c & 3) == 0 ? 65536 : 106496)); }
__device__ constexpr u32 ga_off(int c) { return c % 3 == 1 ? 49152 : (c % 3 == 2 ? 90112 : 131072); }
constexpr u32 L_ST = 147456;
constexpr u32 L_WB = 49152;
constexpr u32 L_RED = 24576;
constexpr u32 L_WARM = L_MISC + 1792;
constexpr u32 L_RT = 131072;
constexpr u32 SPIN_CAP = 1u << 15;
constexpr float LN_EPS = 1e-5f;

extern __shared__ __attribute__((aligned(16))) char smem[];

DI u32x4 lds128(u32 off) { return *(LAS const u32x4*)(smem + off); }
DI u32x2 ldstr(u32 off) { return __builtin_bit_cast(u32x2, __builtin_amdgcn_ds_read_tr16_b64_v4i16((LAS s16x4*)(smem + off))); }
DI u32x4 ldstr2(u32 lo, u32 hi) { const u32x2 a = ldstr(lo), b = ldstr(hi); return u32x4{a[0], a[1], b[0], b[1]}; }
DI f32x4 mfma(u32x4 a, u32x4 b, f32x4 c) { return __builtin_amdgcn_mfma_f32_16x16x32_bf16(__builtin_bit_cast(bf16x8, a), __builtin_bit_cast(bf16x8, b), c, 0, 0, 0); }
typedef float f32x16 __attribute__((ext_vector_type(16)));
DI f32x16 mfma32(u32x4 a, u32x4 b, f32x16 c) { return __builtin_amdgcn_mfma_f32_32x32x16_bf16(__builtin_bit_cast(bf16x8, a), __builtin_bit_cast(bf16x8, b), c, 0, 0, 0); }
DI float max3(float a, float b, float c) { return __builtin_fmaxf(__builtin_fmaxf(a, b), c); }
DI f32x4 mfma16(u32x2 a, u32x2 b, f32x4 c) { return __builtin_amdgcn_mfma_f32_16x16x16bf16_1k(__builtin_bit_cast(s16x4, a), __builtin_bit_cast(s16x4, b), c, 0, 0, 0); }
DI float dot2(u32 a, u32 b, float c) { return __builtin_amdgcn_fdot2_f32_bf16(__builtin_bit_cast(bf16x2, a), __builtin_bit_cast(bf16x2, b), c, false); }
DI u32 pack2(float lo, float hi) { const f32x2 v = {lo, hi}; return __builtin_bit_cast(u32, __builtin_convertvector(v, bf16x2)); }
DI float bflo(u32 v) { return __uint_as_float(v << 16); }
DI float bfhi(u32 v) { return __uint_as_float(v & 0xffff0000u); }
constexpr u32 ONES2 = 0x3f803f80u;
DI float sum8(u32x4 a, float s) { const u32 a0 = a[0], a1 = a[1], a2 = a[2], a3 = a[3]; return dot2(a3, ONES2, dot2(a2, ONES2, dot2(a1, ONES2, dot2(a0, ONES2, s)))); }
DI float sq8(u32x4 a, float s) { const u32 a0 = a[0], a1 = a[1], a2 = a[2], a3 = a[3]; return dot2(a3, a3, dot2(a2, a2, dot2(a1, a1, dot2(a0, a0, s)))); }
DI float gsum(float x) {
  auto a = __builtin_amdgcn_permlane16_swap(__float_as_uint(x), __float_as_uint(x), false, false);
  x = __uint_as_float(a[0]) + __uint_as_float(a[1]);
  auto b = __builtin_amdgcn_permlane32_swap(__float_as_uint(x), __float_as_uint(x), false, false);
  return __uint_as_float(b[0]) + __uint_as_float(b[1]);
}
DI float gmax(float x) {
  auto a = __builtin_amdgcn_permlane16_swap(__float_as_uint(x), __float_as_uint(x), false, false);
  x = __builtin_fmaxf(__uint_as_float(a[0]), __uint_as_float(a[1]));
  auto b = __builtin_amdgcn_permlane32_swap(__float_as_uint(x), __float_as_uint(x), false, false);
  return __builtin_fmaxf(__uint_as_float(b[0]), __uint_as_float(b[1]));
}
DI u32x4 quad2chunk(u32x2 A, u32x2 B) {
  const u32 a0 = A[0], a1 = A[1], b0 = B[0], b1 = B[1];
  auto s0 = __builtin_amdgcn_permlane16_swap(a0, b0, false, false);
  auto s1 = __builtin_amdgcn_permlane16_swap(a1, b1, false, false);
  return u32x4{s0[0], s1[0], s0[1], s1[1]};
}
DI void dma(const void* sbase, u32 voff, u32 lds) {
  asm volatile("s_mov_b32 m0, %2\n\ts_nop 0\n\tglobal_load_lds_dwordx4 %0, %1" ::"v"(voff), "s"(sbase), "s"(lds));
}
DI void dma_sc1(const void* sbase, u32 voff, u32 lds) {
  asm volatile("s_mov_b32 m0, %2\n\ts_nop 0\n\tglobal_load_lds_dwordx4 %0, %1 sc1" ::"v"(voff), "s"(sbase), "s"(lds));
}
DI void gld_sc1(u32x4& dst, const void* sbase, u32 voff) {
  asm volatile("global_load_dwordx4 %0, %1, %2 sc1" : "=v"(dst) : "v"(voff), "s"(sbase));
}
DI void gld(u32x4& dst, const void* sbase, u32 voff) {
  asm volatile("global_load_dwordx4 %0, %1, %2" : "=v"(dst) : "v"(voff), "s"(sbase));
}
DI void gld64(u32x2& dst, const void* sbase, u32 voff) {
  asm volatile("global_load_dwordx2 %0, %1, %2" : "=v"(dst) : "v"(voff), "s"(sbase));
}
template <int N> DI void vm_wait() { asm volatile("s_waitcnt vmcnt(%0)" ::"n"(N) : "memory"); }
template <int N> DI void vm_wait_use(u32x4& r) { asm volatile("s_waitcnt vmcnt(%1)" : "+v"(r) : "n"(N)); }
#define SB() __builtin_amdgcn_sched_barrier(0)
DI void warm(const void* sbase, u32 voff, u32 lds) {
  asm volatile("s_mov_b32 m0, %2\n\ts_nop 0\n\tglobal_load_lds_dword %0, %1" ::"v"(voff), "s"(sbase), "s"(lds));
}
DI void bar0() { asm volatile("s_barrier" ::: "memory"); }
DI u32x4 cat(u32x2 a, u32x2 b) { return u32x4{a[0], a[1], b[0], b[1]}; }
DI void bar() { asm volatile("s_waitcnt lgkmcnt(0)\n\ts_barrier" ::: "memory"); }
DI void st128_sc1(__amdgpu_buffer_rsrc_t rs, u32 off, u32x4 v) { __builtin_amdgcn_raw_buffer_store_b128(v, rs, (int)off, 0, 16); }
DI void st64_sc1(__amdgpu_buffer_rsrc_t rs, u32 off, u32x2 v) { __builtin_amdgcn_raw_buffer_store_b64(v, rs, (int)off, 0, 16); }

DI void poll_flags(const char* ws, u32 off, u64 tag) {
  GAS u64* a = (GAS u64*)(ws + off);
  for (u32 spins = 0; spins < SPIN_CAP; ++spins) {
    const u64 v = __hip_atomic_load(a, RLX_AGENT);
    if (__all(v == tag)) break;
  }
  __builtin_amdgcn_fence(__ATOMIC_ACQUIRE, "wavefront");
}
DI u32 flag_off(int phase, int p, int j) { return WS_FLAG + (u32)((phase * 256 + p * 16 + j) * 256); }

#define STAMP(i) do { if ((i) == 0) hold0 = __builtin_amdgcn_s_memrealtime(); if ((i) == HOLD_AT) { while (__builtin_amdgcn_s_memrealtime() - hold0 < 2500) __builtin_amdgcn_s_sleep(8); } } while (0)

__global__ __launch_bounds__(512, 2) void fused_block(const char* __restrict__ x, const char* __restrict__ wqkv, const char* __restrict__ wo,
                                                      const char* __restrict__ wg, const char* __restrict__ wu, const char* __restrict__ wd,
                                                      char* __restrict__ out, char* ws) {
  const int tid = threadIdx.x, lane = tid & 63, w = __builtin_amdgcn_readfirstlane(tid >> 6);
  const int r16 = lane & 15, g = lane >> 4, q4 = r16 >> 2, pq = r16 & 3;
  const int bx = blockIdx.x, xcd = bx & 7, rr = bx >> 3;
  const int p = (xcd >> 1) * 4 + (rr >> 3), j = (xcd & 1) * 8 + (rr & 7);
  const int pa = rr >> 1, ja = 2 * xcd + (rr & 1);
  const int pg = 8 * (xcd >> 2) + (rr >> 2), jg = 4 * (xcd & 3) + (rr & 3);
  const int half = j >> 3, slice = j & 7;
  constexpr int PW = 3;
  const __amdgpu_buffer_rsrc_t rs = __builtin_amdgcn_make_buffer_rsrc(ws, 0, WS_END, 0x00020000);
  u64 hold0;
  STAMP(0);
  u64 tag;
  auto load_epoch = [&](u32x2& e) { const u32 z = 0; asm volatile("global_load_dwordx2 %0, %1, %2 sc1" : "=v"(e) : "v"(z), "s"(ws + WS_EPOCH)); };
  auto make_tag = [&](u32x2& e) {
    asm volatile("" : "+v"(e));
    const u32 e0 = e[0], e1 = e[1];
    tag = (((u64)(u32)__builtin_amdgcn_readfirstlane(e1) << 32) | (u32)__builtin_amdgcn_readfirstlane(e0)) + TAG_STEP;
  };
  const char* xa = x + (size_t)p * 128 * 1280;
  const u32x4 ones8 = {ONES2, ONES2, ONES2, ONES2};
  const char* wt = ws + WS_WT + slice * WT_SLICE;
  auto rt_addr = [&](int piece, int gi, u32 slot, u32& l, u32& d) {
    const int tile = 4 * piece + g, sl = tile / 5, t = tile - sl * 5;
    l = slot + q4 * 1280 + tile * 32 + pq * 8;
    d = WS_WT + sl * WT_SLICE + (gi >> 2) * 5120 + t * 1024 + (gi & 3) * 256 + r16 * 16;
  };

  if (j < 15) {
    const int wm = w & 1, wn = w >> 1;
    u32 voffA[2], voffB[2];
#pragma unroll
    for (int i = 0; i < 2; ++i) {
      const int ra = 16 * w + 8 * i + (lane >> 3);
      voffA[i] = ra * 1280 + (((lane & 7) ^ ((ra >> 1) & 7)) << 4);
      const int rb = 8 * w + 4 * i + g, f = (rb & 3) | (((rb >> 3) & 1) << 2);
      voffB[i] = rb * 3840 + (((((r16 >> 1) ^ f) << 1) | (r16 & 1)) << 4);
    }
    const char* wb = wqkv + j * 256;
    const int rot = RG == 10 ? ((rr & 7) + ((rr & 7) >> 2) + ((5 * (rr >> 3)) >> 1)) % 10 : ((rr & 7) + (rr >> 3)) % RG;
    auto stage = [&](int c, int piece) {
      int kc = c % RG + rot; kc = (kc >= RG ? kc - RG : kc) + (c / RG) * RG;
      const u32 slot = (c & 3) * 32768;
      const char* a = xa + kc * 128; const char* b = wb + (size_t)kc * 64 * 3840;
      if (piece < 0 || piece == 0) dma(a, voffA[0], slot + w * 2048);
      if (piece < 0 || piece == 1) dma(a, voffA[1], slot + w * 2048 + 1024);
      if (piece < 0 || piece == 2) dma(b, voffB[0], slot + 16384 + w * 2048);
      if (piece < 0 || piece == 3) dma(b, voffB[1], slot + 16384 + w * 2048 + 1024);
    };
    asm volatile("" :: "s"(xa), "s"(wb), "s"(ws));
    STAMP(40);
    stage(0, -1);
    u32x2 epoch;
    load_epoch(epoch);
    {
      u64 pc; asm volatile("s_getpc_b64 %0" : "=s"(pc));
      if (w == 0) warm((const char*)(pc & ~127ull) + rr * 1280, (lane < 10 ? lane : 9) * 128, L_WARM);
    }
    stage(1, -1);
    u32 adA[2], adB[2];
#pragma unroll
    for (int ks = 0; ks < 2; ++ks) adA[ks] = (64 * wm + r16) * 128 + (((4 * ks + g) ^ ((r16 >> 1) & 7)) << 4);
#pragma unroll
    for (int n = 0; n < 2; ++n) adB[n] = 16384 + (8 * g + q4) * 256 + (((2 * wn + (n ^ wm)) ^ (q4 | ((g & 1) << 2))) << 5) + pq * 8;
    f32x4 acc[4][2], csum = {0, 0, 0, 0};
#pragma unroll
    for (int m = 0; m < 4; ++m) { acc[m][0] = f32x4{0, 0, 0, 0}; acc[m][1] = f32x4{0, 0, 0, 0}; }
    u32x4 fa[2][4]; u32x2 fb[2][2][2];
    u32x4 rsg[4];
    auto ld_rs = [&]() {
#pragma unroll
      for (int m = 0; m < 4; ++m) gld_sc1(rsg[m], ws + WS_RS + (p * 128 + 64 * wm + 16 * m) * 16, r16 * 16);
    };
    auto rdp = [&](int c, int ks, int i) {
      const u32 slot = (c & 3) * 32768;
      if (i == 0 || i == 1) fb[ks][0][i] = ldstr(slot + adB[0] + ks * 8192 + i * 1024);
      else if (i == 2) fa[ks][0] = lds128(slot + adA[ks]);
      else if (i == 3 || i == 4) fb[ks][1][i - 3] = ldstr(slot + adB[1] + ks * 8192 + (i - 3) * 1024);
      else if (i < 8) fa[ks][i - 4] = lds128(slot + adA[ks] + (i - 4) * 2048);
    };
    auto mm = [&](int ks, int cr, int cn) {
      const u32x4 b0 = cat(fb[ks][0][0], fb[ks][0][1]), b1 = cat(fb[ks][1][0], fb[ks][1][1]);
#pragma unroll
      for (int i = 0; i < 9; ++i) {
        if (i < 8) acc[i >> 1][i & 1] = mfma((i & 1) ? b1 : b0, fa[ks][i >> 1], acc[i >> 1][i & 1]);
        else csum = mfma(b0, ones8, csum);
        if (cr < 10) rdp(cr, ks ^ 1, i);
        if (cn < 10 && i == 4) stage(cn, 2 * (ks ^ 1));
        if (cn < 10 && i == 8) stage(cn, 2 * (ks ^ 1) + 1);
        SB();
      }
    };
    if (tid < 8) *(LAS u32*)(smem + L_MISC + tid * 4) = 0;
    vm_wait<4>();
    make_tag(epoch);
    bar();
    STAMP(16);
#pragma unroll
    for (int i = 0; i < 8; ++i) rdp(0, 0, i);
    SB();
    stage(2, 0); stage(2, 1);
    SB();
#pragma unroll
    for (int c = 0; c < 10; ++c) {
      mm(0, c, c + 2);
      if (c < 9) {
        if (c <= 7) vm_wait<4>(); else vm_wait<0>();
        bar0();
        if (c == 8) {
          ld_rs();
          if (j < 12 && w < 5) {
            dma(wd + (p * 12 + j) * 10240 + w * 2048, lane * 16, L_RT + w * 2048);
            dma(wd + (p * 12 + j) * 10240 + w * 2048 + 1024, lane * 16, L_RT + w * 2048 + 1024);
          }
          SB();
        }
      }
      mm(1, c + 1, c + 3);
    }
    STAMP(1);
    if (r16 == 0) *(LAS f32x4*)(smem + L_MISC + 1024 + (32 * wn + 16 * wm + 4 * g) * 4) = csum;
    asm volatile("s_waitcnt lgkmcnt(0)" ::: "memory");
    if (lane == 0) *(volatile LAS u32*)(smem + L_MISC + w * 4) = 1;
    if (j < 12 && w < 5) vm_wait<2>(); else vm_wait<0>();
    for (u32 spins = 0; spins < SPIN_CAP; ++spins) {
      asm volatile("" : "+v"(rsg[0]), "+v"(rsg[1]), "+v"(rsg[2]), "+v"(rsg[3]));
      bool ok = true;
#pragma unroll
      for (int m = 0; m < 4; ++m) ok = ok && rsg[m][1] == (u32)tag && rsg[m][3] == (u32)(tag >> 32);
      if (__all(ok)) break;
      ld_rs();
      vm_wait<0>();
    }
    asm volatile("" : "+v"(rsg[0]), "+v"(rsg[1]), "+v"(rsg[2]), "+v"(rsg[3]));
    while (*(volatile LAS u32*)(smem + L_MISC + (w ^ 1) * 4) == 0) {}
    asm volatile("" ::: "memory");
    STAMP(17);
    const float qs = j < 5 ? 0.22360679774997896f * 1.4426950408889634f : 1.f;
    f32x4 cs[2];
#pragma unroll
    for (int n = 0; n < 2; ++n) cs[n] = n == 0 ? csum : *(LAS const f32x4*)(smem + L_MISC + 1024 + (32 * wn + 16 * (1 ^ wm) + 4 * g) * 4);
    const u32 qb = WS_QKV + (p * 240 + 16 * j + 4 * wn + 2 * ((g & 1) ^ wm) + (g >> 1)) * 2048 + (64 * wm + r16) * 16;
#pragma unroll
    for (int m = 0; m < 4; ++m) {
      const u32 um = rsg[m][0], ur = rsg[m][2];
      const float mean = __uint_as_float(um), sc = __uint_as_float(ur) * qs;
      u32x2 pk[2];
#pragma unroll
      for (int n = 0; n < 2; ++n) {
        const f32x4 v = (acc[m][n] - mean * cs[n]) * sc;
        pk[n] = u32x2{pack2(v[0], v[1]), pack2(v[2], v[3])};
      }
      st128_sc1(rs, qb + m * 256, quad2chunk(pk[0], pk[1]));
    }
  } else {
    asm volatile("" :: "s"(xa), "s"(wo), "s"(ws));
    STAMP(40);
    u32x2 epoch;
    load_epoch(epoch);
    if (w < 5) {
#pragma unroll
      for (int i = 0; i < 10; ++i) dma(wo + (p * 5 + w) * 10240 + i * 1024, lane * 16, w * 10240 + i * 1024);
    }
    u32x4 xv[2][10];
#pragma unroll
    for (int t = 0; t < 10; ++t)
#pragma unroll
      for (int s2 = 0; s2 < 2; ++s2) gld(xv[s2][t], xa + (16 * w + 8 * s2) * 1280 + t * 128, (lane >> 3) * 1280 + (lane & 7) * 16);
    float s1[2] = {0.f, 0.f}, sq[2] = {0.f, 0.f};
#pragma unroll
    for (int t = 0; t < 10; ++t) {
      if (t == 0) vm_wait<10>();
      if (t == 5) vm_wait<4>();
      if (t == 8) vm_wait<0>();
#pragma unroll
      for (int s2 = 0; s2 < 2; ++s2) { asm volatile("" : "+v"(xv[s2][t])); s1[s2] = sum8(xv[s2][t], s1[s2]); sq[s2] = sq8(xv[s2][t], sq[s2]); }
    }
    make_tag(epoch);
#pragma unroll
    for (int o = 1; o < 8; o <<= 1) { s1[0] += __shfl_xor(s1[0], o); sq[0] += __shfl_xor(sq[0], o); s1[1] += __shfl_xor(s1[1], o); sq[1] += __shfl_xor(sq[1], o); }
    {
      const float a1 = (lane & 1) ? s1[1] : s1[0], a2 = (lane & 1) ? sq[1] : sq[0];
      const float mean = a1 * (1.f / 640.f), var = __builtin_fmaxf(a2 * (1.f / 640.f) - mean * mean, 0.f);
      if ((lane & 7) < 2)
        st128_sc1(rs, WS_RS + (p * 128 + 16 * w + 8 * (lane & 1) + (lane >> 3)) * 16, u32x4{__float_as_uint(mean), (u32)tag, __float_as_uint(__builtin_amdgcn_rsqf(var + LN_EPS)), (u32)(tag >> 32)});
    }

    bar();
    {
#pragma unroll
      for (int i = 0; i < 7; ++i) {
        const int id = w + 8 * i, gr = id / 10;
        if (id < 50) { u32 l, d; rt_addr(id - 10 * gr, p * 5 + gr, gr * 10240, l, d); st128_sc1(rs, d, ldstr2(l, l + 5120)); }
      }
      if (w >= 3) {
        float a1 = 0.f;
#pragma unroll
        for (int t = 0; t < 10; ++t) a1 = sum8(lds128((w - 3) * 10240 + (lane >> 3) * 1280 + t * 128 + (lane & 7) * 16), a1);
#pragma unroll
        for (int o = 1; o < 8; o <<= 1) a1 += __shfl_xor(a1, o);
        a1 *= (1.f / 640.f);
        const u32 hi = pack2(a1, 0.f), lo = pack2(a1 - bflo(hi), 0.f);
        if ((lane & 7) < 2) __builtin_amdgcn_raw_buffer_store_b16((u16)((lane & 1) ? lo : hi), rs, (int)(WS_WB + (lane & 1) * 1280 + ((p * 5 + w - 3) * 8 + (lane >> 3)) * 2), 0, 16);
      }
    }
  }
  STAMP(18);
  vm_wait<0>();
  bar();
  STAMP(2);
  if (w == PW && lane == 0) __hip_atomic_store((GAS u64*)(ws + flag_off(0, p, j)), tag, RLX_AGENT);
  asm volatile("" ::: "memory");
  u32 kvsrc[8], kvdst[8];
#pragma unroll
  for (int i = 0; i < 8; ++i) {
    const int id = w + 8 * i, kv = id >= 30, r = id - 30 * kv, c = r / 6, r2 = r - 6 * c;
    int pp = pa - 2 + (r2 >> 1); pp = pp < 0 ? 0 : pp;
    kvsrc[i] = WS_QKV + (pp * 240 + 80 * (1 + kv) + 5 * ja + c) * 2048 + (r2 & 1) * 1024;
    kvdst[i] = (kv ? L_V + c * VCH : L_K + c * KCH) + r2 * 1024;
    asm volatile("" : "+s"(kvsrc[i]), "+s"(kvdst[i]));
  }
  if (tid < 384) {
    const bool valid = 128 * (pa - 2) + tid >= 0;
    *(LAS u32x4*)(smem + L_K + 5 * KCH + tid * 16) = u32x4{valid ? 0u : 0xf149u, 0u, ONES2, ONES2};
  }
  u32 rtl[2], rtg[2];
#pragma unroll
  for (int i = 0; i < 2; ++i) { rt_addr(2 * w + i, 80 + p * 12 + j, L_RT, rtl[i], rtg[i]); asm volatile("" : "+v"(rtl[i]), "+v"(rtg[i])); }
  u32 fl_put, fl_get;
  {
    const int e = r16 < 14 ? r16 : 0, sec = e < 2 ? 0 : (e < 8 ? 1 : 2), i = e < 2 ? e : (e - 2) % 6;
    int pp = e < 2 ? pa : pa - (i >> 1); pp = pp < 0 ? 0 : pp;
    fl_get = (g & 1) ? flag_off(0, r16, 15) : flag_off(0, pp, (80 * sec + 5 * ja + 4 * (i & 1)) >> 4);
    fl_put = lane == 0 ? flag_off(1, pa, ja) : WS_DENSE + bx * 8;
    asm volatile("" : "+v"(fl_get), "+v"(fl_put));
  }
  if (w == PW) poll_flags(ws, fl_get, tag);
  bar();
  STAMP(3);

  u32x2 xr[6];
  u32 voffG[3];
  {
    const int h = w & 1, qt = w >> 1, l32 = lane & 31, hh = lane >> 5;
    u32x4 qf[2];
    gld_sc1(qf[0], ws + WS_QKV + (pa * 240 + 5 * ja + 2 * h) * 2048 + qt * 512, hh * 2048 + l32 * 16);
    gld_sc1(qf[1], ws + WS_QKV + (pa * 240 + 5 * ja + 2 * h + 2) * 2048 + qt * 512, l32 * 16);
#pragma unroll
    for (int i = 0; i < 5; ++i) dma_sc1(ws + kvsrc[i], lane * 16, kvdst[i]);
    u32 dm[8];
#pragma unroll
    for (int m = 0; m < 8; ++m) {
      const int k0 = ((2 * m) & 3) + 8 * (m >> 1) + 4 * hh;
      dm[m] = (k0 <= l32 ? 0xffffu : 0u) | (k0 + 1 <= l32 ? 0xffff0000u : 0u);
      asm volatile("" : "+v"(dm[m]));
    }
    u32 adK0 = L_K + (2 * h + hh) * KCH + (32 * qt + l32) * 16, adK1 = L_K + (hh ? 5 : 2 * h + 2) * KCH + (32 * qt + l32) * 16;
    const u32 vrow = 32 * qt + 4 * hh + q4;
    u32 adV = (g & 1) == 0 ? L_V + (3 * h + (pq >> 1)) * VCH + vrow * 16 + (pq & 1) * 8
                           : (pq == 0 ? L_V + 2 * VCH + vrow * 16 + 8 * h : L_K + 5 * KCH + ((4 * hh + (h ? 4 : 12)) & 15) * 16 + 8);
    u32 voffwarm;
    { const int l6 = lane < 30 ? lane : 29, rw = l6 / 6; voffwarm = rw * 3072 + (l6 - rw * 6) * 128; }
#pragma unroll
    for (int i = 0; i < 3; ++i) {
      const int s = 64 * i + lane, rw = s / 12, cp = s - rw * 12;
      int c = cp - (w & 1) - 2 * ((rw >> 3) & 1); c = c < 0 ? c + 12 : c;
      voffG[i] = rw * 3072 + c * 16;
    }
    asm volatile("" : "+v"(voffwarm), "+v"(adK0), "+v"(adK1), "+v"(adV), "+v"(voffG[0]), "+v"(voffG[1]), "+v"(voffG[2]));
    vm_wait<1>();
    asm volatile("" : "+v"(qf[0]), "+v"(qf[1]));
    bar();
    STAMP(4);
    const f32x16 zero16 = {0, 0, 0, 0, 0, 0, 0, 0, 0, 0, 0, 0, 0, 0, 0, 0};
    u32x4 kf[9][2];
    constexpr int ord[9] = {0, 8, 1, 2, 3, 4, 5, 6, 7};
#pragma unroll
    for (int n = 0; n < 4; ++n) { kf[ord[n]][0] = lds128(adK0 + 512 * ord[n]); kf[ord[n]][1] = lds128(adK1 + 512 * ord[n]); }
    SB();
#pragma unroll
    for (int i = 5; i < 8; ++i)
      if (i < 7 || w < 4) dma_sc1(ws + kvsrc[i], lane * 16, kvdst[i]);
    if (h == 1 && hh == 0) { qf[0][0] = 0; qf[0][1] = 0; }
    if (h == 0) { qf[1][2] = 0; qf[1][3] = 0; }
    if (hh == 1) qf[1] = u32x4{0x3f80u, 0, 0, 0};
    SB();
    f32x16 sc[9];
    float m4[4];
    auto qk = [&](int i, int st) { sc[i] = mfma32(kf[i][st], qf[st], st == 0 ? zero16 : sc[i]); };
    qk(0, 0); qk(8, 0); qk(0, 1); qk(8, 1);
    SB();
#pragma unroll
    for (int n = 2; n < 11; ++n) {
      if (n < 9) { qk(ord[n], 0); SB(); }
      if (n + 2 < 9) { kf[ord[n + 2]][0] = lds128(adK0 + 512 * ord[n + 2]); SB(); }
      if (n == 3) {
#pragma unroll
        for (int r = 6; r < 16; ++r) sc[0][r] = ((r & 3) + 8 * (r >> 2) + 4 * hh <= l32) ? sc[8][r] : sc[0][r];
      } else if (n > 3) {
#pragma unroll
        for (int c = 0; c < 4; ++c) m4[c] = max3(m4[c], sc[n - 3][2 * c], sc[n - 3][2 * c + 1]);
      }
      SB();
      if (n < 9) { qk(ord[n], 1); SB(); }
      if (n + 2 < 9) { kf[ord[n + 2]][1] = lds128(adK1 + 512 * ord[n + 2]); SB(); }
      if (n == 2) {
#pragma unroll
        for (int r = 0; r < 6; ++r) sc[0][r] = ((r & 3) + 8 * (r >> 2) + 4 * hh <= l32) ? sc[8][r] : sc[0][r];
      } else if (n == 3) {
#pragma unroll
        for (int c = 0; c < 4; ++c) m4[c] = max3(max3(sc[0][4 * c], sc[0][4 * c + 1], sc[0][4 * c + 2]), sc[0][4 * c + 3], sc[0][4 * c + 3]);
      } else {
#pragma unroll
        for (int c = 0; c < 4; ++c) m4[c] = max3(m4[c], sc[n - 3][8 + 2 * c], sc[n - 3][9 + 2 * c]);
      }
      SB();
    }
    float mx = __builtin_fmaxf(max3(m4[0], m4[1], m4[2]), m4[3]);
    { auto sw = __builtin_amdgcn_permlane32_swap(__float_as_uint(mx), __float_as_uint(mx), false, false); mx = __builtin_fmaxf(__uint_as_float(sw[0]), __uint_as_float(sw[1])); }
    u32x4 nmf;
    {
      const float nmx = -mx;
      const u32 p0 = __float_as_uint(nmx) & 0xffff0000u;
      const float r1 = nmx - __uint_as_float(p0);
      const u32 p1 = __float_as_uint(r1) & 0xffff0000u;
      const u32 p2 = __float_as_uint(r1 - __uint_as_float(p1));
      nmf = u32x4{hh ? p2 >> 16 : (p0 >> 16) | p1, 0, 0, 0};
    }
    auto submax = [&](int i) { sc[i] = mfma32(ones8, nmf, sc[i]); };
    submax(0); submax(1);
    SB();
    u32x4 rt0, rt1;
    if (j < 12 && w < 5) { rt0 = ldstr2(rtl[0], rtl[0] + 5120); rt1 = ldstr2(rtl[1], rtl[1] + 5120); }
    STAMP(29);
    vm_wait<0>();
    bar();
    STAMP(26);
    if (w >= 4) {
#pragma unroll
      for (int t = 0; t < 5; ++t) gld64(xr[t], xa + (64 * half + 16 * (w - 4)) * 1280 + slice * 160 + t * 32, r16 * 1280 + g * 8);
      asm volatile("global_load_dwordx2 %0, %1, %2 sc1" : "=v"(xr[5]) : "v"(r16 * 16), "s"(ws + WS_RS + (p * 128 + 64 * half + 16 * (w - 4)) * 16));
    } else {
#pragma unroll
      for (int t = 0; t < 6; ++t) asm volatile("v_mov_b64 %0, 0" : "=v"(xr[t]));
    }
    if (j < 12 && w < 5) { st128_sc1(rs, rtg[0], rt0); st128_sc1(rs, rtg[1], rt1); }
    warm(((w & 1) ? wu : wg) + (size_t)(rr * 20 + (w >> 1) * 5) * 3072 + (xcd & 3) * 768, voffwarm, L_WARM);
    SB();
    f32x16 o = zero16;
    u32 pw[2][8];
    u32x2 vf[2][4], vx[4];
    auto rdV = [&](int i, int e) { return ldstr(adV + 512 * i + 128 * e); };
    auto pfrag = [&](const u32* q, int st) { return u32x4{q[4 * st], q[4 * st + 1], q[4 * st + 2], q[4 * st + 3]}; };
    u32 p8[8];
#pragma unroll
    for (int n = 0; n < 8; ++n) {
      float ex[16];
#pragma unroll
      for (int e = 0; e < 8; ++e) {
        ex[2 * e] = __builtin_amdgcn_exp2f(sc[n][2 * e]); SB();
        if (e < 4) { vf[n & 1][e] = rdV(n, e); SB(); }
        else if (n == 0) { vx[e - 4] = rdV(8, e - 4); SB(); }
        ex[2 * e + 1] = __builtin_amdgcn_exp2f(sc[n][2 * e + 1]); SB();
        if (e > 0) { pw[n & 1][e - 1] = pack2(ex[2 * e - 2], ex[2 * e - 1]); SB(); }
        if (n >= 1) {
          if (e == 0 && n < 7) { submax(n + 1); SB(); }
          if (e == 3) { o = mfma32(cat(vf[(n - 1) & 1][0], vf[(n - 1) & 1][1]), pfrag(pw[(n - 1) & 1], 0), o); SB(); }
          if (e == 6) { o = mfma32(cat(vf[(n - 1) & 1][2], vf[(n - 1) & 1][3]), pfrag(pw[(n - 1) & 1], 1), o); SB(); }
          if (n == 2 && e == 2) { o = mfma32(cat(vx[0], vx[1]), pfrag(p8, 0), o); SB(); }
          if (n == 2 && e == 5) { o = mfma32(cat(vx[2], vx[3]), pfrag(p8, 1), o); SB(); }
        }
      }
      pw[n & 1][7] = pack2(ex[14], ex[15]);
      if (n == 0) {
#pragma unroll
        for (int m = 0; m < 8; ++m) { p8[m] = pw[0][m] & dm[m]; pw[0][m] &= ~dm[m]; }
      }
      SB();
      if (n == 3) STAMP(27);
    }
    o = mfma32(cat(vf[1][0], vf[1][1]), pfrag(pw[1], 0), o);
    o = mfma32(cat(vf[1][2], vf[1][3]), pfrag(pw[1], 1), o);
    const float inv = __builtin_amdgcn_rcpf(o[12]);
    const u32 x0 = pack2(o[0] * inv, o[1] * inv), x1 = pack2(o[2] * inv, o[3] * inv), y0 = pack2(o[4] * inv, o[5] * inv), y1 = pack2(o[6] * inv, o[7] * inv);
    auto s0 = __builtin_amdgcn_permlane32_swap(x0, y0, false, false);
    auto s1 = __builtin_amdgcn_permlane32_swap(x1, y1, false, false);
    const u32 ob = WS_O + (pa * 80 + 5 * ja) * 2048 + (32 * qt + l32) * 16;
    st128_sc1(rs, ob + (3 * h + hh) * 2048, u32x4{s0[0], s1[0], s0[1], s1[1]});
    if (hh == 0) st64_sc1(rs, ob + 2 * 2048 + 8 * h, u32x2{pack2(o[8] * inv, o[9] * inv), pack2(o[10] * inv, o[11] * inv)});
    STAMP(5);
  }
  const char* gsrc = ((w & 1) ? wu : wg) + jg * 192;
  auto stageGBp = [&](int c, int rb, int i) {
    dma(gsrc + (size_t)(c * 64 + rb * 16) * 3072, voffG[i], gb_off(c) + (w & 1) * 12288 + rb * 3072 + i * 1024);
  };
  auto stageGB = [&](int c, int rb) { stageGBp(c, rb, 0); stageGBp(c, rb, 1); stageGBp(c, rb, 2); };
  const int wm4 = w & 3, wk = w >> 2;
  u32 voffR = g * 2048 + (64 * half + 16 * wm4 + r16) * 16;
  fl_get = flag_off(1, p, r16);
  u32 fl_get2 = flag_off(2, pg, r16);
  asm volatile("" : "+v"(voffR), "+v"(fl_get), "+v"(fl_get2));
  asm volatile("" ::: "memory");
  if (w < 4) {
#pragma unroll
    for (int i = 0; i < 20; ++i) dma_sc1(wt + 20480 * (1 + w) + i * 1024, lane * 16, L_WOB + 20480 * (1 + w) + i * 1024);
  }
  bar();
  if (w < 4) {
#pragma unroll
    for (int i = 0; i < 5; ++i) dma_sc1(wt + 5120 * w + i * 1024, lane * 16, L_WOB + 5120 * w + i * 1024);
    dma_sc1(ws + WS_WB + (w < 3 ? w : 2) * 1024, lane * 16, L_WB + (w < 3 ? w : 2) * 1024);
    vm_wait<6>();
  } else {
    stageGB(2, w >> 1); stageGB(2, (w >> 1) - 2);
    vm_wait<6>();
  }
  bar0();
  STAMP(6);
  u32x2 hp[5];
  {
    if (w == PW && lane < 2) __hip_atomic_store((GAS u64*)(ws + fl_put), tag, RLX_AGENT);
    u32x4 a[10], b[6][5];
    const u32 ad0 = L_WOB + wk * 10 * 5120 + lane * 16, adm = L_WB + (r16 & 1) * 1280 + wk * 640 + g * 16;
    auto rdB = [&](int s, int t) { b[s % 6][t] = lds128(ad0 + ((s + 4) % 10) * 5120 + t * 1024); };
#pragma unroll
    for (int s = 0; s < 6; ++s)
#pragma unroll
      for (int t = 0; t < 5; ++t) rdB(s, t);
    asm volatile("" : "+v"(xr[0]), "+v"(xr[1]), "+v"(xr[2]), "+v"(xr[3]), "+v"(xr[4]), "+v"(xr[5]));
    f32x4 hacc[5], macc;
    { const u32 xm = xr[5][0]; macc = f32x4{__uint_as_float(xm), 0.f, 0.f, 0.f}; }
#pragma unroll
    for (int t = 0; t < 5; ++t) { const u32 a0 = xr[t][0], a1 = xr[t][1]; hacc[t] = f32x4{bflo(a0), bfhi(a0), bflo(a1), bfhi(a1)}; }
    SB();
    if (w == PW) poll_flags(ws, fl_get, tag); else if (w < 4) vm_wait<0>();
    bar();
    STAMP(7);
    const char* osrc = ws + WS_O + (p * 80 + 40 * wk) * 2048;
#pragma unroll
    for (int s = 0; s < 10; ++s) gld_sc1(a[s], osrc + ((s + 4) % 10) * 8192, voffR);
    u32x4 bm[2];
    bm[0] = lds128(adm + 4 * 64);
    SB();
#pragma unroll
    for (int s = 0; s < 10; ++s) {
      switch (s) {
        case 0: vm_wait_use<9>(a[0]); break; case 1: vm_wait_use<8>(a[1]); break; case 2: vm_wait_use<7>(a[2]); break;
        case 3: vm_wait_use<6>(a[3]); break; case 4: vm_wait_use<5>(a[4]); break; case 5: vm_wait_use<4>(a[5]); break;
        case 6: vm_wait_use<3>(a[6]); break; case 7: vm_wait_use<2>(a[7]); break; case 8: vm_wait_use<1>(a[8]); break;
        default: vm_wait_use<0>(a[9]); break;
      }
      if (s == 0) STAMP(28);
#pragma unroll
      for (int t = 0; t < 5; ++t) {
        hacc[t] = mfma(b[s % 6][t], a[s], hacc[t]);
        SB();
        if (s + 6 < 10) { rdB(s + 6, t); SB(); }
        if (t == 0 && s < 9) { bm[(s + 1) & 1] = lds128(adm + ((s + 5) % 10) * 64); SB(); }
      }
      macc = mfma(bm[s & 1], a[s], macc);
      SB();
    }
    STAMP(8);
    if (wk == 0) {
#pragma unroll
      for (int t = 0; t < 5; ++t) *(LAS f32x4*)(smem + L_RED + (wm4 * 5 + t) * 1024 + lane * 16) = hacc[t];
      *(LAS float*)(smem + L_RED + 20480 + wm4 * 256 + lane * 4) = macc[0] + macc[1];
    }
    bar();
    if (wk == 1) {
      float s1 = 0.f, s2 = 0.f;
      const float mean = macc[0] + macc[1] + *(LAS const float*)(smem + L_RED + 20480 + wm4 * 256 + lane * 4);
      u32x2 hc[5];
#pragma unroll
      for (int t = 0; t < 5; ++t) {
        const f32x4 v = hacc[t] + *(LAS const f32x4*)(smem + L_RED + (wm4 * 5 + t) * 1024 + lane * 16);
        const u32 lo = pack2(v[0], v[1]), hi = pack2(v[2], v[3]);
        hp[t] = u32x2{lo, hi};
        hc[t] = u32x2{pack2(bflo(lo) - mean, bfhi(lo) - mean), pack2(bflo(hi) - mean, bfhi(hi) - mean)};
      }
      const u32 hb = WS_H + (p * 80 + 10 * slice + 2 * (g & 1) + (g >> 1)) * 2048 + (64 * half + 16 * wm4 + r16) * 16;
      st128_sc1(rs, hb, quad2chunk(hc[0], hc[1]));
      st128_sc1(rs, hb + 4 * 2048, quad2chunk(hc[2], hc[3]));
      const u32x4 last = quad2chunk(hc[4], hc[4]);
      if ((g & 1) == 0) st128_sc1(rs, hb + 8 * 2048, last);
#pragma unroll
      for (int t = 0; t < 5; ++t) { const u32 lo = hc[t][0], hi = hc[t][1]; s1 = dot2(hi, ONES2, dot2(lo, ONES2, s1)); s2 = dot2(hi, hi, dot2(lo, lo, s2)); }
      s1 = gsum(s1); s2 = gsum(s2);
      if (g == 0) st64_sc1(rs, WS_ST + (p * 8 + slice) * 1024 + (64 * half + 16 * wm4 + r16) * 8, u32x2{__float_as_uint(s1), __float_as_uint(s2)});
      vm_wait<0>();
    } else {
#pragma unroll
      for (int t = 0; t < 5; ++t) hp[t] = u32x2{0, 0};
      stageGB(0, w >> 1); stageGB(0, 2 + (w >> 1)); stageGB(1, w >> 1); stageGB(1, 2 + (w >> 1));
    }
  }
  bar();
  STAMP(9);
  if (w == PW) {
    if (lane == 0) __hip_atomic_store((GAS u64*)(ws + flag_off(2, p, j)), tag, RLX_AGENT);
    poll_flags(ws, fl_get2, tag);
  } else if (w == 2) {
    GAS u64* a = (GAS u64*)(ws + WS_DENSE + lane * 8);
    for (u32 spins = 0; spins < SPIN_CAP; ++spins) {
      const u64 v0 = __hip_atomic_load(a, RLX_AGENT), v1 = __hip_atomic_load(a + 64, RLX_AGENT), v2 = __hip_atomic_load(a + 128, RLX_AGENT), v3 = __hip_atomic_load(a + 192, RLX_AGENT);
      if (__all(v0 == tag && v1 == tag && v2 == tag && v3 == tag)) break;
    }
    if (bx == 0 && lane == 0) __hip_atomic_store((GAS u64*)(ws + WS_EPOCH), tag, RLX_AGENT);
  } else if (wk == 1) {
    stageGB(3, w >> 1); stageGB(3, (w >> 1) - 2);
  }
  bar();
  STAMP(10);

  const int rotd = (((slice * 6) >> 3) + ((3 * (rr >> 3)) >> 1)) % 6;
  auto stageDq = [&](int c, int blk, int i) {
    int kc = c + rotd; kc = kc >= 6 ? kc - 6 : kc;
    dma_sc1(wt + (20 + kc * 8 + blk) * 5120 + i * 1024, lane * 16, (c & 3) * 40960 + blk * 5120 + i * 1024);
  };
  auto stageDp = [&](int c, int i) { stageDq(c, w, i); };
  auto stageD = [&](int c) {
#pragma unroll
    for (int i = 0; i < 5; ++i) stageDp(c, i);
  };

  {
    const int wm = w >> 2, wn = w & 3;
    const char* hsrc = ws + WS_H + (pg * 80 + w) * 2048;
    auto stageGAp = [&](int c, int i) { dma_sc1(hsrc + c * 16384 + i * 1024, lane * 16, ga_off(c) + w * 2048 + i * 1024); };
    auto stageGA = [&](int c) { stageGAp(c, 0); stageGAp(c, 1); };
    stageGA(0);
    dma_sc1(ws + WS_ST + (pg * 8 + w) * 1024, lane * 16, L_ST + w * 1024);
    stageGA(1);
    const u32 adA = g * 2048 + (64 * wm + r16) * 16;
    u32 adB[3];
#pragma unroll
    for (int t = 0; t < 3; ++t) {
      const int mat = t == 0 ? 0 : (t == 1 ? 1 : (pq >> 1)), c = t < 2 ? 3 * wn + (pq >> 1) : 3 * wn + 2;
      int cp = c + mat + 2 * (g & 1); cp = cp >= 12 ? cp - 12 : cp;
      adB[t] = mat * 12288 + (8 * g + q4) * 192 + cp * 16 + (pq & 1) * 8;
    }
    f32x4 acc[4][3];
#pragma unroll
    for (int m = 0; m < 4; ++m)
#pragma unroll
      for (int t = 0; t < 3; ++t) acc[m][t] = f32x4{0, 0, 0, 0};
    u32x4 fa[2][4]; u32x2 fb[2][3][2];
    auto rdp = [&](int c, int s, int i) {
      const u32 sa = ga_off(c) + s * 8192, sb = gb_off(c) + s * 6144;
      if (i < 2) fb[s][0][i] = ldstr(sb + adB[0] + i * 768);
      else if (i == 2) fa[s][0] = lds128(sa + adA);
      else if (i < 7) fb[s][1 + ((i - 3) >> 1)][(i - 3) & 1] = ldstr(sb + adB[1 + ((i - 3) >> 1)] + ((i - 3) & 1) * 768);
      else if (i < 10) fa[s][i - 6] = lds128(sa + adA + (i - 6) * 256);
    };
    auto mm = [&](int s, int c) {
      u32x4 b[3];
#pragma unroll
      for (int t = 0; t < 3; ++t) b[t] = cat(fb[s][t][0], fb[s][t][1]);
      const int cr = s == 0 ? c : c + 1;
#pragma unroll
      for (int i = 0; i < 12; ++i) {
        acc[i / 3][i % 3] = mfma(b[i % 3], fa[s][i / 3], acc[i / 3][i % 3]);
        if (cr < 10) rdp(cr, s ^ 1, i);
        if (s == 1 && (i == 4 || i == 10) && c + 3 < 10) stageGAp(c + 3, i == 10);
        if (s == 0 && i % 4 == 3 && c >= 1 && c + 3 < 10) stageGBp(c + 3, w >> 1, i >> 2);
        if (s == 1 && (c == 7 || c == 8) && i % 2 == 1 && i < 10) stageDp(c - 7, i >> 1);
        SB();
      }
    };
    vm_wait<3>();
    bar();
    STAMP(19);
#pragma unroll
    for (int i = 0; i < 10; ++i) rdp(0, 0, i);
    SB();
    stageGA(2);
    SB();
#pragma unroll
    for (int c = 0; c < 10; ++c) {
      if (c == 1) {
        const f32x2 u0 = *(LAS const f32x2*)(smem + L_ST + (2 * g) * 1024 + (16 * w + r16) * 8), u1 = *(LAS const f32x2*)(smem + L_ST + (2 * g + 1) * 1024 + (16 * w + r16) * 8);
        const float s1 = gsum(u0[0] + u1[0]), s2 = gsum(u0[1] + u1[1]);
        const float mean = s1 * (1.f / 640.f), var = __builtin_fmaxf(s2 * (1.f / 640.f) - mean * mean, 0.f);
        if (g == 0) *(LAS float*)(smem + L_MISC + (16 * w + r16) * 4) = __builtin_amdgcn_rcpf(var + LN_EPS);
      }
      mm(0, c);
      if (c < 9) {
        if (c == 0) vm_wait<2>(); else if (c == 1) vm_wait<5>(); else if (c <= 6) vm_wait<8>(); else vm_wait<5>();
        bar();
        if (c == 4) STAMP(20);
        if (c == 7) STAMP(30);
      }
      mm(1, c);
    }
    STAMP(11);
    u32x2 p01[4];
    float r2[4];
#pragma unroll
    for (int m = 0; m < 4; ++m) r2[m] = *(LAS const float*)(smem + L_MISC + (64 * wm + 16 * m + r16) * 4);
    if (wm == 1) { vm_wait<5>(); bar(); }
#pragma unroll
    for (int m = 0; m < 4; ++m) {
      const f32x4 v = acc[m][0] * acc[m][1] * r2[m];
      p01[m] = u32x2{pack2(v[0], v[1]), pack2(v[2], v[3])};
    }
    const u32 hb = WS_HH + (pg * 192 + 12 * jg + 3 * wn) * 2048 + (64 * wm + r16) * 16;
    st128_sc1(rs, hb + (g >> 1) * 2048 + (g & 1) * 256, quad2chunk(p01[0], p01[1]));
    st128_sc1(rs, hb + (g >> 1) * 2048 + (2 + (g & 1)) * 256, quad2chunk(p01[2], p01[3]));
    u32x2 p2[2];
#pragma unroll
    for (int mm2 = 0; mm2 < 2; ++mm2) {
      f32x4 v;
#pragma unroll
      for (int r = 0; r < 4; ++r) {
        auto sw = __builtin_amdgcn_permlane32_swap(__float_as_uint(acc[2 * mm2][2][r]), __float_as_uint(acc[2 * mm2 + 1][2][r]), false, false);
        v[r] = __uint_as_float(sw[0]) * __uint_as_float(sw[1]) * (lane < 32 ? r2[2 * mm2] : r2[2 * mm2 + 1]);
      }
      p2[mm2] = u32x2{pack2(v[0], v[1]), pack2(v[2], v[3])};
    }
    st128_sc1(rs, hb + 2 * 2048 + (2 * (g & 1) + (g >> 1)) * 256, quad2chunk(p2[0], p2[1]));
    STAMP(31);
    if (wm == 0) {
      vm_wait<8>();
      bar();
#pragma unroll
      for (int i = 0; i < 10; ++i) stageDq(2, w + 4 * (i / 5), i % 5);
    }
  }

  {
    u32x4 b[6][5];
    const u32 ad0 = wk * 4 * 5120 + lane * 16;
    auto rdB = [&](int s, int t) { b[s % 6][t] = lds128(ad0 + ((s >> 2) & 3) * 40960 + (s & 3) * 5120 + t * 1024); };
#pragma unroll
    for (int s = 0; s < 4; ++s)
#pragma unroll
      for (int t = 0; t < 5; ++t) rdB(s, t);
    SB();
    if (wk == 0) vm_wait<10>(); else vm_wait<0>();
    bar();
    STAMP(12);
    if (w == PW && lane == 0) __hip_atomic_store((GAS u64*)(ws + flag_off(3, pg, jg)), tag, RLX_AGENT);
    if (wk == 1) {
#pragma unroll
      for (int i = 0; i < 10; ++i) stageDq(3, w - 4 * (i / 5), i % 5);
    }
#pragma unroll
    for (int s = 4; s < 6; ++s)
#pragma unroll
      for (int t = 0; t < 5; ++t) rdB(s, t);
    f32x4 acc[5];
#pragma unroll
    for (int t = 0; t < 5; ++t) { const u32 a = hp[t][0], b2 = hp[t][1]; acc[t] = f32x4{bflo(a), bfhi(a), bflo(b2), bfhi(b2)}; }
    SB();
    if (w == PW) poll_flags(ws, fl_get + 2 * 65536, tag); else if (wk == 0) vm_wait<0>();
    bar();
    STAMP(13);
    u32x4 a[6][4];
    const char* asrc = ws + WS_HH + (p * 192 + 16 * wk) * 2048;
    auto loadAp = [&](int c, int s) {
      int kc = c + rotd; kc = kc >= 6 ? kc - 6 : kc;
      gld_sc1(a[c][s], asrc + kc * 65536 + s * 8192, voffR);
    };
    auto issue = [&](int n) {
      if (n < 4) loadAp(2, n); else if (n < 9) stageDp(4, n - 4); else if (n < 13) loadAp(3, n - 9); else if (n < 18) stageDp(5, n - 13);
      else if (n < 22) loadAp(4, n - 18); else if (n < 26) loadAp(5, n - 22);
    };
#pragma unroll
    for (int s = 0; s < 4; ++s) loadAp(0, s);
#pragma unroll
    for (int s = 0; s < 4; ++s) loadAp(1, s);
#pragma unroll
    for (int s = 0; s < 24; ++s) {
      if (s == 0) vm_wait<4>();
      if (s == 4) vm_wait<8>();
      if (s == 8) vm_wait<12>();
      if (s == 10) vm_wait<11>();
      if (s == 12) vm_wait<11>();
      if (s == 14) vm_wait<8>();
      if (s == 16) vm_wait<4>();
      if (s == 20) vm_wait<0>();
      if (s % 4 == 0) asm volatile("" : "+v"(a[s >> 2][0]), "+v"(a[s >> 2][1]), "+v"(a[s >> 2][2]), "+v"(a[s >> 2][3]));
      if (s == 6) bar();
      if (s == 10 || s == 14) bar0();
      if (s == 0) STAMP(21);
      if (s == 4) STAMP(22); if (s == 8) STAMP(23); if (s == 12) STAMP(24); if (s == 16) STAMP(25);
#pragma unroll
      for (int t = 0; t < 5; ++t) {
        acc[t] = mfma(b[s % 6][t], a[s >> 2][s & 3], acc[t]);
        SB();
        if (s + 6 < 24) { rdB(s + 6, t); SB(); }
        if (t == 1) { issue(2 * s); SB(); }
        if (t == 4) { issue(2 * s + 1); SB(); }
      }
    }
    STAMP(14);
    constexpr u32 L_RED2 = 2 * 40960;
    if (wk == 0) {
#pragma unroll
      for (int t = 0; t < 5; ++t) *(LAS f32x4*)(smem + L_RED2 + (wm4 * 5 + t) * 1024 + lane * 16) = acc[t];
    }
    bar();
    if (wk == 1) {
      u32x2 op[5];
#pragma unroll
      for (int t = 0; t < 5; ++t) {
        const f32x4 v = acc[t] + *(LAS const f32x4*)(smem + L_RED2 + (wm4 * 5 + t) * 1024 + lane * 16);
        op[t] = u32x2{pack2(v[0], v[1]), pack2(v[2], v[3])};
      }
      const __amdgpu_buffer_rsrc_t ro = __builtin_amdgcn_make_buffer_rsrc(out, 0, 2048 * 1280, 0x00020000);
      const u32 oo = (128 * p + 64 * half + 16 * wm4 + r16) * 1280 + slice * 160 + (2 * (g & 1) + (g >> 1)) * 16;
      __builtin_amdgcn_raw_buffer_store_b128(quad2chunk(op[0], op[1]), ro, (int)oo, 0, OUT_AUX);
      __builtin_amdgcn_raw_buffer_store_b128(quad2chunk(op[2], op[3]), ro, (int)oo + 64, 0, OUT_AUX);
      const u32x4 last = quad2chunk(op[4], op[4]);
      if ((g & 1) == 0) __builtin_amdgcn_raw_buffer_store_b128(last, ro, (int)oo + 128, 0, OUT_AUX);
    }
    STAMP(15);
  }
}

extern "C" void kernel_launch(void* const* d_in, const int* in_sizes, int n_in, void* d_out, int out_size, void* d_ws, size_t ws_size, hipStream_t stream) {
  (void)in_sizes; (void)n_in; (void)out_size; (void)ws_size;
  (void)hipFuncSetAttribute((const void*)fused_block, hipFuncAttributeMaxDynamicSharedMemorySize, LDS_BYTES);
  fused_block<<<256, 512, LDS_BYTES, stream>>>((const char*)d_in[0], (const char*)d_in[1], (const char*)d_in[2], (const char*)d_in[4], (const char*)d_in[5],
                                               (const char*)d_in[6], (char*)d_out, (char*)d_ws);
}
```

```cpp
#include <hip/hip_runtime.h>
#include <stdint.h>

typedef unsigned short u16;
typedef unsigned int u32;
typedef unsigned long long u64;
typedef __bf16 bf16x8 __attribute__((ext_vector_type(8)));
typedef __bf16 bf16x2 __attribute__((ext_vector_type(2)));
typedef short s16x4 __attribute__((ext_vector_type(4)));
typedef float f32x4 __attribute__((ext_vector_type(4)));
typedef float f32x2 __attribute__((ext_vector_type(2)));
typedef u32 u32x4 __attribute__((ext_vector_type(4)));
typedef u32 u32x2 __attribute__((ext_vector_type(2)));
#define LAS __attribute__((address_space(3)))
#define GAS __attribute__((address_space(1)))
#define DI __device__ __forceinline__
#define RLX_AGENT __ATOMIC_RELAXED, __HIP_MEMORY_SCOPE_AGENT

#define RG 5
#define OUT_AUX 0
#define W3BAR 0
#define DWARM 0
#define GMAP 0
#define DMAP 1
#define DROT 0
#define RSAT 8
#define X_A 0
#define X_B 0
#define X_C 0
#define X_D 0
#define POLLW 0
#define KSWAP 0
#define KROT 0
#define POLLV 8
#define FW 0
#define NEARLY 20
#define ATT_PRIO 0
#define STAMPS 0

constexpr u32 WS_EPOCH = 0;
constexpr u32 WS_DENSE = 2048;
constexpr u32 WS_FLAG = 4096;
constexpr u32 WS_QKV = WS_FLAG + 4 * 256 * 256;
constexpr u32 WS_O = WS_QKV + 16 * 240 * 2048;
constexpr u32 WS_H = WS_O + 16 * 80 * 2048;
constexpr u32 WS_HH = WS_H + 16 * 80 * 2048;
constexpr u32 WS_ST = WS_HH + 16 * 192 * 2048;
constexpr u32 WS_RS = WS_ST + 16 * 8 * 1024;
constexpr u32 WS_WB = WS_RS + 2048 * 16;
constexpr u32 WT_SLICE = 68 * 5120;
constexpr u32 WS_WT = WS_WB + 3072;
constexpr u32 WS_END = WS_WT + 8 * WT_SLICE;
constexpr u32 WS_STAMP = WS_END;
constexpr u64 TAG_STEP = 0x9E3779B97F4A7C15ull;

constexpr u32 LDS_BYTES = 163840;
constexpr u32 L_MISC = 161792;
constexpr u32 L_K = 0, KCH = 6144;
constexpr u32 L_V = 6 * KCH, VCH = 6208;
constexpr u32 L_WOB = L_MISC - 102400;
__device__ constexpr u32 gb_off(int c) { return (c & 3) == 2 ? 0 : ((c & 3) == 3 ? 24576 : ((c & 3) == 0 ? 65536 : 106496)); }
__device__ constexpr u32 ga_off(int c) { return c % 3 == 1 ? 49152 : (c % 3 == 2 ? 90112 : 131072); }
constexpr u32 L_ST = 147456;
constexpr u32 L_WB = 49152;
constexpr u32 L_RED = 24576;
constexpr u32 L_WARM = L_MISC + 1792;
constexpr u32 L_RT = 131072;
constexpr u32 SPIN_CAP = 1u << 15;
constexpr float LN_EPS = 1e-5f;

extern __shared__ __attribute__((aligned(16))) char smem[];

DI u32x4 lds128(u32 off) { return *(LAS const u32x4*)(smem + off); }
DI u32x2 ldstr(u32 off) { return __builtin_bit_cast(u32x2, __builtin_amdgcn_ds_read_tr16_b64_v4i16((LAS s16x4*)(smem + off))); }
DI u32x4 ldstr2(u32 lo, u32 hi) { const u32x2 a = ldstr(lo), b = ldstr(hi); return u32x4{a[0], a[1], b[0], b[1]}; }
DI f32x4 mfma(u32x4 a, u32x4 b, f32x4 c) { return __builtin_amdgcn_mfma_f32_16x16x32_bf16(__builtin_bit_cast(bf16x8, a), __builtin_bit_cast(bf16x8, b), c, 0, 0, 0); }
typedef float f32x16 __attribute__((ext_vector_type(16)));
DI f32x16 mfma32(u32x4 a, u32x4 b, f32x16 c) { return __builtin_amdgcn_mfma_f32_32x32x16_bf16(__builtin_bit_cast(bf16x8, a), __builtin_bit_cast(bf16x8, b), c, 0, 0, 0); }
DI float max3(float a, float b, float c) { return __builtin_fmaxf(__builtin_fmaxf(a, b), c); }
DI f32x4 mfma16(u32x2 a, u32x2 b, f32x4 c) { return __builtin_amdgcn_mfma_f32_16x16x16bf16_1k(__builtin_bit_cast(s16x4, a), __builtin_bit_cast(s16x4, b), c, 0, 0, 0); }
DI float dot2(u32 a, u32 b, float c) { return __builtin_amdgcn_fdot2_f32_bf16(__builtin_bit_cast(bf16x2, a), __builtin_bit_cast(bf16x2, b), c, false); }
DI u32 pack2(float lo, float hi) { const f32x2 v = {lo, hi}; return __builtin_bit_cast(u32, __builtin_convertvector(v, bf16x2)); }
DI float bflo(u32 v) { return __uint_as_float(v << 16); }
DI float bfhi(u32 v) { return __uint_as_float(v & 0xffff0000u); }
constexpr u32 ONES2 = 0x3f803f80u;
DI float sum8(u32x4 a, float s) { const u32 a0 = a[0], a1 = a[1], a2 = a[2], a3 = a[3]; return dot2(a3, ONES2, dot2(a2, ONES2, dot2(a1, ONES2, dot2(a0, ONES2, s)))); }
DI float sq8(u32x4 a, float s) { const u32 a0 = a[0], a1 = a[1], a2 = a[2], a3 = a[3]; return dot2(a3, a3, dot2(a2, a2, dot2(a1, a1, dot2(a0, a0, s)))); }
DI float gsum(float x) {
  auto a = __builtin_amdgcn_permlane16_swap(__float_as_uint(x), __float_as_uint(x), false, false);
  x = __uint_as_float(a[0]) + __uint_as_float(a[1]);
  auto b = __builtin_amdgcn_permlane32_swap(__float_as_uint(x), __float_as_uint(x), false, false);
  return __uint_as_float(b[0]) + __uint_as_float(b[1]);
}
DI float gmax(float x) {
  auto a = __builtin_amdgcn_permlane16_swap(__float_as_uint(x), __float_as_uint(x), false, false);
  x = __builtin_fmaxf(__uint_as_float(a[0]), __uint_as_float(a[1]));
  auto b = __builtin_amdgcn_permlane32_swap(__float_as_uint(x), __float_as_uint(x), false, false);
  return __builtin_fmaxf(__uint_as_float(b[0]), __uint_as_float(b[1]));
}
DI u32x4 quad2chunk(u32x2 A, u32x2 B) {
  const u32 a0 = A[0], a1 = A[1], b0 = B[0], b1 = B[1];
  auto s0 = __builtin_amdgcn_permlane16_swap(a0, b0, false, false);
  auto s1 = __builtin_amdgcn_permlane16_swap(a1, b1, false, false);
  return u32x4{s0[0], s1[0], s0[1], s1[1]};
}
DI void dma(const void* sbase, u32 voff, u32 lds) {
  asm volatile("s_mov_b32 m0, %2\n\ts_nop 0\n\tglobal_load_lds_dwordx4 %0, %1" ::"v"(voff), "s"(sbase), "s"(lds));
}
DI void dma_sc1(const void* sbase, u32 voff, u32 lds) {
  asm volatile("s_mov_b32 m0, %2\n\ts_nop 0\n\tglobal_load_lds_dwordx4 %0, %1 sc1" ::"v"(voff), "s"(sbase), "s"(lds));
}
DI void gld_sc1(u32x4& dst, const void* sbase, u32 voff) {
  asm volatile("global_load_dwordx4 %0, %1, %2 sc1" : "=v"(dst) : "v"(voff), "s"(sbase));
}
DI void gld(u32x4& dst, const void* sbase, u32 voff) {
  asm volatile("global_load_dwordx4 %0, %1, %2" : "=v"(dst) : "v"(voff), "s"(sbase));
}
DI void gld64(u32x2& dst, const void* sbase, u32 voff) {
  asm volatile("global_load_dwordx2 %0, %1, %2" : "=v"(dst) : "v"(voff), "s"(sbase));
}
template <int N> DI void vm_wait() { asm volatile("s_waitcnt vmcnt(%0)" ::"n"(N) : "memory"); }
template <int N> DI void vm_wait_use(u32x4& r) { asm volatile("s_waitcnt vmcnt(%1)" : "+v"(r) : "n"(N)); }
#define INL __attribute__((always_inline))
#define SB() __builtin_amdgcn_sched_barrier(0)
DI void warm(const void* sbase, u32 voff, u32 lds) {
  asm volatile("s_mov_b32 m0, %2\n\ts_nop 0\n\tglobal_load_lds_dword %0, %1" ::"v"(voff), "s"(sbase), "s"(lds));
}
DI void warm_sc1(const void* sbase, u32 voff, u32 lds) {
  asm volatile("s_mov_b32 m0, %2\n\ts_nop 0\n\tglobal_load_lds_dword %0, %1 sc1" ::"v"(voff), "s"(sbase), "s"(lds));
}
DI void bar0() { asm volatile("s_barrier" ::: "memory"); }
DI u32x4 cat(u32x2 a, u32x2 b) { return u32x4{a[0], a[1], b[0], b[1]}; }
DI void bar() { asm volatile("s_waitcnt lgkmcnt(0)\n\ts_barrier" ::: "memory"); }
DI void st128_sc1(__amdgpu_buffer_rsrc_t rs, u32 off, u32x4 v) { __builtin_amdgcn_raw_buffer_store_b128(v, rs, (int)off, 0, 16); }
DI void st64_sc1(__amdgpu_buffer_rsrc_t rs, u32 off, u32x2 v) { __builtin_amdgcn_raw_buffer_store_b64(v, rs, (int)off, 0, 16); }

DI void poll_flags(const char* ws, u32 off, u64 tag) {
  GAS u64* a = (GAS u64*)(ws + off);
  __builtin_amdgcn_s_sleep(POLLV);
  for (u32 spins = 0; spins < SPIN_CAP; ++spins) {
    const u64 v = __hip_atomic_load(a, RLX_AGENT);
    if (__all(v == tag)) break;
    if (POLLW) __builtin_amdgcn_s_sleep(POLLW);
  }
  __builtin_amdgcn_fence(__ATOMIC_ACQUIRE, "wavefront");
}
DI u32 flag_off(int phase, int p, int j) { return WS_FLAG + (u32)((phase * 256 + p * 16 + j) * 256); }

#define STAMP(i) do {} while (0)

__global__ __launch_bounds__(512, 2) void fused_block(const char* __restrict__ x, const char* __restrict__ wqkv, const char* __restrict__ wo,
                                                      const char* __restrict__ wg, const char* __restrict__ wu, const char* __restrict__ wd,
                                                      char* __restrict__ out, char* ws) {
  const int tid = threadIdx.x, lane = tid & 63, w = __builtin_amdgcn_readfirstlane(tid >> 6);
  const int r16 = lane & 15, g = lane >> 4, q4 = r16 >> 2, pq = r16 & 3;
  const int bx = blockIdx.x, xcd = bx & 7, rr = bx >> 3;
  const int p = (xcd >> 1) * 4 + (rr >> 3), j = (xcd & 1) * 8 + (rr & 7);
  const int pa = rr >> 1, ja = 2 * xcd + (rr & 1);
  const int pg = GMAP ? pa : 8 * (xcd >> 2) + (rr >> 2), jg = GMAP ? ja : 4 * (xcd & 3) + (rr & 3);
  const int half = DMAP ? (rr >> 2) & 1 : j >> 3, slice = DMAP ? 4 * (xcd & 1) + (rr & 3) : j & 7;
  constexpr int PW = 3;
  const __amdgpu_buffer_rsrc_t rs = __builtin_amdgcn_make_buffer_rsrc(ws, 0, WS_END, 0x00020000);
  STAMP(0);
  u64 tag;
  auto load_epoch = [&](u32x2& e) { const u32 z = 0; asm volatile("global_load_dwordx2 %0, %1, %2 sc1" : "=v"(e) : "v"(z), "s"(ws + WS_EPOCH)); };
  auto make_tag = [&](u32x2& e) {
    asm volatile("" : "+v"(e));
    const u32 e0 = e[0], e1 = e[1];
    tag = (((u64)(u32)__builtin_amdgcn_readfirstlane(e1) << 32) | (u32)__builtin_amdgcn_readfirstlane(e0)) + TAG_STEP;
  };
  const char* xa = x + (size_t)p * 128 * 1280;
  const u32x4 ones8 = {ONES2, ONES2, ONES2, ONES2};
  const char* wt = ws + WS_WT + slice * WT_SLICE;
  auto rt_dst = [&](int piece, int blk, int gq) {
    const int tile = 4 * piece + g, sl = tile / 5, t = tile - sl * 5;
    return WS_WT + sl * WT_SLICE + blk * 5120 + t * 1024 + gq * 256 + r16 * 16;
  };
  auto warm_gu = [&]() INL {
    if (GMAP) {
      const int l3 = lane < 30 ? lane : 29, rw = l3 / 3;
      warm(((w & 1) ? wu : wg) + (size_t)(rr * 20 + (w >> 1) * 10) * 3072 + xcd * 384, rw * 3072 + (l3 - rw * 3) * 128, L_WARM);
      return;
    }
    const int l6 = lane < 60 ? lane : 59, rw = l6 / 6;
    warm(((w & 1) ? wu : wg) + (size_t)(rr * 20 + (w >> 1) * 10) * 3072 + (xcd & 3) * 768, rw * 3072 + (l6 - rw * 6) * 128, L_WARM);
  };
  u32 kvsrc[8], kvdst[8], fl_get;
  auto setup = [&]() INL {
#pragma unroll
    for (int i = 0; i < 8; ++i) {
      const int id = w + 8 * i, kv = id >= 30, r = id - 30 * kv, c = r / 6, r2 = r - 6 * c;
      int pp = pa - 2 + (r2 >> 1); pp = pp < 0 ? 0 : pp;
      kvsrc[i] = WS_QKV + (pp * 240 + 80 * (1 + kv) + 5 * ja + c) * 2048 + (r2 & 1) * 1024;
      kvdst[i] = (kv ? L_V + c * VCH : L_K + c * KCH) + r2 * 1024;
      asm volatile("" : "+s"(kvsrc[i]), "+s"(kvdst[i]));
    }
    const int e = r16 < 14 ? r16 : 0, sec = e < 2 ? 0 : (e < 8 ? 1 : 2), i = e < 2 ? e : (e - 2) % 6;
    int pp = e < 2 ? pa : pa - (i >> 1); pp = pp < 0 ? 0 : pp;
    fl_get = (g & 1) ? flag_off(0, r16, 15) : flag_off(0, pp, (80 * sec + 5 * ja + 4 * (i & 1)) >> 4);
    asm volatile("" : "+v"(fl_get));
  };

  if (j < 15) {
    const int wm = w & 1, wn = w >> 1;
    u32 voffA[2], voffB[2];
#pragma unroll
    for (int i = 0; i < 2; ++i) {
      const int ra = 16 * w + 8 * i + (lane >> 3);
      voffA[i] = ra * 1280 + (((lane & 7) ^ ((ra >> 1) & 7)) << 4);
      const int rb = 8 * w + 4 * i + g, f = (rb & 3) | (((rb >> 3) & 1) << 2);
      voffB[i] = rb * 3840 + (((((r16 >> 1) ^ f) << 1) | (r16 & 1)) << 4);
    }
    const char* wb = wqkv + j * 256;
    const int rot = RG == 10 ? ((rr & 7) + ((rr & 7) >> 2) + ((5 * (rr >> 3)) >> 1)) % 10 : ((rr & 7) + (rr >> 3)) % RG;
    auto stage = [&](int c, int piece) {
      int kc = c % RG + rot; kc = (kc >= RG ? kc - RG : kc) + (c / RG) * RG;
      const u32 slot = (c & 3) * 32768;
      const char* a = xa + kc * 128; const char* b = wb + (size_t)kc * 64 * 3840;
      if (piece < 0 || piece == 0) dma(a, voffA[0], slot + w * 2048);
      if (piece < 0 || piece == 1) dma(a, voffA[1], slot + w * 2048 + 1024);
      if (piece < 0 || piece == 2) dma(b, voffB[0], slot + 16384 + w * 2048);
      if (piece < 0 || piece == 3) dma(b, voffB[1], slot + 16384 + w * 2048 + 1024);
    };
    stage(0, -1);
    u32x2 epoch;
    load_epoch(epoch);
    {
      u64 pc; asm volatile("s_getpc_b64 %0" : "=s"(pc));
      if (w == 0) warm((const char*)(pc & ~127ull) + rr * 1280, (lane < 10 ? lane : 9) * 128, L_WARM);
    }
    setup();
    stage(1, -1);
    u32 adA[2], adB[2];
#pragma unroll
    for (int ks = 0; ks < 2; ++ks) adA[ks] = (64 * wm + r16) * 128 + (((4 * ks + g) ^ ((r16 >> 1) & 7)) << 4);
#pragma unroll
    for (int n = 0; n < 2; ++n) adB[n] = 16384 + (8 * g + q4) * 256 + (((2 * wn + (n ^ wm)) ^ (q4 | ((g & 1) << 2))) << 5) + pq * 8;
    f32x4 acc[4][2], csum = {0, 0, 0, 0};
#pragma unroll
    for (int m = 0; m < 4; ++m) { acc[m][0] = f32x4{0, 0, 0, 0}; acc[m][1] = f32x4{0, 0, 0, 0}; }
    u32x4 fa[2][4]; u32x2 fb[2][2][2];
    u32x4 rsg[4];
    auto ld_rs = [&]() {
#pragma unroll
      for (int m = 0; m < 4; ++m) gld_sc1(rsg[m], ws + WS_RS + (p * 128 + 64 * wm + 16 * m) * 16, r16 * 16);
    };
    auto rdp = [&](int c, int ks, int i) {
      const u32 slot = (c & 3) * 32768;
      if (i == 0 || i == 1) fb[ks][0][i] = ldstr(slot + adB[0] + ks * 8192 + i * 1024);
      else if (i == 2) fa[ks][0] = lds128(slot + adA[ks]);
      else if (i == 3 || i == 4) fb[ks][1][i - 3] = ldstr(slot + adB[1] + ks * 8192 + (i - 3) * 1024);
      else if (i < 8) fa[ks][i - 4] = lds128(slot + adA[ks] + (i - 4) * 2048);
    };
    auto mm = [&](int ks, int cr, int cn) {
      const u32x4 b0 = cat(fb[ks][0][0], fb[ks][0][1]), b1 = cat(fb[ks][1][0], fb[ks][1][1]);
#pragma unroll
      for (int i = 0; i < 9; ++i) {
        if (i > 0) acc[(i - 1) >> 1][(i - 1) & 1] = mfma(((i - 1) & 1) ? b1 : b0, fa[ks][(i - 1) >> 1], acc[(i - 1) >> 1][(i - 1) & 1]);
        else csum = mfma(b0, ones8, csum);
        if (cr == 10 && i == 3) {
          if (r16 == 0) *(LAS f32x4*)(smem + L_MISC + 1024 + (32 * wn + 16 * wm + 4 * g) * 4) = csum;
          asm volatile("" ::: "memory");
          if (lane == 0) *(volatile LAS u32*)(smem + L_MISC + w * 4) = 1;
        }
        if (cr < 10) rdp(cr, ks ^ 1, i);
        if (cn < 10 && i == 4) stage(cn, 2 * (ks ^ 1));
        if (cn < 10 && i == 8) stage(cn, 2 * (ks ^ 1) + 1);
        SB();
      }
    };
    if (tid < 8) *(LAS u32*)(smem + L_MISC + tid * 4) = 0;
    vm_wait<4>();
    make_tag(epoch);
    bar();
    STAMP(16);
#pragma unroll
    for (int i = 0; i < 8; ++i) rdp(0, 0, i);
    SB();
    stage(2, 0); stage(2, 1);
    SB();
#pragma unroll
    for (int c = 0; c < 10; ++c) {
      mm(0, c, c + 2);
      if (c < 9) {
        if (c <= 7 || RSAT == 7) vm_wait<4>(); else vm_wait<0>();
        bar0();
        if (c == RSAT) { ld_rs(); SB(); }
        if (c == 8 && j < 12 && w < 5) {
          dma(wd + (p * 12 + j) * 10240 + w * 256, (lane >> 3) * 1280 + (lane & 7) * 16, L_RT + w * 2048);
          dma(wd + (p * 12 + j) * 10240 + w * 256 + 128, (lane >> 3) * 1280 + (lane & 7) * 16, L_RT + w * 2048 + 1024);
          SB();
        }
      }
      mm(1, c + 1, c + 3);
    }
    STAMP(1);
    if (j < 12 && w < 5) vm_wait<2>(); else vm_wait<0>();
    for (u32 spins = 0; spins < SPIN_CAP; ++spins) {
      asm volatile("" : "+v"(rsg[0]), "+v"(rsg[1]), "+v"(rsg[2]), "+v"(rsg[3]));
      bool ok = true;
#pragma unroll
      for (int m = 0; m < 4; ++m) ok = ok && rsg[m][1] == (u32)tag && rsg[m][3] == (u32)(tag >> 32);
      if (__all(ok)) break;
      ld_rs();
      vm_wait<0>();
    }
    asm volatile("" : "+v"(rsg[0]), "+v"(rsg[1]), "+v"(rsg[2]), "+v"(rsg[3]));
    STAMP(27);
    while (*(volatile LAS u32*)(smem + L_MISC + (w ^ 1) * 4) == 0) {}
    asm volatile("" ::: "memory");
    STAMP(17);
    const float qs = j < 5 ? 0.22360679774997896f * 1.4426950408889634f : 1.f;
    f32x4 cs[2];
#pragma unroll
    for (int n = 0; n < 2; ++n) cs[n] = n == 0 ? csum : *(LAS const f32x4*)(smem + L_MISC + 1024 + (32 * wn + 16 * (1 ^ wm) + 4 * g) * 4);
    const u32 qb = WS_QKV + (p * 240 + 16 * j + 4 * wn + 2 * ((g & 1) ^ wm) + (g >> 1)) * 2048 + (64 * wm + r16) * 16;
#pragma unroll
    for (int m = 0; m < 4; ++m) {
      const u32 um = rsg[m][0], ur = rsg[m][2];
      const float mean = __uint_as_float(um), sc = __uint_as_float(ur) * qs;
      u32x2 pk[2];
#pragma unroll
      for (int n = 0; n < 2; ++n) {
        const f32x4 v = (acc[m][n] - mean * cs[n]) * sc;
        pk[n] = u32x2{pack2(v[0], v[1]), pack2(v[2], v[3])};
      }
      st128_sc1(rs, qb + m * 256, quad2chunk(pk[0], pk[1]));
    }
  } else {
    u32x2 epoch;
    load_epoch(epoch);
    if (w < 5) {
#pragma unroll
      for (int i = 0; i < 10; ++i) dma(wo + (p * 5 + w) * 10240 + i * 1024, lane * 16, w * 10240 + i * 1024);
    }
    u32x4 xv[2][10];
#pragma unroll
    for (int t = 0; t < 10; ++t)
#pragma unroll
      for (int s2 = 0; s2 < 2; ++s2) gld(xv[s2][t], xa + (16 * w + 8 * s2) * 1280 + t * 128, (lane >> 3) * 1280 + (lane & 7) * 16);
    float s1[2] = {0.f, 0.f}, sq[2] = {0.f, 0.f};
#pragma unroll
    for (int t = 0; t < 10; ++t) {
      if (t == 0) vm_wait<10>();
      if (t == 5) vm_wait<4>();
      if (t == 8) vm_wait<0>();
#pragma unroll
      for (int s2 = 0; s2 < 2; ++s2) { asm volatile("" : "+v"(xv[s2][t])); s1[s2] = sum8(xv[s2][t], s1[s2]); sq[s2] = sq8(xv[s2][t], sq[s2]); }
    }
    make_tag(epoch);
#pragma unroll
    for (int o = 1; o < 8; o <<= 1) { s1[0] += __shfl_xor(s1[0], o); sq[0] += __shfl_xor(sq[0], o); s1[1] += __shfl_xor(s1[1], o); sq[1] += __shfl_xor(sq[1], o); }
    {
      const float a1 = (lane & 1) ? s1[1] : s1[0], a2 = (lane & 1) ? sq[1] : sq[0];
      const float mean = a1 * (1.f / 640.f), var = __builtin_fmaxf(a2 * (1.f / 640.f) - mean * mean, 0.f);
      if ((lane & 7) < 2)
        st128_sc1(rs, WS_RS + (p * 128 + 16 * w + 8 * (lane & 1) + (lane >> 3)) * 16, u32x4{__float_as_uint(mean), (u32)tag, __float_as_uint(__builtin_amdgcn_rsqf(var + LN_EPS)), (u32)(tag >> 32)});
    }
    STAMP(16);
    setup();
    bar();
    {
#pragma unroll
      for (int i = 0; i < 7; ++i) {
        const int id = w + 8 * i, gr = id / 10;
        if (id < 50) {
          const u32 l = gr * 10240 + q4 * 1280 + (id - 10 * gr) * 128 + g * 32 + pq * 8;
          st128_sc1(rs, rt_dst(id - 10 * gr, (p * 5 + gr) >> 2, (p * 5 + gr) & 3), ldstr2(l, l + 5120));
        }
      }
      if (w >= 3) {
        float a1 = 0.f;
#pragma unroll
        for (int t = 0; t < 10; ++t) a1 = sum8(lds128((w - 3) * 10240 + (lane >> 3) * 1280 + t * 128 + (lane & 7) * 16), a1);
#pragma unroll
        for (int o = 1; o < 8; o <<= 1) a1 += __shfl_xor(a1, o);
        a1 *= (1.f / 640.f);
        const u32 hi = pack2(a1, 0.f), lo = pack2(a1 - bflo(hi), 0.f);
        if ((lane & 7) < 2) __builtin_amdgcn_raw_buffer_store_b16((u16)((lane & 1) ? lo : hi), rs, (int)(WS_WB + (lane & 1) * 1280 + ((p * 5 + w - 3) * 8 + (lane >> 3)) * 2), 0, 16);
      }
    }
  }
  STAMP(18);
  vm_wait<0>();
  bar();
  STAMP(2);
  if (w == FW && lane == 0) __hip_atomic_store((GAS u64*)(ws + flag_off(0, p, j)), tag, RLX_AGENT);
  asm volatile("" ::: "memory");
  if (w != PW && w != 7) {
    const int kr = (w < PW ? w : w - 1) * 64 + lane;
    const bool valid = 128 * (pa - 2) + kr >= 0;
    *(LAS u32x4*)(smem + L_K + 5 * KCH + kr * 16) = u32x4{valid ? 0u : 0xf149u, 0u, ONES2, ONES2};
  }
  if (w == PW) poll_flags(ws, fl_get, tag);
  bar();
  STAMP(3);

  u32x2 xr[6];
  u32 voffG[3];
  {
    const int h = w & 1, qt = w >> 1, l32 = lane & 31, hh = lane >> 5;
    u32x4 qf[2];
    gld_sc1(qf[0], ws + WS_QKV + (pa * 240 + 5 * ja + 2 * h) * 2048 + qt * 512, hh * 2048 + l32 * 16);
    gld_sc1(qf[1], ws + WS_QKV + (pa * 240 + 5 * ja + 2 * h + 2) * 2048 + qt * 512, l32 * 16);
#pragma unroll
    for (int i = 0; i < 5; ++i) dma_sc1(ws + kvsrc[i], lane * 16, kvdst[i]);
    u32 dm[8];
#pragma unroll
    for (int m = 0; m < 8; ++m) {
      const int k0 = ((2 * m) & 3) + 8 * (m >> 1) + 4 * hh;
      dm[m] = (k0 <= l32 ? 0xffffu : 0u) | (k0 + 1 <= l32 ? 0xffff0000u : 0u);
      asm volatile("" : "+v"(dm[m]));
    }
    u32 adK0 = L_K + (2 * h + hh) * KCH + (32 * qt + l32) * 16, adK1 = L_K + (hh ? 5 : 2 * h + 2) * KCH + (32 * qt + l32) * 16;
    const u32 vrow = 32 * qt + 4 * hh + q4;
    u32 adV = (g & 1) == 0 ? L_V + (3 * h + (pq >> 1)) * VCH + vrow * 16 + (pq & 1) * 8
                           : (pq == 0 ? L_V + 2 * VCH + vrow * 16 + 8 * h : L_K + 5 * KCH + ((4 * hh + (h ? 4 : 12)) & 15) * 16 + 8);
#pragma unroll
    for (int i = 0; i < 3; ++i) {
      const int s = 64 * i + lane, rw = s / 12, cp = s - rw * 12;
      int c = cp - (w & 1) - 2 * ((rw >> 3) & 1); c = c < 0 ? c + 12 : c;
      voffG[i] = rw * 3072 + c * 16;
    }
    asm volatile("" : "+v"(adK0), "+v"(adK1), "+v"(adV), "+v"(voffG[0]), "+v"(voffG[1]), "+v"(voffG[2]));
    vm_wait<1>();
    asm volatile("" : "+v"(qf[0]), "+v"(qf[1]));
    bar();
    STAMP(4);
    const f32x16 zero16 = {0, 0, 0, 0, 0, 0, 0, 0, 0, 0, 0, 0, 0, 0, 0, 0};
    u32x4 kf[9][2];
    constexpr int ord[9] = {0, 8, 1, 2, 3, 4, 5, 6, 7};
#pragma unroll
    for (int n = 0; n < 4; ++n) { kf[ord[n]][0] = lds128(adK0 + 512 * ord[n]); kf[ord[n]][1] = lds128(adK1 + 512 * ord[n]); }
    SB();
    dma_sc1(ws + kvsrc[5], lane * 16, kvdst[5]);
    if (h == 1 && hh == 0) { qf[0][0] = 0; qf[0][1] = 0; }
    if (h == 0) { qf[1][2] = 0; qf[1][3] = 0; }
    if (hh == 1) qf[1] = u32x4{0x3f80u, 0, 0, 0};
    SB();
    f32x16 sc[9];
    float m4[4];
    auto qk = [&](int i, int st) { sc[i] = mfma32(kf[i][st], qf[st], st == 0 ? zero16 : sc[i]); };
    qk(0, 0); qk(8, 0);
    SB();
    dma_sc1(ws + kvsrc[6], lane * 16, kvdst[6]);
    SB();
    qk(0, 1); qk(8, 1);
    SB();
    if (w < 4) dma_sc1(ws + kvsrc[7], lane * 16, kvdst[7]);
    SB();
#pragma unroll
    for (int n = 2; n < 11; ++n) {
      if (n < 9) { qk(ord[n], 0); SB(); }
      if (n + 2 < 9) { kf[ord[n + 2]][0] = lds128(adK0 + 512 * ord[n + 2]); SB(); }
      if (n == 3) {
#pragma unroll
        for (int r = 6; r < 16; ++r) sc[0][r] = ((r & 3) + 8 * (r >> 2) + 4 * hh <= l32) ? sc[8][r] : sc[0][r];
      } else if (n > 3) {
#pragma unroll
        for (int c = 0; c < 4; ++c) m4[c] = max3(m4[c], sc[n - 3][2 * c], sc[n - 3][2 * c + 1]);
      }
      SB();
      if (n < 9) { qk(ord[n], 1); SB(); }
      if (n + 2 < 9) { kf[ord[n + 2]][1] = lds128(adK1 + 512 * ord[n + 2]); SB(); }
      if (n == 2) {
#pragma unroll
        for (int r = 0; r < 6; ++r) sc[0][r] = ((r & 3) + 8 * (r >> 2) + 4 * hh <= l32) ? sc[8][r] : sc[0][r];
      } else if (n == 3) {
#pragma unroll
        for (int c = 0; c < 4; ++c) m4[c] = max3(max3(sc[0][4 * c], sc[0][4 * c + 1], sc[0][4 * c + 2]), sc[0][4 * c + 3], sc[0][4 * c + 3]);
      } else {
#pragma unroll
        for (int c = 0; c < 4; ++c) m4[c] = max3(m4[c], sc[n - 3][8 + 2 * c], sc[n - 3][9 + 2 * c]);
      }
      SB();
    }
    float mx = __builtin_fmaxf(max3(m4[0], m4[1], m4[2]), m4[3]);
    { auto sw = __builtin_amdgcn_permlane32_swap(__float_as_uint(mx), __float_as_uint(mx), false, false); mx = __builtin_fmaxf(__uint_as_float(sw[0]), __uint_as_float(sw[1])); }
    u32x4 nmf;
    {
      const float nmx = -mx;
      const u32 p0 = __float_as_uint(nmx) & 0xffff0000u;
      const float r1 = nmx - __uint_as_float(p0);
      const u32 p1 = __float_as_uint(r1) & 0xffff0000u;
      const u32 p2 = __float_as_uint(r1 - __uint_as_float(p1));
      nmf = u32x4{hh ? p2 >> 16 : (p0 >> 16) | p1, 0, 0, 0};
    }
    auto submax = [&](int i) { sc[i] = mfma32(ones8, nmf, sc[i]); };
    submax(0); submax(1);
    SB();
    u32x4 rtv[3];
    if (w < 4) {
#pragma unroll
      for (int i = 0; i < 3; ++i) { const u32 l = L_RT + (w + 4 * i) * 1024 + q4 * 128 + g * 32 + pq * 8; rtv[i] = ldstr2(l, l + 512); }
    }
    STAMP(29);
    vm_wait<0>();
    bar();
    STAMP(26);
    if (w < 4) {
      if (j < 12) {
        const int gi = p * 12 + j;
#pragma unroll
        for (int i = 0; i < 3; ++i)
          if (w + 4 * i < 10) st128_sc1(rs, rt_dst(w + 4 * i, 20 + (gi >> 2), gi & 3), rtv[i]);
      }
#pragma unroll
      for (int i = 0; i < NEARLY; ++i) dma_sc1(wt + 20480 * (1 + w) + i * 1024, lane * 16, L_WOB + 20480 * (1 + w) + i * 1024);
#pragma unroll
      for (int t = 0; t < 5; ++t) gld64(xr[t], xa + (64 * half + 16 * w) * 1280 + slice * 160 + t * 32, r16 * 1280 + g * 8);
      asm volatile("global_load_dwordx2 %0, %1, %2 sc1" : "=v"(xr[5]) : "v"(r16 * 16), "s"(ws + WS_RS + (p * 128 + 64 * half + 16 * w) * 16));
      warm_gu();
    } else {
#pragma unroll
      for (int t = 0; t < 6; ++t) asm volatile("v_mov_b64 %0, 0" : "=v"(xr[t]));
    }
    SB();
    f32x16 o = zero16;
    u32 pw[2][8];
    u32x2 vf[2][4], vx[4];
    auto rdV = [&](int i, int e) { return ldstr(adV + 512 * i + 128 * e); };
    auto pfrag = [&](const u32* q, int st) { return u32x4{q[4 * st], q[4 * st + 1], q[4 * st + 2], q[4 * st + 3]}; };
    u32 p8[8];
#pragma unroll
    for (int n = 0; n < 8; ++n) {
      float ex[16];
#pragma unroll
      for (int e = 0; e < 8; ++e) {
        ex[2 * e] = __builtin_amdgcn_exp2f(sc[n][2 * e]); SB();
        if (e < 4) { vf[n & 1][e] = rdV(n, e); SB(); }
        else if (n == 0) { vx[e - 4] = rdV(8, e - 4); SB(); }
        ex[2 * e + 1] = __builtin_amdgcn_exp2f(sc[n][2 * e + 1]); SB();
        if (e > 0) { pw[n & 1][e - 1] = pack2(ex[2 * e - 2], ex[2 * e - 1]); SB(); }
        if (n >= 1) {
          if (e == 0 && n < 7) { submax(n + 1); SB(); }
          if (e == 3) { o = mfma32(cat(vf[(n - 1) & 1][0], vf[(n - 1) & 1][1]), pfrag(pw[(n - 1) & 1], 0), o); SB(); }
          if (e == 6) { o = mfma32(cat(vf[(n - 1) & 1][2], vf[(n - 1) & 1][3]), pfrag(pw[(n - 1) & 1], 1), o); SB(); }
          if (n == 2 && e == 2) { o = mfma32(cat(vx[0], vx[1]), pfrag(p8, 0), o); SB(); }
          if (n == 2 && e == 5) { o = mfma32(cat(vx[2], vx[3]), pfrag(p8, 1), o); SB(); }
        }
      }
      pw[n & 1][7] = pack2(ex[14], ex[15]);
      if (n == 0) {
#pragma unroll
        for (int m = 0; m < 8; ++m) { p8[m] = pw[0][m] & dm[m]; pw[0][m] &= ~dm[m]; }
      }
      SB();
    }
    o = mfma32(cat(vf[1][0], vf[1][1]), pfrag(pw[1], 0), o);
    o = mfma32(cat(vf[1][2], vf[1][3]), pfrag(pw[1], 1), o);
    const float inv = __builtin_amdgcn_rcpf(o[12]);
    const u32 x0 = pack2(o[0] * inv, o[1] * inv), x1 = pack2(o[2] * inv, o[3] * inv), y0 = pack2(o[4] * inv, o[5] * inv), y1 = pack2(o[6] * inv, o[7] * inv);
    auto s0 = __builtin_amdgcn_permlane32_swap(x0, y0, false, false);
    auto s1 = __builtin_amdgcn_permlane32_swap(x1, y1, false, false);
    const u32 ob = WS_O + (pa * 80 + 5 * ja) * 2048 + (32 * qt + l32) * 16;
    st128_sc1(rs, ob + (3 * h + hh) * 2048, u32x4{s0[0], s1[0], s0[1], s1[1]});
    if (hh == 0) st64_sc1(rs, ob + 2 * 2048 + 8 * h, u32x2{pack2(o[8] * inv, o[9] * inv), pack2(o[10] * inv, o[11] * inv)});
    STAMP(5);
  }
  const char* gsrc = ((w & 1) ? wu : wg) + jg * 192;
  auto stageGBp = [&](int c, int rb, int i) {
    dma(gsrc + (size_t)(c * 64 + rb * 16) * 3072, voffG[i], gb_off(c) + (w & 1) * 12288 + rb * 3072 + i * 1024);
  };
  auto stageGB = [&](int c, int rb) { stageGBp(c, rb, 0); stageGBp(c, rb, 1); stageGBp(c, rb, 2); };
  const int wm4 = w & 3, wk = w >> 2, kh = KSWAP ? wk ^ (slice & 1) : wk;
  u32 voffR = g * 2048 + (64 * half + 16 * wm4 + r16) * 16;
  fl_get = flag_off(1, p, r16);
  u32 fl_get2 = flag_off(2, pg, r16), fl_put = lane == 0 ? flag_off(1, pa, ja) : WS_DENSE + bx * 8;
  asm volatile("" : "+v"(fl_put));
  asm volatile("" : "+v"(voffR), "+v"(fl_get), "+v"(fl_get2));
  asm volatile("" ::: "memory");
  if (w >= 4) {
#pragma unroll
    for (int i = NEARLY; i < 20; ++i) dma_sc1(wt + 20480 * (w - 3) + i * 1024, lane * 16, L_WOB + 20480 * (w - 3) + i * 1024);
  }
  bar();
  if (w < 4) {
#pragma unroll
    for (int i = 0; i < 5; ++i) dma_sc1(wt + 5120 * w + i * 1024, lane * 16, L_WOB + 5120 * w + i * 1024);
    dma_sc1(ws + WS_WB + (w < 3 ? w : 2) * 1024, lane * 16, L_WB + (w < 3 ? w : 2) * 1024);
    vm_wait<6>();
  } else {
    if (X_A) vm_wait<0>(); else {
    stageGB(2, w >> 1); stageGB(2, (w >> 1) - 2);
    vm_wait<6>(); }
  }
  bar0();
  STAMP(6);
  u32x2 hp[5];
  {
    if (w == FW && lane < 2) __hip_atomic_store((GAS u64*)(ws + fl_put), tag, RLX_AGENT);
    u32x4 a[10], b[6][5];
    const u32 ad0 = L_WOB + kh * 10 * 5120 + lane * 16, adm = L_WB + (r16 & 1) * 1280 + kh * 640 + g * 16;
    const bool alt = KROT && (slice & 2);
    auto kso = [&](int s) { return alt ? (s < 6 ? 4 + (s + 3) % 6 : (s - 4) & 3) : (s + 4) % 10; };
    auto rdB = [&](int s, int t) { b[s % 6][t] = lds128(ad0 + kso(s) * 5120 + t * 1024); };
#pragma unroll
    for (int s = 0; s < 6; ++s)
#pragma unroll
      for (int t = 0; t < 5; ++t) rdB(s, t);
    asm volatile("" : "+v"(xr[0]), "+v"(xr[1]), "+v"(xr[2]), "+v"(xr[3]), "+v"(xr[4]), "+v"(xr[5]));
    f32x4 hacc[5], macc;
    { const u32 xm = xr[5][0]; macc = f32x4{__uint_as_float(xm), 0.f, 0.f, 0.f}; }
#pragma unroll
    for (int t = 0; t < 5; ++t) { const u32 a0 = xr[t][0], a1 = xr[t][1]; hacc[t] = f32x4{bflo(a0), bfhi(a0), bflo(a1), bfhi(a1)}; }
    SB();
    if (w == PW) poll_flags(ws, fl_get, tag); else if (w < 4) vm_wait<0>();
    bar();
    STAMP(7);
    const char* osrc = ws + WS_O + (p * 80 + 40 * kh) * 2048;
#pragma unroll
    for (int s = 0; s < 10; ++s) gld_sc1(a[s], osrc + kso(s) * 8192, voffR);
    u32x4 bm[2];
    bm[0] = lds128(adm + kso(0) * 64);
    SB();
#pragma unroll
    for (int s = 0; s < 10; ++s) {
      switch (s) {
        case 0: vm_wait_use<9>(a[0]); break; case 1: vm_wait_use<8>(a[1]); break; case 2: vm_wait_use<7>(a[2]); break;
        case 3: vm_wait_use<6>(a[3]); break; case 4: vm_wait_use<5>(a[4]); break; case 5: vm_wait_use<4>(a[5]); break;
        case 6: vm_wait_use<3>(a[6]); break; case 7: vm_wait_use<2>(a[7]); break; case 8: vm_wait_use<1>(a[8]); break;
        default: vm_wait_use<0>(a[9]); break;
      }
      if (s == 0) STAMP(28);
#pragma unroll
      for (int t = 0; t < 5; ++t) {
        hacc[t] = mfma(b[s % 6][t], a[s], hacc[t]);
        SB();
        if (s + 6 < 10) { rdB(s + 6, t); SB(); }
        if (t == 0 && s < 9) { bm[(s + 1) & 1] = lds128(adm + kso(s + 1) * 64); SB(); }
      }
      macc = mfma(bm[s & 1], a[s], macc);
      SB();
    }
    STAMP(8);
    if (wk == 0) {
#pragma unroll
      for (int t = 0; t < 5; ++t) *(LAS f32x4*)(smem + L_RED + (wm4 * 5 + t) * 1024 + lane * 16) = hacc[t];
      *(LAS float*)(smem + L_RED + 20480 + wm4 * 256 + lane * 4) = macc[0] + macc[1];
    }
    bar();
    if (wk == 1) {
      float s1 = 0.f, s2 = 0.f;
      const float mean = macc[0] + macc[1] + *(LAS const float*)(smem + L_RED + 20480 + wm4 * 256 + lane * 4);
      u32x2 hc[5];
#pragma unroll
      for (int t = 0; t < 5; ++t) {
        const f32x4 v = hacc[t] + *(LAS const f32x4*)(smem + L_RED + (wm4 * 5 + t) * 1024 + lane * 16);
        const u32 lo = pack2(v[0], v[1]), hi = pack2(v[2], v[3]);
        hp[t] = u32x2{lo, hi};
        hc[t] = u32x2{pack2(bflo(lo) - mean, bfhi(lo) - mean), pack2(bflo(hi) - mean, bfhi(hi) - mean)};
      }
      const u32 hb = WS_H + (p * 80 + 10 * slice + 2 * (g & 1) + (g >> 1)) * 2048 + (64 * half + 16 * wm4 + r16) * 16;
      st128_sc1(rs, hb, quad2chunk(hc[0], hc[1]));
      st128_sc1(rs, hb + 4 * 2048, quad2chunk(hc[2], hc[3]));
      const u32x4 last = quad2chunk(hc[4], hc[4]);
      if ((g & 1) == 0) st128_sc1(rs, hb + 8 * 2048, last);
#pragma unroll
      for (int t = 0; t < 5; ++t) { const u32 lo = hc[t][0], hi = hc[t][1]; s1 = dot2(hi, ONES2, dot2(lo, ONES2, s1)); s2 = dot2(hi, hi, dot2(lo, lo, s2)); }
      s1 = gsum(s1); s2 = gsum(s2);
      if (g == 0) st64_sc1(rs, WS_ST + (p * 8 + slice) * 1024 + (64 * half + 16 * wm4 + r16) * 8, u32x2{__float_as_uint(s1), __float_as_uint(s2)});
      if (W3BAR) bar();
      vm_wait<0>();
    } else {
#pragma unroll
      for (int t = 0; t < 5; ++t) hp[t] = u32x2{0, 0};
      if (W3BAR) bar();
      if (!X_B) { stageGB(0, w >> 1); stageGB(0, 2 + (w >> 1)); stageGB(1, w >> 1); stageGB(1, 2 + (w >> 1)); }
    }
  }
  bar();
  STAMP(9);
  if (w == FW && lane == 0) __hip_atomic_store((GAS u64*)(ws + flag_off(2, p, j)), tag, RLX_AGENT);
  if (w == PW) {
    poll_flags(ws, fl_get2, tag);
  } else if (w == 2) {
    GAS u64* a = (GAS u64*)(ws + WS_DENSE + lane * 8);
    for (u32 spins = 0; spins < SPIN_CAP; ++spins) {
      const u64 v0 = __hip_atomic_load(a, RLX_AGENT), v1 = __hip_atomic_load(a + 64, RLX_AGENT), v2 = __hip_atomic_load(a + 128, RLX_AGENT), v3 = __hip_atomic_load(a + 192, RLX_AGENT);
      if (__all(v0 == tag && v1 == tag && v2 == tag && v3 == tag)) break;
    }
    if (bx == 0 && lane == 0) __hip_atomic_store((GAS u64*)(ws + WS_EPOCH), tag, RLX_AGENT);
  } else if (wk == 1) {
    if (!X_C) { stageGB(3, w >> 1); stageGB(3, (w >> 1) - 2); }
  }
  bar();
  STAMP(10);

  const int rotd = DROT == 0 ? 0 : (DMAP ? (((3 * (rr & 3)) >> 1) + (DROT == 2 ? ((6 * (rr >> 2)) >> 3) : 0)) % 6 : (((slice * 6) >> 3) + (DROT == 2 ? ((3 * (rr >> 3)) >> 1) : 0)) % 6);
  auto stageDq = [&](int c, int blk, int i) {
    int kc = c + rotd; kc = kc >= 6 ? kc - 6 : kc;
    dma_sc1(wt + (20 + kc * 8 + blk) * 5120 + i * 1024, lane * 16, (c & 3) * 40960 + blk * 5120 + i * 1024);
  };
  auto stageDp = [&](int c, int i) { stageDq(c, w, i); };
  auto stageD = [&](int c) {
#pragma unroll
    for (int i = 0; i < 5; ++i) stageDp(c, i);
  };

  {
    const int wm = w >> 2, wn = w & 3;
    const char* hsrc = ws + WS_H + (pg * 80 + w) * 2048;
    auto stageGAp = [&](int c, int i) { dma_sc1(hsrc + c * 16384 + i * 1024, lane * 16, ga_off(c) + w * 2048 + i * 1024); };
    auto stageGA = [&](int c) { stageGAp(c, 0); stageGAp(c, 1); };
    stageGA(0);
    dma_sc1(ws + WS_ST + (pg * 8 + w) * 1024, lane * 16, L_ST + w * 1024);
    stageGA(1);
    const u32 adA = g * 2048 + (64 * wm + r16) * 16;
    u32 adB[3];
#pragma unroll
    for (int t = 0; t < 3; ++t) {
      const int mat = t == 0 ? 0 : (t == 1 ? 1 : (pq >> 1)), c = t < 2 ? 3 * wn + (pq >> 1) : 3 * wn + 2;
      int cp = c + mat + 2 * (g & 1); cp = cp >= 12 ? cp - 12 : cp;
      adB[t] = mat * 12288 + (8 * g + q4) * 192 + cp * 16 + (pq & 1) * 8;
    }
    f32x4 acc[4][3];
#pragma unroll
    for (int m = 0; m < 4; ++m)
#pragma unroll
      for (int t = 0; t < 3; ++t) acc[m][t] = f32x4{0, 0, 0, 0};
    u32x4 fa[2][4]; u32x2 fb[2][3][2];
    auto rdp = [&](int c, int s, int i) {
      const u32 sa = ga_off(c) + s * 8192, sb = gb_off(c) + s * 6144;
      if (i < 2) fb[s][0][i] = ldstr(sb + adB[0] + i * 768);
      else if (i == 2) fa[s][0] = lds128(sa + adA);
      else if (i < 7) fb[s][1 + ((i - 3) >> 1)][(i - 3) & 1] = ldstr(sb + adB[1 + ((i - 3) >> 1)] + ((i - 3) & 1) * 768);
      else if (i < 10) fa[s][i - 6] = lds128(sa + adA + (i - 6) * 256);
    };
    auto mm = [&](int s, int c) {
      u32x4 b[3];
#pragma unroll
      for (int t = 0; t < 3; ++t) b[t] = cat(fb[s][t][0], fb[s][t][1]);
      const int cr = s == 0 ? c : c + 1;
#pragma unroll
      for (int i = 0; i < 12; ++i) {
        acc[i / 3][i % 3] = mfma(b[i % 3], fa[s][i / 3], acc[i / 3][i % 3]);
        if (cr < 10) rdp(cr, s ^ 1, i);
        if (s == 1 && (i == 4 || i == 10) && c + 3 < 10) stageGAp(c + 3, i == 10);
        if (s == 0 && i % 4 == 3 && c >= 1 && c + 3 < 10) stageGBp(c + 3, w >> 1, i >> 2);
        if (DWARM && s == 1 && i == 7 && c >= 1 && c <= 4 && (w >> 1) == c - 1)
          warm_sc1(ws + WS_WT + w * WT_SLICE + 102400 + rr * 7680, (lane < 60 ? lane : 59) * 128, L_WARM);
        if (s == 1 && (c == 7 || c == 8) && i % 2 == 1 && i < 10) stageDp(c - 7, i >> 1);
        SB();
      }
    };
    vm_wait<3>();
    bar();
    STAMP(19);
#pragma unroll
    for (int i = 0; i < 10; ++i) rdp(0, 0, i);
    SB();
    stageGA(2);
    SB();
#pragma unroll
    for (int c = 0; c < 10; ++c) {
      if (c == 1) {
        const f32x2 u0 = *(LAS const f32x2*)(smem + L_ST + (2 * g) * 1024 + (16 * w + r16) * 8), u1 = *(LAS const f32x2*)(smem + L_ST + (2 * g + 1) * 1024 + (16 * w + r16) * 8);
        const float s1 = gsum(u0[0] + u1[0]), s2 = gsum(u0[1] + u1[1]);
        const float mean = s1 * (1.f / 640.f), var = __builtin_fmaxf(s2 * (1.f / 640.f) - mean * mean, 0.f);
        if (g == 0) *(LAS float*)(smem + L_MISC + (16 * w + r16) * 4) = __builtin_amdgcn_rcpf(var + LN_EPS);
      }
      mm(0, c);
      if (c < 9) {
        if (c == 0) vm_wait<2>(); else if (c == 1) vm_wait<5>(); else if (c <= 6) vm_wait<8>(); else vm_wait<5>();
        bar();
        if (c == 4) STAMP(20);
        if (c == 7) STAMP(30);
      }
      mm(1, c);
    }
    STAMP(11);
    u32x2 p01[4];
    float r2[4];
#pragma unroll
    for (int m = 0; m < 4; ++m) r2[m] = *(LAS const float*)(smem + L_MISC + (64 * wm + 16 * m + r16) * 4);
    if (wm == 1) { vm_wait<5>(); bar(); }
#pragma unroll
    for (int m = 0; m < 4; ++m) {
      const f32x4 v = acc[m][0] * acc[m][1] * r2[m];
      p01[m] = u32x2{pack2(v[0], v[1]), pack2(v[2], v[3])};
    }
    const u32 hb = WS_HH + (pg * 192 + 12 * jg + 3 * wn) * 2048 + (64 * wm + r16) * 16;
    st128_sc1(rs, hb + (g >> 1) * 2048 + (g & 1) * 256, quad2chunk(p01[0], p01[1]));
    st128_sc1(rs, hb + (g >> 1) * 2048 + (2 + (g & 1)) * 256, quad2chunk(p01[2], p01[3]));
    u32x2 p2[2];
#pragma unroll
    for (int mm2 = 0; mm2 < 2; ++mm2) {
      f32x4 v;
#pragma unroll
      for (int r = 0; r < 4; ++r) {
        auto sw = __builtin_amdgcn_permlane32_swap(__float_as_uint(acc[2 * mm2][2][r]), __float_as_uint(acc[2 * mm2 + 1][2][r]), false, false);
        v[r] = __uint_as_float(sw[0]) * __uint_as_float(sw[1]) * (lane < 32 ? r2[2 * mm2] : r2[2 * mm2 + 1]);
      }
      p2[mm2] = u32x2{pack2(v[0], v[1]), pack2(v[2], v[3])};
    }
    st128_sc1(rs, hb + 2 * 2048 + (2 * (g & 1) + (g >> 1)) * 256, quad2chunk(p2[0], p2[1]));
    STAMP(31);
    if (wm == 0) {
      vm_wait<8>();
      bar();
#pragma unroll
      for (int i = 0; i < 10; ++i) stageDq(2, w + 4 * (i / 5), i % 5);
    }
  }

  {
    u32x4 b[6][5];
    const u32 ad0 = kh * 4 * 5120 + lane * 16;
    auto rdB = [&](int s, int t) { b[s % 6][t] = lds128(ad0 + ((s >> 2) & 3) * 40960 + (s & 3) * 5120 + t * 1024); };
#pragma unroll
    for (int s = 0; s < 4; ++s)
#pragma unroll
      for (int t = 0; t < 5; ++t) rdB(s, t);
    SB();
    if (wk == 0) vm_wait<10>(); else vm_wait<0>();
    bar();
    STAMP(12);
    if (w == FW && lane == 0) __hip_atomic_store((GAS u64*)(ws + flag_off(3, pg, jg)), tag, RLX_AGENT);
    if (wk == 1) {
#pragma unroll
      for (int i = 0; i < 10; ++i) if (!X_D) stageDq(3, w - 4 * (i / 5), i % 5);
    }
#pragma unroll
    for (int s = 4; s < 6; ++s)
#pragma unroll
      for (int t = 0; t < 5; ++t) rdB(s, t);
    f32x4 acc[5];
#pragma unroll
    for (int t = 0; t < 5; ++t) { const u32 a = hp[t][0], b2 = hp[t][1]; acc[t] = f32x4{bflo(a), bfhi(a), bflo(b2), bfhi(b2)}; }
    SB();
    if (w == PW) poll_flags(ws, fl_get + 2 * 65536, tag); else if (wk == 0) vm_wait<0>();
    bar();
    STAMP(13);
    u32x4 a[6][4];
    const char* asrc = ws + WS_HH + (p * 192 + 16 * kh) * 2048;
    auto loadAp = [&](int c, int s) {
      int kc = c + rotd; kc = kc >= 6 ? kc - 6 : kc;
      gld_sc1(a[c][s], asrc + kc * 65536 + s * 8192, voffR);
    };
    auto issue = [&](int n) {
      if (n < 4) loadAp(2, n); else if (n < 9) stageDp(4, n - 4); else if (n < 13) loadAp(3, n - 9); else if (n < 18) stageDp(5, n - 13);
      else if (n < 22) loadAp(4, n - 18); else if (n < 26) loadAp(5, n - 22);
    };
#pragma unroll
    for (int s = 0; s < 4; ++s) loadAp(0, s);
#pragma unroll
    for (int s = 0; s < 4; ++s) loadAp(1, s);
#pragma unroll
    for (int s = 0; s < 24; ++s) {
      if (s == 0) vm_wait<4>();
      if (s == 4) vm_wait<8>();
      if (s == 8) vm_wait<12>();
      if (s == 10) vm_wait<11>();
      if (s == 12) vm_wait<11>();
      if (s == 14) vm_wait<8>();
      if (s == 16) vm_wait<4>();
      if (s == 20) vm_wait<0>();
      if (s % 4 == 0) asm volatile("" : "+v"(a[s >> 2][0]), "+v"(a[s >> 2][1]), "+v"(a[s >> 2][2]), "+v"(a[s >> 2][3]));
      if (s == 6) bar();
      if (s == 10 || s == 14) bar0();
      if (s == 0) STAMP(21);
      if (s == 4) STAMP(22); if (s == 8) STAMP(23); if (s == 12) STAMP(24); if (s == 16) STAMP(25);
#pragma unroll
      for (int t = 0; t < 5; ++t) {
        acc[t] = mfma(b[s % 6][t], a[s >> 2][s & 3], acc[t]);
        SB();
        if (s + 6 < 24) { rdB(s + 6, t); SB(); }
        if (t == 1) { issue(2 * s); SB(); }
        if (t == 4) { issue(2 * s + 1); SB(); }
      }
    }
    STAMP(14);
    constexpr u32 L_RED2 = 2 * 40960;
    if (wk == 0) {
#pragma unroll
      for (int t = 0; t < 5; ++t) *(LAS f32x4*)(smem + L_RED2 + (wm4 * 5 + t) * 1024 + lane * 16) = acc[t];
    }
    bar();
    if (wk == 1) {
      u32x2 op[5];
#pragma unroll
      for (int t = 0; t < 5; ++t) {
        const f32x4 v = acc[t] + *(LAS const f32x4*)(smem + L_RED2 + (wm4 * 5 + t) * 1024 + lane * 16);
        op[t] = u32x2{pack2(v[0], v[1]), pack2(v[2], v[3])};
      }
      const __amdgpu_buffer_rsrc_t ro = __builtin_amdgcn_make_buffer_rsrc(out, 0, 2048 * 1280, 0x00020000);
      const u32 oo = (128 * p + 64 * half + 16 * wm4 + r16) * 1280 + slice * 160 + (2 * (g & 1) + (g >> 1)) * 16;
      __builtin_amdgcn_raw_buffer_store_b128(quad2chunk(op[0], op[1]), ro, (int)oo, 0, OUT_AUX);
      __builtin_amdgcn_raw_buffer_store_b128(quad2chunk(op[2], op[3]), ro, (int)oo + 64, 0, OUT_AUX);
      const u32x4 last = quad2chunk(op[4], op[4]);
      if ((g & 1) == 0) __builtin_amdgcn_raw_buffer_store_b128(last, ro, (int)oo + 128, 0, OUT_AUX);
    }
    STAMP(15);
  }
}

extern "C" void kernel_launch(void* const* d_in, const int* in_sizes, int n_in, void* d_out, int out_size, void* d_ws, size_t ws_size, hipStream_t stream) {
  (void)in_sizes; (void)n_in; (void)out_size; (void)ws_size;
  (void)hipFuncSetAttribute((const void*)fused_block, hipFuncAttributeMaxDynamicSharedMemorySize, LDS_BYTES);
  fused_block<<<256, 512, LDS_BYTES, stream>>>((const char*)d_in[0], (const char*)d_in[1], (const char*)d_in[2], (const char*)d_in[4], (const char*)d_in[5],
                                               (const char*)d_in[6], (char*)d_out, (char*)d_ws);
}
```

```cpp
#include <hip/hip_runtime.h>
#include <stdint.h>

typedef unsigned short u16;
typedef unsigned int u32;
typedef unsigned long long u64;
typedef __bf16 bf16x8 __attribute__((ext_vector_type(8)));
typedef __bf16 bf16x2 __attribute__((ext_vector_type(2)));
typedef short s16x4 __attribute__((ext_vector_type(4)));
typedef float f32x4 __attribute__((ext_vector_type(4)));
typedef float f32x2 __attribute__((ext_vector_type(2)));
typedef u32 u32x4 __attribute__((ext_vector_type(4)));
typedef u32 u32x2 __attribute__((ext_vector_type(2)));
#define LAS __attribute__((address_space(3)))
#define GAS __attribute__((address_space(1)))
#define DI __device__ __forceinline__
#define RLX_AGENT __ATOMIC_RELAXED, __HIP_MEMORY_SCOPE_AGENT

#define RG 5
#define OUT_AUX 0
#define ATT_PRIO 0
#define STAMPS 0

constexpr u32 WS_EPOCH = 0;
constexpr u32 WS_DENSE = 2048;
constexpr u32 WS_FLAG = 4096;
constexpr u32 WS_QKV = WS_FLAG + 4 * 256 * 256;
constexpr u32 WS_O = WS_QKV + 16 * 240 * 2048;
constexpr u32 WS_H = WS_O + 16 * 80 * 2048;
constexpr u32 WS_HH = WS_H + 16 * 80 * 2048;
constexpr u32 WS_ST = WS_HH + 16 * 192 * 2048;
constexpr u32 WS_RS = WS_ST + 16 * 8 * 1024;
constexpr u32 WS_WB = WS_RS + 2048 * 16;
constexpr u32 WT_SLICE = 68 * 5120;
constexpr u32 WS_WT = WS_WB + 3072;
constexpr u32 WS_END = WS_WT + 8 * WT_SLICE;
constexpr u32 WS_STAMP = WS_END;
constexpr u64 TAG_STEP = 0x9E3779B97F4A7C15ull;

constexpr u32 LDS_BYTES = 163840;
constexpr u32 L_MISC = 161792;
constexpr u32 L_K = 0, KCH = 6144;
constexpr u32 L_V = 6 * KCH, VCH = 6208;
constexpr u32 L_WOB = L_MISC - 102400;
__device__ constexpr u32 gb_off(int c) { return (c & 3) == 2 ? 0 : ((c & 3) == 3 ? 24576 : ((c & 3) == 0 ? 65536 : 106496)); }
__device__ constexpr u32 ga_off(int c) { return c % 3 == 1 ? 49152 : (c % 3 == 2 ? 90112 : 131072); }
constexpr u32 L_ST = 147456;
constexpr u32 L_WB = 49152;
constexpr u32 L_RED = 24576;
constexpr u32 L_WARM = L_MISC + 1792;
constexpr u32 L_RT = 131072;
constexpr u32 SPIN_CAP = 1u << 15;
constexpr float LN_EPS = 1e-5f;

extern __shared__ __attribute__((aligned(16))) char smem[];

DI u32x4 lds128(u32 off) { return *(LAS const u32x4*)(smem + off); }
DI u32x2 ldstr(u32 off) { return __builtin_bit_cast(u32x2, __builtin_amdgcn_ds_read_tr16_b64_v4i16((LAS s16x4*)(smem + off))); }
DI u32x4 ldstr2(u32 lo, u32 hi) { const u32x2 a = ldstr(lo), b = ldstr(hi); return u32x4{a[0], a[1], b[0], b[1]}; }
DI f32x4 mfma(u32x4 a, u32x4 b, f32x4 c) { return __builtin_amdgcn_mfma_f32_16x16x32_bf16(__builtin_bit_cast(bf16x8, a), __builtin_bit_cast(bf16x8, b), c, 0, 0, 0); }
typedef float f32x16 __attribute__((ext_vector_type(16)));
DI f32x16 mfma32(u32x4 a, u32x4 b, f32x16 c) { return __builtin_amdgcn_mfma_f32_32x32x16_bf16(__builtin_bit_cast(bf16x8, a), __builtin_bit_cast(bf16x8, b), c, 0, 0, 0); }
DI float max3(float a, float b, float c) { return __builtin_fmaxf(__builtin_fmaxf(a, b), c); }
DI f32x4 mfma16(u32x2 a, u32x2 b, f32x4 c) { return __builtin_amdgcn_mfma_f32_16x16x16bf16_1k(__builtin_bit_cast(s16x4, a), __builtin_bit_cast(s16x4, b), c, 0, 0, 0); }
DI float dot2(u32 a, u32 b, float c) { return __builtin_amdgcn_fdot2_f32_bf16(__builtin_bit_cast(bf16x2, a), __builtin_bit_cast(bf16x2, b), c, false); }
DI u32 pack2(float lo, float hi) { const f32x2 v = {lo, hi}; return __builtin_bit_cast(u32, __builtin_convertvector(v, bf16x2)); }
DI float bflo(u32 v) { return __uint_as_float(v << 16); }
DI float bfhi(u32 v) { return __uint_as_float(v & 0xffff0000u); }
constexpr u32 ONES2 = 0x3f803f80u;
DI float sum8(u32x4 a, float s) { const u32 a0 = a[0], a1 = a[1], a2 = a[2], a3 = a[3]; return dot2(a3, ONES2, dot2(a2, ONES2, dot2(a1, ONES2, dot2(a0, ONES2, s)))); }
DI float sq8(u32x4 a, float s) { const u32 a0 = a[0], a1 = a[1], a2 = a[2], a3 = a[3]; return dot2(a3, a3, dot2(a2, a2, dot2(a1, a1, dot2(a0, a0, s)))); }
DI float gsum(float x) {
  auto a = __builtin_amdgcn_permlane16_swap(__float_as_uint(x), __float_as_uint(x), false, false);
  x = __uint_as_float(a[0]) + __uint_as_float(a[1]);
  auto b = __builtin_amdgcn_permlane32_swap(__float_as_uint(x), __float_as_uint(x), false, false);
  return __uint_as_float(b[0]) + __uint_as_float(b[1]);
}
DI float gmax(float x) {
  auto a = __builtin_amdgcn_permlane16_swap(__float_as_uint(x), __float_as_uint(x), false, false);
  x = __builtin_fmaxf(__uint_as_float(a[0]), __uint_as_float(a[1]));
  auto b = __builtin_amdgcn_permlane32_swap(__float_as_uint(x), __float_as_uint(x), false, false);
  return __builtin_fmaxf(__uint_as_float(b[0]), __uint_as_float(b[1]));
}
DI u32x4 quad2chunk(u32x2 A, u32x2 B) {
  const u32 a0 = A[0], a1 = A[1], b0 = B[0], b1 = B[1];
  auto s0 = __builtin_amdgcn_permlane16_swap(a0, b0, false, false);
  auto s1 = __builtin_amdgcn_permlane16_swap(a1, b1, false, false);
  return u32x4{s0[0], s1[0], s0[1], s1[1]};
}
DI void dma(const void* sbase, u32 voff, u32 lds) {
  asm volatile("s_mov_b32 m0, %2\n\ts_nop 0\n\tglobal_load_lds_dwordx4 %0, %1" ::"v"(voff), "s"(sbase), "s"(lds));
}
DI void dma_sc1(const void* sbase, u32 voff, u32 lds) {
  asm volatile("s_mov_b32 m0, %2\n\ts_nop 0\n\tglobal_load_lds_dwordx4 %0, %1 sc1" ::"v"(voff), "s"(sbase), "s"(lds));
}
DI void gld_sc1(u32x4& dst, const void* sbase, u32 voff) {
  asm volatile("global_load_dwordx4 %0, %1, %2 sc1" : "=v"(dst) : "v"(voff), "s"(sbase));
}
DI void gld(u32x4& dst, const void* sbase, u32 voff) {
  asm volatile("global_load_dwordx4 %0, %1, %2" : "=v"(dst) : "v"(voff), "s"(sbase));
}
DI void gld64(u32x2& dst, const void* sbase, u32 voff) {
  asm volatile("global_load_dwordx2 %0, %1, %2" : "=v"(dst) : "v"(voff), "s"(sbase));
}
template <int N> DI void vm_wait() { asm volatile("s_waitcnt vmcnt(%0)" ::"n"(N) : "memory"); }
template <int N> DI void vm_wait_use(u32x4& r) { asm volatile("s_waitcnt vmcnt(%1)" : "+v"(r) : "n"(N)); }
#define SB() __builtin_amdgcn_sched_barrier(0)
DI void warm(const void* sbase, u32 voff, u32 lds) {
  asm volatile("s_mov_b32 m0, %2\n\ts_nop 0\n\tglobal_load_lds_dword %0, %1" ::"v"(voff), "s"(sbase), "s"(lds));
}
DI void bar0() { asm volatile("s_barrier" ::: "memory"); }
DI u32x4 cat(u32x2 a, u32x2 b) { return u32x4{a[0], a[1], b[0], b[1]}; }
DI void bar() { asm volatile("s_waitcnt lgkmcnt(0)\n\ts_barrier" ::: "memory"); }
DI void st128_sc1(__amdgpu_buffer_rsrc_t rs, u32 off, u32x4 v) { __builtin_amdgcn_raw_buffer_store_b128(v, rs, (int)off, 0, 16); }
DI void st64_sc1(__amdgpu_buffer_rsrc_t rs, u32 off, u32x2 v) { __builtin_amdgcn_raw_buffer_store_b64(v, rs, (int)off, 0, 16); }

DI void poll_flags(const char* ws, u32 off, u64 tag) {
  GAS u64* a = (GAS u64*)(ws + off);
  for (u32 spins = 0; spins < SPIN_CAP; ++spins) {
    const u64 v = __hip_atomic_load(a, RLX_AGENT);
    if (__all(v == tag)) break;
  }
  __builtin_amdgcn_fence(__ATOMIC_ACQUIRE, "wavefront");
}
DI u32 flag_off(int phase, int p, int j) { return WS_FLAG + (u32)((phase * 256 + p * 16 + j) * 256); }

#define STAMP(i) do {} while (0)

__global__ __launch_bounds__(512, 2) void fused_block(const char* __restrict__ x, const char* __restrict__ wqkv, const char* __restrict__ wo,
                                                      const char* __restrict__ wg, const char* __restrict__ wu, const char* __restrict__ wd,
                                                      char* __restrict__ out, char* ws) {
  const int tid = threadIdx.x, lane = tid & 63, w = __builtin_amdgcn_readfirstlane(tid >> 6);
  const int r16 = lane & 15, g = lane >> 4, q4 = r16 >> 2, pq = r16 & 3;
  const int bx = blockIdx.x, xcd = bx & 7, rr = bx >> 3;
  const int p = (xcd >> 1) * 4 + (rr >> 3), j = (xcd & 1) * 8 + (rr & 7);
  const int pa = rr >> 1, ja = 2 * xcd + (rr & 1);
  const int pg = 8 * (xcd >> 2) + (rr >> 2), jg = 4 * (xcd & 3) + (rr & 3);
  const int half = j >> 3, slice = j & 7;
  constexpr int PW = 3;
  const __amdgpu_buffer_rsrc_t rs = __builtin_amdgcn_make_buffer_rsrc(ws, 0, WS_END, 0x00020000);
  STAMP(0);
  u64 tag;
  auto load_epoch = [&](u32x2& e) { const u32 z = 0; asm volatile("global_load_dwordx2 %0, %1, %2 sc1" : "=v"(e) : "v"(z), "s"(ws + WS_EPOCH)); };
  auto make_tag = [&](u32x2& e) {
    asm volatile("" : "+v"(e));
    const u32 e0 = e[0], e1 = e[1];
    tag = (((u64)(u32)__builtin_amdgcn_readfirstlane(e1) << 32) | (u32)__builtin_amdgcn_readfirstlane(e0)) + TAG_STEP;
  };
  const char* xa = x + (size_t)p * 128 * 1280;
  const u32x4 ones8 = {ONES2, ONES2, ONES2, ONES2};
  const char* wt = ws + WS_WT + slice * WT_SLICE;
  auto rt_addr = [&](int piece, int gi, u32 slot, u32& l, u32& d) {
    const int tile = 4 * piece + g, sl = tile / 5, t = tile - sl * 5;
    l = slot + q4 * 1280 + tile * 32 + pq * 8;
    d = WS_WT + sl * WT_SLICE + (gi >> 2) * 5120 + t * 1024 + (gi & 3) * 256 + r16 * 16;
  };

  if (j < 15) {
    const int wm = w & 1, wn = w >> 1;
    u32 voffA[2], voffB[2];
#pragma unroll
    for (int i = 0; i < 2; ++i) {
      const int ra = 16 * w + 8 * i + (lane >> 3);
      voffA[i] = ra * 1280 + (((lane & 7) ^ ((ra >> 1) & 7)) << 4);
      const int rb = 8 * w + 4 * i + g, f = (rb & 3) | (((rb >> 3) & 1) << 2);
      voffB[i] = rb * 3840 + (((((r16 >> 1) ^ f) << 1) | (r16 & 1)) << 4);
    }
    const char* wb = wqkv + j * 256;
    const int rot = RG == 10 ? ((rr & 7) + ((rr & 7) >> 2) + ((5 * (rr >> 3)) >> 1)) % 10 : ((rr & 7) + (rr >> 3)) % RG;
    auto stage = [&](int c, int piece) {
      int kc = c % RG + rot; kc = (kc >= RG ? kc - RG : kc) + (c / RG) * RG;
      const u32 slot = (c & 3) * 32768;
      const char* a = xa + kc * 128; const char* b = wb + (size_t)kc * 64 * 3840;
      if (piece < 0 || piece == 0) dma(a, voffA[0], slot + w * 2048);
      if (piece < 0 || piece == 1) dma(a, voffA[1], slot + w * 2048 + 1024);
      if (piece < 0 || piece == 2) dma(b, voffB[0], slot + 16384 + w * 2048);
      if (piece < 0 || piece == 3) dma(b, voffB[1], slot + 16384 + w * 2048 + 1024);
    };
    stage(0, -1);
    u32x2 epoch;
    load_epoch(epoch);
    {
      u64 pc; asm volatile("s_getpc_b64 %0" : "=s"(pc));
      if (w == 0) warm((const char*)(pc & ~127ull) + rr * 1280, (lane < 10 ? lane : 9) * 128, L_WARM);
    }
    stage(1, -1);
    u32 adA[2], adB[2];
#pragma unroll
    for (int ks = 0; ks < 2; ++ks) adA[ks] = (64 * wm + r16) * 128 + (((4 * ks + g) ^ ((r16 >> 1) & 7)) << 4);
#pragma unroll
    for (int n = 0; n < 2; ++n) adB[n] = 16384 + (8 * g + q4) * 256 + (((2 * wn + (n ^ wm)) ^ (q4 | ((g & 1) << 2))) << 5) + pq * 8;
    f32x4 acc[4][2], csum = {0, 0, 0, 0};
#pragma unroll
    for (int m = 0; m < 4; ++m) { acc[m][0] = f32x4{0, 0, 0, 0}; acc[m][1] = f32x4{0, 0, 0, 0}; }
    u32x4 fa[2][4]; u32x2 fb[2][2][2];
    u32x4 rsg[4];
    auto ld_rs = [&]() {
#pragma unroll
      for (int m = 0; m < 4; ++m) gld_sc1(rsg[m], ws + WS_RS + (p * 128 + 64 * wm + 16 * m) * 16, r16 * 16);
    };
    auto rdp = [&](int c, int ks, int i) {
      const u32 slot = (c & 3) * 32768;
      if (i == 0 || i == 1) fb[ks][0][i] = ldstr(slot + adB[0] + ks * 8192 + i * 1024);
      else if (i == 2) fa[ks][0] = lds128(slot + adA[ks]);
      else if (i == 3 || i == 4) fb[ks][1][i - 3] = ldstr(slot + adB[1] + ks * 8192 + (i - 3) * 1024);
      else if (i < 8) fa[ks][i - 4] = lds128(slot + adA[ks] + (i - 4) * 2048);
    };
    auto mm = [&](int ks, int cr, int cn) {
      const u32x4 b0 = cat(fb[ks][0][0], fb[ks][0][1]), b1 = cat(fb[ks][1][0], fb[ks][1][1]);
#pragma unroll
      for (int i = 0; i < 9; ++i) {
        if (i < 8) acc[i >> 1][i & 1] = mfma((i & 1) ? b1 : b0, fa[ks][i >> 1], acc[i >> 1][i & 1]);
        else csum = mfma(b0, ones8, csum);
        if (cr < 10) rdp(cr, ks ^ 1, i);
        if (cn < 10 && i == 4) stage(cn, 2 * (ks ^ 1));
        if (cn < 10 && i == 8) stage(cn, 2 * (ks ^ 1) + 1);
        SB();
      }
    };
    if (tid < 8) *(LAS u32*)(smem + L_MISC + tid * 4) = 0;
    vm_wait<4>();
    make_tag(epoch);
    bar();
    STAMP(16);
#pragma unroll
    for (int i = 0; i < 8; ++i) rdp(0, 0, i);
    SB();
    stage(2, 0); stage(2, 1);
    SB();
#pragma unroll
    for (int c = 0; c < 10; ++c) {
      mm(0, c, c + 2);
      if (c < 9) {
        if (c <= 7) vm_wait<4>(); else vm_wait<0>();
        bar0();
        if (c == 8) {
          ld_rs();
          if (j < 12 && w < 5) {
            dma(wd + (p * 12 + j) * 10240 + w * 2048, lane * 16, L_RT + w * 2048);
            dma(wd + (p * 12 + j) * 10240 + w * 2048 + 1024, lane * 16, L_RT + w * 2048 + 1024);
          }
          SB();
        }
      }
      mm(1, c + 1, c + 3);
    }
    STAMP(1);
    if (r16 == 0) *(LAS f32x4*)(smem + L_MISC + 1024 + (32 * wn + 16 * wm + 4 * g) * 4) = csum;
    asm volatile("s_waitcnt lgkmcnt(0)" ::: "memory");
    if (lane == 0) *(volatile LAS u32*)(smem + L_MISC + w * 4) = 1;
    if (j < 12 && w < 5) vm_wait<2>(); else vm_wait<0>();
    for (u32 spins = 0; spins < SPIN_CAP; ++spins) {
      asm volatile("" : "+v"(rsg[0]), "+v"(rsg[1]), "+v"(rsg[2]), "+v"(rsg[3]));
      bool ok = true;
#pragma unroll
      for (int m = 0; m < 4; ++m) ok = ok && rsg[m][1] == (u32)tag && rsg[m][3] == (u32)(tag >> 32);
      if (__all(ok)) break;
      ld_rs();
      vm_wait<0>();
    }
    asm volatile("" : "+v"(rsg[0]), "+v"(rsg[1]), "+v"(rsg[2]), "+v"(rsg[3]));
    while (*(volatile LAS u32*)(smem + L_MISC + (w ^ 1) * 4) == 0) {}
    asm volatile("" ::: "memory");
    STAMP(17);
    const float qs = j < 5 ? 0.22360679774997896f * 1.4426950408889634f : 1.f;
    f32x4 cs[2];
#pragma unroll
    for (int n = 0; n < 2; ++n) cs[n] = n == 0 ? csum : *(LAS const f32x4*)(smem + L_MISC + 1024 + (32 * wn + 16 * (1 ^ wm) + 4 * g) * 4);
    const u32 qb = WS_QKV + (p * 240 + 16 * j + 4 * wn + 2 * ((g & 1) ^ wm) + (g >> 1)) * 2048 + (64 * wm + r16) * 16;
#pragma unroll
    for (int m = 0; m < 4; ++m) {
      const u32 um = rsg[m][0], ur = rsg[m][2];
      const float mean = __uint_as_float(um), sc = __uint_as_float(ur) * qs;
      u32x2 pk[2];
#pragma unroll
      for (int n = 0; n < 2; ++n) {
        const f32x4 v = (acc[m][n] - mean * cs[n]) * sc;
        pk[n] = u32x2{pack2(v[0], v[1]), pack2(v[2], v[3])};
      }
      st128_sc1(rs, qb + m * 256, quad2chunk(pk[0], pk[1]));
    }
  } else {
    u32x2 epoch;
    load_epoch(epoch);
    if (w < 5) {
#pragma unroll
      for (int i = 0; i < 10; ++i) dma(wo + (p * 5 + w) * 10240 + i * 1024, lane * 16, w * 10240 + i * 1024);
    }
    u32x4 xv[2][10];
#pragma unroll
    for (int t = 0; t < 10; ++t)
#pragma unroll
      for (int s2 = 0; s2 < 2; ++s2) gld(xv[s2][t], xa + (16 * w + 8 * s2) * 1280 + t * 128, (lane >> 3) * 1280 + (lane & 7) * 16);
    float s1[2] = {0.f, 0.f}, sq[2] = {0.f, 0.f};
#pragma unroll
    for (int t = 0; t < 10; ++t) {
      if (t == 0) vm_wait<10>();
      if (t == 5) vm_wait<4>();
      if (t == 8) vm_wait<0>();
#pragma unroll
      for (int s2 = 0; s2 < 2; ++s2) { asm volatile("" : "+v"(xv[s2][t])); s1[s2] = sum8(xv[s2][t], s1[s2]); sq[s2] = sq8(xv[s2][t], sq[s2]); }
    }
    make_tag(epoch);
#pragma unroll
    for (int o = 1; o < 8; o <<= 1) { s1[0] += __shfl_xor(s1[0], o); sq[0] += __shfl_xor(sq[0], o); s1[1] += __shfl_xor(s1[1], o); sq[1] += __shfl_xor(sq[1], o); }
    {
      const float a1 = (lane & 1) ? s1[1] : s1[0], a2 = (lane & 1) ? sq[1] : sq[0];
      const float mean = a1 * (1.f / 640.f), var = __builtin_fmaxf(a2 * (1.f / 640.f) - mean * mean, 0.f);
      if ((lane & 7) < 2)
        st128_sc1(rs, WS_RS + (p * 128 + 16 * w + 8 * (lane & 1) + (lane >> 3)) * 16, u32x4{__float_as_uint(mean), (u32)tag, __float_as_uint(__builtin_amdgcn_rsqf(var + LN_EPS)), (u32)(tag >> 32)});
    }
    STAMP(16);
    bar();
    {
#pragma unroll
      for (int i = 0; i < 7; ++i) {
        const int id = w + 8 * i, gr = id / 10;
        if (id < 50) { u32 l, d; rt_addr(id - 10 * gr, p * 5 + gr, gr * 10240, l, d); st128_sc1(rs, d, ldstr2(l, l + 5120)); }
      }
      if (w >= 3) {
        float a1 = 0.f;
#pragma unroll
        for (int t = 0; t < 10; ++t) a1 = sum8(lds128((w - 3) * 10240 + (lane >> 3) * 1280 + t * 128 + (lane & 7) * 16), a1);
#pragma unroll
        for (int o = 1; o < 8; o <<= 1) a1 += __shfl_xor(a1, o);
        a1 *= (1.f / 640.f);
        const u32 hi = pack2(a1, 0.f), lo = pack2(a1 - bflo(hi), 0.f);
        if ((lane & 7) < 2) __builtin_amdgcn_raw_buffer_store_b16((u16)((lane & 1) ? lo : hi), rs, (int)(WS_WB + (lane & 1) * 1280 + ((p * 5 + w - 3) * 8 + (lane >> 3)) * 2), 0, 16);
      }
    }
  }
  STAMP(18);
  vm_wait<0>();
  bar();
  STAMP(2);
  if (w == PW && lane == 0) __hip_atomic_store((GAS u64*)(ws + flag_off(0, p, j)), tag, RLX_AGENT);
  asm volatile("" ::: "memory");
  u32 kvsrc[8], kvdst[8];
#pragma unroll
  for (int i = 0; i < 8; ++i) {
    const int id = w + 8 * i, kv = id >= 30, r = id - 30 * kv, c = r / 6, r2 = r - 6 * c;
    int pp = pa - 2 + (r2 >> 1); pp = pp < 0 ? 0 : pp;
    kvsrc[i] = WS_QKV + (pp * 240 + 80 * (1 + kv) + 5 * ja + c) * 2048 + (r2 & 1) * 1024;
    kvdst[i] = (kv ? L_V + c * VCH : L_K + c * KCH) + r2 * 1024;
    asm volatile("" : "+s"(kvsrc[i]), "+s"(kvdst[i]));
  }
  if (tid < 384) {
    const bool valid = 128 * (pa - 2) + tid >= 0;
    *(LAS u32x4*)(smem + L_K + 5 * KCH + tid * 16) = u32x4{valid ? 0u : 0xf149u, 0u, ONES2, ONES2};
  }
  u32 rtl[2], rtg[2];
#pragma unroll
  for (int i = 0; i < 2; ++i) { rt_addr(2 * w + i, 80 + p * 12 + j, L_RT, rtl[i], rtg[i]); asm volatile("" : "+v"(rtl[i]), "+v"(rtg[i])); }
  u32 fl_put, fl_get;
  {
    const int e = r16 < 14 ? r16 : 0, sec = e < 2 ? 0 : (e < 8 ? 1 : 2), i = e < 2 ? e : (e - 2) % 6;
    int pp = e < 2 ? pa : pa - (i >> 1); pp = pp < 0 ? 0 : pp;
    fl_get = (g & 1) ? flag_off(0, r16, 15) : flag_off(0, pp, (80 * sec + 5 * ja + 4 * (i & 1)) >> 4);
    fl_put = lane == 0 ? flag_off(1, pa, ja) : WS_DENSE + bx * 8;
    asm volatile("" : "+v"(fl_get), "+v"(fl_put));
  }
  if (w == PW) poll_flags(ws, fl_get, tag);
  bar();
  STAMP(3);

  u32x2 xr[6];
  u32 voffG[3];
  {
    const int h = w & 1, qt = w >> 1, l32 = lane & 31, hh = lane >> 5;
    u32x4 qf[2];
    gld_sc1(qf[0], ws + WS_QKV + (pa * 240 + 5 * ja + 2 * h) * 2048 + qt * 512, hh * 2048 + l32 * 16);
    gld_sc1(qf[1], ws + WS_QKV + (pa * 240 + 5 * ja + 2 * h + 2) * 2048 + qt * 512, l32 * 16);
#pragma unroll
    for (int i = 0; i < 5; ++i) dma_sc1(ws + kvsrc[i], lane * 16, kvdst[i]);
    u32 dm[8];
#pragma unroll
    for (int m = 0; m < 8; ++m) {
      const int k0 = ((2 * m) & 3) + 8 * (m >> 1) + 4 * hh;
      dm[m] = (k0 <= l32 ? 0xffffu : 0u) | (k0 + 1 <= l32 ? 0xffff0000u : 0u);
      asm volatile("" : "+v"(dm[m]));
    }
    u32 adK0 = L_K + (2 * h + hh) * KCH + (32 * qt + l32) * 16, adK1 = L_K + (hh ? 5 : 2 * h + 2) * KCH + (32 * qt + l32) * 16;
    const u32 vrow = 32 * qt + 4 * hh + q4;
    u32 adV = (g & 1) == 0 ? L_V + (3 * h + (pq >> 1)) * VCH + vrow * 16 + (pq & 1) * 8
                           : (pq == 0 ? L_V + 2 * VCH + vrow * 16 + 8 * h : L_K + 5 * KCH + ((4 * hh + (h ? 4 : 12)) & 15) * 16 + 8);
    u32 voffwarm;
    { const int l6 = lane < 30 ? lane : 29, rw = l6 / 6; voffwarm = rw * 3072 + (l6 - rw * 6) * 128; }
#pragma unroll
    for (int i = 0; i < 3; ++i) {
      const int s = 64 * i + lane, rw = s / 12, cp = s - rw * 12;
      int c = cp - (w & 1) - 2 * ((rw >> 3) & 1); c = c < 0 ? c + 12 : c;
      voffG[i] = rw * 3072 + c * 16;
    }
    asm volatile("" : "+v"(voffwarm), "+v"(adK0), "+v"(adK1), "+v"(adV), "+v"(voffG[0]), "+v"(voffG[1]), "+v"(voffG[2]));
    vm_wait<1>();
    asm volatile("" : "+v"(qf[0]), "+v"(qf[1]));
    bar();
    STAMP(4);
    const f32x16 zero16 = {0, 0, 0, 0, 0, 0, 0, 0, 0, 0, 0, 0, 0, 0, 0, 0};
    u32x4 kf[9][2];
    constexpr int ord[9] = {0, 8, 1, 2, 3, 4, 5, 6, 7};
#pragma unroll
    for (int n = 0; n < 4; ++n) { kf[ord[n]][0] = lds128(adK0 + 512 * ord[n]); kf[ord[n]][1] = lds128(adK1 + 512 * ord[n]); }
    SB();
#pragma unroll
    for (int i = 5; i < 8; ++i)
      if (i < 7 || w < 4) dma_sc1(ws + kvsrc[i], lane * 16, kvdst[i]);
    if (h == 1 && hh == 0) { qf[0][0] = 0; qf[0][1] = 0; }
    if (h == 0) { qf[1][2] = 0; qf[1][3] = 0; }
    if (hh == 1) qf[1] = u32x4{0x3f80u, 0, 0, 0};
    SB();
    f32x16 sc[9];
    float m4[4];
    auto qk = [&](int i, int st) { sc[i] = mfma32(kf[i][st], qf[st], st == 0 ? zero16 : sc[i]); };
    qk(0, 0); qk(8, 0); qk(0, 1); qk(8, 1);
    SB();
#pragma unroll
    for (int n = 2; n < 11; ++n) {
      if (n < 9) { qk(ord[n], 0); SB(); }
      if (n + 2 < 9) { kf[ord[n + 2]][0] = lds128(adK0 + 512 * ord[n + 2]); SB(); }
      if (n == 3) {
#pragma unroll
        for (int r = 6; r < 16; ++r) sc[0][r] = ((r & 3) + 8 * (r >> 2) + 4 * hh <= l32) ? sc[8][r] : sc[0][r];
      } else if (n > 3) {
#pragma unroll
        for (int c = 0; c < 4; ++c) m4[c] = max3(m4[c], sc[n - 3][2 * c], sc[n - 3][2 * c + 1]);
      }
      SB();
      if (n < 9) { qk(ord[n], 1); SB(); }
      if (n + 2 < 9) { kf[ord[n + 2]][1] = lds128(adK1 + 512 * ord[n + 2]); SB(); }
      if (n == 2) {
#pragma unroll
        for (int r = 0; r < 6; ++r) sc[0][r] = ((r & 3) + 8 * (r >> 2) + 4 * hh <= l32) ? sc[8][r] : sc[0][r];
      } else if (n == 3) {
#pragma unroll
        for (int c = 0; c < 4; ++c) m4[c] = max3(max3(sc[0][4 * c], sc[0][4 * c + 1], sc[0][4 * c + 2]), sc[0][4 * c + 3], sc[0][4 * c + 3]);
      } else {
#pragma unroll
        for (int c = 0; c < 4; ++c) m4[c] = max3(m4[c], sc[n - 3][8 + 2 * c], sc[n - 3][9 + 2 * c]);
      }
      SB();
    }
    float mx = __builtin_fmaxf(max3(m4[0], m4[1], m4[2]), m4[3]);
    { auto sw = __builtin_amdgcn_permlane32_swap(__float_as_uint(mx), __float_as_uint(mx), false, false); mx = __builtin_fmaxf(__uint_as_float(sw[0]), __uint_as_float(sw[1])); }
    u32x4 nmf;
    {
      const float nmx = -mx;
      const u32 p0 = __float_as_uint(nmx) & 0xffff0000u;
      const float r1 = nmx - __uint_as_float(p0);
      const u32 p1 = __float_as_uint(r1) & 0xffff0000u;
      const u32 p2 = __float_as_uint(r1 - __uint_as_float(p1));
      nmf = u32x4{hh ? p2 >> 16 : (p0 >> 16) | p1, 0, 0, 0};
    }
    auto submax = [&](int i) { sc[i] = mfma32(ones8, nmf, sc[i]); };
    submax(0); submax(1);
    SB();
    u32x4 rt0, rt1;
    if (j < 12 && w < 5) { rt0 = ldstr2(rtl[0], rtl[0] + 5120); rt1 = ldstr2(rtl[1], rtl[1] + 5120); }
    STAMP(29);
    vm_wait<0>();
    bar();
    STAMP(26);
    if (w >= 4) {
#pragma unroll
      for (int t = 0; t < 5; ++t) gld64(xr[t], xa + (64 * half + 16 * (w - 4)) * 1280 + slice * 160 + t * 32, r16 * 1280 + g * 8);
      asm volatile("global_load_dwordx2 %0, %1, %2 sc1" : "=v"(xr[5]) : "v"(r16 * 16), "s"(ws + WS_RS + (p * 128 + 64 * half + 16 * (w - 4)) * 16));
    } else {
#pragma unroll
      for (int t = 0; t < 6; ++t) asm volatile("v_mov_b64 %0, 0" : "=v"(xr[t]));
    }
    if (j < 12 && w < 5) { st128_sc1(rs, rtg[0], rt0); st128_sc1(rs, rtg[1], rt1); }
    warm(((w & 1) ? wu : wg) + (size_t)(rr * 20 + (w >> 1) * 5) * 3072 + (xcd & 3) * 768, voffwarm, L_WARM);
    SB();
    f32x16 o = zero16;
    u32 pw[2][8];
    u32x2 vf[2][4], vx[4];
    auto rdV = [&](int i, int e) { return ldstr(adV + 512 * i + 128 * e); };
    auto pfrag = [&](const u32* q, int st) { return u32x4{q[4 * st], q[4 * st + 1], q[4 * st + 2], q[4 * st + 3]}; };
    u32 p8[8];
#pragma unroll
    for (int n = 0; n < 8; ++n) {
      float ex[16];
#pragma unroll
      for (int e = 0; e < 8; ++e) {
        ex[2 * e] = __builtin_amdgcn_exp2f(sc[n][2 * e]); SB();
        if (e < 4) { vf[n & 1][e] = rdV(n, e); SB(); }
        else if (n == 0) { vx[e - 4] = rdV(8, e - 4); SB(); }
        ex[2 * e + 1] = __builtin_amdgcn_exp2f(sc[n][2 * e + 1]); SB();
        if (e > 0) { pw[n & 1][e - 1] = pack2(ex[2 * e - 2], ex[2 * e - 1]); SB(); }
        if (n >= 1) {
          if (e == 0 && n < 7) { submax(n + 1); SB(); }
          if (e == 3) { o = mfma32(cat(vf[(n - 1) & 1][0], vf[(n - 1) & 1][1]), pfrag(pw[(n - 1) & 1], 0), o); SB(); }
          if (e == 6) { o = mfma32(cat(vf[(n - 1) & 1][2], vf[(n - 1) & 1][3]), pfrag(pw[(n - 1) & 1], 1), o); SB(); }
          if (n == 2 && e == 2) { o = mfma32(cat(vx[0], vx[1]), pfrag(p8, 0), o); SB(); }
          if (n == 2 && e == 5) { o = mfma32(cat(vx[2], vx[3]), pfrag(p8, 1), o); SB(); }
        }
      }
      pw[n & 1][7] = pack2(ex[14], ex[15]);
      if (n == 0) {
#pragma unroll
        for (int m = 0; m < 8; ++m) { p8[m] = pw[0][m] & dm[m]; pw[0][m] &= ~dm[m]; }
      }
      SB();
      if (n == 3) STAMP(27);
    }
    o = mfma32(cat(vf[1][0], vf[1][1]), pfrag(pw[1], 0), o);
    o = mfma32(cat(vf[1][2], vf[1][3]), pfrag(pw[1], 1), o);
    const float inv = __builtin_amdgcn_rcpf(o[12]);
    const u32 x0 = pack2(o[0] * inv, o[1] * inv), x1 = pack2(o[2] * inv, o[3] * inv), y0 = pack2(o[4] * inv, o[5] * inv), y1 = pack2(o[6] * inv, o[7] * inv);
    auto s0 = __builtin_amdgcn_permlane32_swap(x0, y0, false, false);
    auto s1 = __builtin_amdgcn_permlane32_swap(x1, y1, false, false);
    const u32 ob = WS_O + (pa * 80 + 5 * ja) * 2048 + (32 * qt + l32) * 16;
    st128_sc1(rs, ob + (3 * h + hh) * 2048, u32x4{s0[0], s1[0], s0[1], s1[1]});
    if (hh == 0) st64_sc1(rs, ob + 2 * 2048 + 8 * h, u32x2{pack2(o[8] * inv, o[9] * inv), pack2(o[10] * inv, o[11] * inv)});
    STAMP(5);
  }
  const char* gsrc = ((w & 1) ? wu : wg) + jg * 192;
  auto stageGBp = [&](int c, int rb, int i) {
    dma(gsrc + (size_t)(c * 64 + rb * 16) * 3072, voffG[i], gb_off(c) + (w & 1) * 12288 + rb * 3072 + i * 1024);
  };
  auto stageGB = [&](int c, int rb) { stageGBp(c, rb, 0); stageGBp(c, rb, 1); stageGBp(c, rb, 2); };
  const int wm4 = w & 3, wk = w >> 2;
  u32 voffR = g * 2048 + (64 * half + 16 * wm4 + r16) * 16;
  fl_get = flag_off(1, p, r16);
  u32 fl_get2 = flag_off(2, pg, r16);
  asm volatile("" : "+v"(voffR), "+v"(fl_get), "+v"(fl_get2));
  asm volatile("" ::: "memory");
  if (w < 4) {
#pragma unroll
    for (int i = 0; i < 20; ++i) dma_sc1(wt + 20480 * (1 + w) + i * 1024, lane * 16, L_WOB + 20480 * (1 + w) + i * 1024);
  }
  bar();
  if (w < 4) {
#pragma unroll
    for (int i = 0; i < 5; ++i) dma_sc1(wt + 5120 * w + i * 1024, lane * 16, L_WOB + 5120 * w + i * 1024);
    dma_sc1(ws + WS_WB + (w < 3 ? w : 2) * 1024, lane * 16, L_WB + (w < 3 ? w : 2) * 1024);
    vm_wait<6>();
  } else {
    stageGB(2, w >> 1); stageGB(2, (w >> 1) - 2);
    vm_wait<6>();
  }
  bar0();
  STAMP(6);
  u32x2 hp[5];
  {
    if (w == PW && lane < 2) __hip_atomic_store((GAS u64*)(ws + fl_put), tag, RLX_AGENT);
    u32x4 a[10], b[6][5];
    const u32 ad0 = L_WOB + wk * 10 * 5120 + lane * 16, adm = L_WB + (r16 & 1) * 1280 + wk * 640 + g * 16;
    auto rdB = [&](int s, int t) { b[s % 6][t] = lds128(ad0 + ((s + 4) % 10) * 5120 + t * 1024); };
#pragma unroll
    for (int s = 0; s < 6; ++s)
#pragma unroll
      for (int t = 0; t < 5; ++t) rdB(s, t);
    asm volatile("" : "+v"(xr[0]), "+v"(xr[1]), "+v"(xr[2]), "+v"(xr[3]), "+v"(xr[4]), "+v"(xr[5]));
    f32x4 hacc[5], macc;
    { const u32 xm = xr[5][0]; macc = f32x4{__uint_as_float(xm), 0.f, 0.f, 0.f}; }
#pragma unroll
    for (int t = 0; t < 5; ++t) { const u32 a0 = xr[t][0], a1 = xr[t][1]; hacc[t] = f32x4{bflo(a0), bfhi(a0), bflo(a1), bfhi(a1)}; }
    SB();
    if (w == PW) poll_flags(ws, fl_get, tag); else if (w < 4) vm_wait<0>();
    bar();
    STAMP(7);
    const char* osrc = ws + WS_O + (p * 80 + 40 * wk) * 2048;
#pragma unroll
    for (int s = 0; s < 10; ++s) gld_sc1(a[s], osrc + ((s + 4) % 10) * 8192, voffR);
    u32x4 bm[2];
    bm[0] = lds128(adm + 4 * 64);
    SB();
#pragma unroll
    for (int s = 0; s < 10; ++s) {
      switch (s) {
        case 0: vm_wait_use<9>(a[0]); break; case 1: vm_wait_use<8>(a[1]); break; case 2: vm_wait_use<7>(a[2]); break;
        case 3: vm_wait_use<6>(a[3]); break; case 4: vm_wait_use<5>(a[4]); break; case 5: vm_wait_use<4>(a[5]); break;
        case 6: vm_wait_use<3>(a[6]); break; case 7: vm_wait_use<2>(a[7]); break; case 8: vm_wait_use<1>(a[8]); break;
        default: vm_wait_use<0>(a[9]); break;
      }
      if (s == 0) STAMP(28);
#pragma unroll
      for (int t = 0; t < 5; ++t) {
        hacc[t] = mfma(b[s % 6][t], a[s], hacc[t]);
        SB();
        if (s + 6 < 10) { rdB(s + 6, t); SB(); }
        if (t == 0 && s < 9) { bm[(s + 1) & 1] = lds128(adm + ((s + 5) % 10) * 64); SB(); }
      }
      macc = mfma(bm[s & 1], a[s], macc);
      SB();
    }
    STAMP(8);
    if (wk == 0) {
#pragma unroll
      for (int t = 0; t < 5; ++t) *(LAS f32x4*)(smem + L_RED + (wm4 * 5 + t) * 1024 + lane * 16) = hacc[t];
      *(LAS float*)(smem + L_RED + 20480 + wm4 * 256 + lane * 4) = macc[0] + macc[1];
    }
    bar();
    if (wk == 1) {
      float s1 = 0.f, s2 = 0.f;
      const float mean = macc[0] + macc[1] + *(LAS const float*)(smem + L_RED + 20480 + wm4 * 256 + lane * 4);
      u32x2 hc[5];
#pragma unroll
      for (int t = 0; t < 5; ++t) {
        const f32x4 v = hacc[t] + *(LAS const f32x4*)(smem + L_RED + (wm4 * 5 + t) * 1024 + lane * 16);
        const u32 lo = pack2(v[0], v[1]), hi = pack2(v[2], v[3]);
        hp[t] = u32x2{lo, hi};
        hc[t] = u32x2{pack2(bflo(lo) - mean, bfhi(lo) - mean), pack2(bflo(hi) - mean, bfhi(hi) - mean)};
      }
      const u32 hb = WS_H + (p * 80 + 10 * slice + 2 * (g & 1) + (g >> 1)) * 2048 + (64 * half + 16 * wm4 + r16) * 16;
      st128_sc1(rs, hb, quad2chunk(hc[0], hc[1]));
      st128_sc1(rs, hb + 4 * 2048, quad2chunk(hc[2], hc[3]));
      const u32x4 last = quad2chunk(hc[4], hc[4]);
      if ((g & 1) == 0) st128_sc1(rs, hb + 8 * 2048, last);
#pragma unroll
      for (int t = 0; t < 5; ++t) { const u32 lo = hc[t][0], hi = hc[t][1]; s1 = dot2(hi, ONES2, dot2(lo, ONES2, s1)); s2 = dot2(hi, hi, dot2(lo, lo, s2)); }
      s1 = gsum(s1); s2 = gsum(s2);
      if (g == 0) st64_sc1(rs, WS_ST + (p * 8 + slice) * 1024 + (64 * half + 16 * wm4 + r16) * 8, u32x2{__float_as_uint(s1), __float_as_uint(s2)});
      vm_wait<0>();
    } else {
#pragma unroll
      for (int t = 0; t < 5; ++t) hp[t] = u32x2{0, 0};
      stageGB(0, w >> 1); stageGB(0, 2 + (w >> 1)); stageGB(1, w >> 1); stageGB(1, 2 + (w >> 1));
    }
  }
  bar();
  STAMP(9);
  if (w == PW) {
    if (lane == 0) __hip_atomic_store((GAS u64*)(ws + flag_off(2, p, j)), tag, RLX_AGENT);
    poll_flags(ws, fl_get2, tag);
  } else if (w == 2) {
    GAS u64* a = (GAS u64*)(ws + WS_DENSE + lane * 8);
    for (u32 spins = 0; spins < SPIN_CAP; ++spins) {
      const u64 v0 = __hip_atomic_load(a, RLX_AGENT), v1 = __hip_atomic_load(a + 64, RLX_AGENT), v2 = __hip_atomic_load(a + 128, RLX_AGENT), v3 = __hip_atomic_load(a + 192, RLX_AGENT);
      if (__all(v0 == tag && v1 == tag && v2 == tag && v3 == tag)) break;
    }
    if (bx == 0 && lane == 0) __hip_atomic_store((GAS u64*)(ws + WS_EPOCH), tag, RLX_AGENT);
  } else if (wk == 1) {
    stageGB(3, w >> 1); stageGB(3, (w >> 1) - 2);
  }
  bar();
  STAMP(10);

  const int rotd = (((slice * 6) >> 3) + ((3 * (rr >> 3)) >> 1)) % 6;
  auto stageDq = [&](int c, int blk, int i) {
    int kc = c + rotd; kc = kc >= 6 ? kc - 6 : kc;
    dma_sc1(wt + (20 + kc * 8 + blk) * 5120 + i * 1024, lane * 16, (c & 3) * 40960 + blk * 5120 + i * 1024);
  };
  auto stageDp = [&](int c, int i) { stageDq(c, w, i); };
  auto stageD = [&](int c) {
#pragma unroll
    for (int i = 0; i < 5; ++i) stageDp(c, i);
  };

  {
    const int wm = w >> 2, wn = w & 3;
    const char* hsrc = ws + WS_H + (pg * 80 + w) * 2048;
    auto stageGAp = [&](int c, int i) { dma_sc1(hsrc + c * 16384 + i * 1024, lane * 16, ga_off(c) + w * 2048 + i * 1024); };
    auto stageGA = [&](int c) { stageGAp(c, 0); stageGAp(c, 1); };
    stageGA(0);
    dma_sc1(ws + WS_ST + (pg * 8 + w) * 1024, lane * 16, L_ST + w * 1024);
    stageGA(1);
    const u32 adA = g * 2048 + (64 * wm + r16) * 16;
    u32 adB[3];
#pragma unroll
    for (int t = 0; t < 3; ++t) {
      const int mat = t == 0 ? 0 : (t == 1 ? 1 : (pq >> 1)), c = t < 2 ? 3 * wn + (pq >> 1) : 3 * wn + 2;
      int cp = c + mat + 2 * (g & 1); cp = cp >= 12 ? cp - 12 : cp;
      adB[t] = mat * 12288 + (8 * g + q4) * 192 + cp * 16 + (pq & 1) * 8;
    }
    f32x4 acc[4][3];
#pragma unroll
    for (int m = 0; m < 4; ++m)
#pragma unroll
      for (int t = 0; t < 3; ++t) acc[m][t] = f32x4{0, 0, 0, 0};
    u32x4 fa[2][4]; u32x2 fb[2][3][2];
    auto rdp = [&](int c, int s, int i) {
      const u32 sa = ga_off(c) + s * 8192, sb = gb_off(c) + s * 6144;
      if (i < 2) fb[s][0][i] = ldstr(sb + adB[0] + i * 768);
      else if (i == 2) fa[s][0] = lds128(sa + adA);
      else if (i < 7) fb[s][1 + ((i - 3) >> 1)][(i - 3) & 1] = ldstr(sb + adB[1 + ((i - 3) >> 1)] + ((i - 3) & 1) * 768);
      else if (i < 10) fa[s][i - 6] = lds128(sa + adA + (i - 6) * 256);
    };
    auto mm = [&](int s, int c) {
      u32x4 b[3];
#pragma unroll
      for (int t = 0; t < 3; ++t) b[t] = cat(fb[s][t][0], fb[s][t][1]);
      const int cr = s == 0 ? c : c + 1;
#pragma unroll
      for (int i = 0; i < 12; ++i) {
        acc[i / 3][i % 3] = mfma(b[i % 3], fa[s][i / 3], acc[i / 3][i % 3]);
        if (cr < 10) rdp(cr, s ^ 1, i);
        if (s == 1 && (i == 4 || i == 10) && c + 3 < 10) stageGAp(c + 3, i == 10);
        if (s == 0 && i % 4 == 3 && c >= 1 && c + 3 < 10) stageGBp(c + 3, w >> 1, i >> 2);
        if (s == 1 && (c == 7 || c == 8) && i % 2 == 1 && i < 10) stageDp(c - 7, i >> 1);
        SB();
      }
    };
    vm_wait<3>();
    bar();
    STAMP(19);
#pragma unroll
    for (int i = 0; i < 10; ++i) rdp(0, 0, i);
    SB();
    stageGA(2);
    SB();
#pragma unroll
    for (int c = 0; c < 10; ++c) {
      if (c == 1) {
        const f32x2 u0 = *(LAS const f32x2*)(smem + L_ST + (2 * g) * 1024 + (16 * w + r16) * 8), u1 = *(LAS const f32x2*)(smem + L_ST + (2 * g + 1) * 1024 + (16 * w + r16) * 8);
        const float s1 = gsum(u0[0] + u1[0]), s2 = gsum(u0[1] + u1[1]);
        const float mean = s1 * (1.f / 640.f), var = __builtin_fmaxf(s2 * (1.f / 640.f) - mean * mean, 0.f);
        if (g == 0) *(LAS float*)(smem + L_MISC + (16 * w + r16) * 4) = __builtin_amdgcn_rcpf(var + LN_EPS);
      }
      mm(0, c);
      if (c < 9) {
        if (c == 0) vm_wait<2>(); else if (c == 1) vm_wait<5>(); else if (c <= 6) vm_wait<8>(); else vm_wait<5>();
        bar();
        if (c == 4) STAMP(20);
        if (c == 7) STAMP(30);
      }
      mm(1, c);
    }
    STAMP(11);
    u32x2 p01[4];
    float r2[4];
#pragma unroll
    for (int m = 0; m < 4; ++m) r2[m] = *(LAS const float*)(smem + L_MISC + (64 * wm + 16 * m + r16) * 4);
    if (wm == 1) { vm_wait<5>(); bar(); }
#pragma unroll
    for (int m = 0; m < 4; ++m) {
      const f32x4 v = acc[m][0] * acc[m][1] * r2[m];
      p01[m] = u32x2{pack2(v[0], v[1]), pack2(v[2], v[3])};
    }
    const u32 hb = WS_HH + (pg * 192 + 12 * jg + 3 * wn) * 2048 + (64 * wm + r16) * 16;
    st128_sc1(rs, hb + (g >> 1) * 2048 + (g & 1) * 256, quad2chunk(p01[0], p01[1]));
    st128_sc1(rs, hb + (g >> 1) * 2048 + (2 + (g & 1)) * 256, quad2chunk(p01[2], p01[3]));
    u32x2 p2[2];
#pragma unroll
    for (int mm2 = 0; mm2 < 2; ++mm2) {
      f32x4 v;
#pragma unroll
      for (int r = 0; r < 4; ++r) {
        auto sw = __builtin_amdgcn_permlane32_swap(__float_as_uint(acc[2 * mm2][2][r]), __float_as_uint(acc[2 * mm2 + 1][2][r]), false, false);
        v[r] = __uint_as_float(sw[0]) * __uint_as_float(sw[1]) * (lane < 32 ? r2[2 * mm2] : r2[2 * mm2 + 1]);
      }
      p2[mm2] = u32x2{pack2(v[0], v[1]), pack2(v[2], v[3])};
    }
    st128_sc1(rs, hb + 2 * 2048 + (2 * (g & 1) + (g >> 1)) * 256, quad2chunk(p2[0], p2[1]));
    STAMP(31);
    if (wm == 0) {
      vm_wait<8>();
      bar();
#pragma unroll
      for (int i = 0; i < 10; ++i) stageDq(2, w + 4 * (i / 5), i % 5);
    }
  }

  {
    u32x4 b[6][5];
    const u32 ad0 = wk * 4 * 5120 + lane * 16;
    auto rdB = [&](int s, int t) { b[s % 6][t] = lds128(ad0 + ((s >> 2) & 3) * 40960 + (s & 3) * 5120 + t * 1024); };
#pragma unroll
    for (int s = 0; s < 4; ++s)
#pragma unroll
      for (int t = 0; t < 5; ++t) rdB(s, t);
    SB();
    if (wk == 0) vm_wait<10>(); else vm_wait<0>();
    bar();
    STAMP(12);
    if (w == PW && lane == 0) __hip_atomic_store((GAS u64*)(ws + flag_off(3, pg, jg)), tag, RLX_AGENT);
    if (wk == 1) {
#pragma unroll
      for (int i = 0; i < 10; ++i) stageDq(3, w - 4 * (i / 5), i % 5);
    }
#pragma unroll
    for (int s = 4; s < 6; ++s)
#pragma unroll
      for (int t = 0; t < 5; ++t) rdB(s, t);
    f32x4 acc[5];
#pragma unroll
    for (int t = 0; t < 5; ++t) { const u32 a = hp[t][0], b2 = hp[t][1]; acc[t] = f32x4{bflo(a), bfhi(a), bflo(b2), bfhi(b2)}; }
    SB();
    if (w == PW) poll_flags(ws, fl_get + 2 * 65536, tag); else if (wk == 0) vm_wait<0>();
    bar();
    STAMP(13);
    u32x4 a[6][4];
    const char* asrc = ws + WS_HH + (p * 192 + 16 * wk) * 2048;
    auto loadAp = [&](int c, int s) {
      int kc = c + rotd; kc = kc >= 6 ? kc - 6 : kc;
      gld_sc1(a[c][s], asrc + kc * 65536 + s * 8192, voffR);
    };
    auto issue = [&](int n) {
      if (n < 4) loadAp(2, n); else if (n < 9) stageDp(4, n - 4); else if (n < 13) loadAp(3, n - 9); else if (n < 18) stageDp(5, n - 13);
      else if (n < 22) loadAp(4, n - 18); else if (n < 26) loadAp(5, n - 22);
    };
#pragma unroll
    for (int s = 0; s < 4; ++s) loadAp(0, s);
#pragma unroll
    for (int s = 0; s < 4; ++s) loadAp(1, s);
#pragma unroll
    for (int s = 0; s < 24; ++s) {
      if (s == 0) vm_wait<4>();
      if (s == 4) vm_wait<8>();
      if (s == 8) vm_wait<12>();
      if (s == 10) vm_wait<11>();
      if (s == 12) vm_wait<11>();
      if (s == 14) vm_wait<8>();
      if (s == 16) vm_wait<4>();
      if (s == 20) vm_wait<0>();
      if (s % 4 == 0) asm volatile("" : "+v"(a[s >> 2][0]), "+v"(a[s >> 2][1]), "+v"(a[s >> 2][2]), "+v"(a[s >> 2][3]));
      if (s == 6) bar();
      if (s == 10 || s == 14) bar0();
      if (s == 0) STAMP(21);
      if (s == 4) STAMP(22); if (s == 8) STAMP(23); if (s == 12) STAMP(24); if (s == 16) STAMP(25);
#pragma unroll
      for (int t = 0; t < 5; ++t) {
        acc[t] = mfma(b[s % 6][t], a[s >> 2][s & 3], acc[t]);
        SB();
        if (s + 6 < 24) { rdB(s + 6, t); SB(); }
        if (t == 1) { issue(2 * s); SB(); }
        if (t == 4) { issue(2 * s + 1); SB(); }
      }
    }
    STAMP(14);
    constexpr u32 L_RED2 = 2 * 40960;
    if (wk == 0) {
#pragma unroll
      for (int t = 0; t < 5; ++t) *(LAS f32x4*)(smem + L_RED2 + (wm4 * 5 + t) * 1024 + lane * 16) = acc[t];
    }
    bar();
    if (wk == 1) {
      u32x2 op[5];
#pragma unroll
      for (int t = 0; t < 5; ++t) {
        const f32x4 v = acc[t] + *(LAS const f32x4*)(smem + L_RED2 + (wm4 * 5 + t) * 1024 + lane * 16);
        op[t] = u32x2{pack2(v[0], v[1]), pack2(v[2], v[3])};
      }
      const __amdgpu_buffer_rsrc_t ro = __builtin_amdgcn_make_buffer_rsrc(out, 0, 2048 * 1280, 0x00020000);
      const u32 oo = (128 * p + 64 * half + 16 * wm4 + r16) * 1280 + slice * 160 + (2 * (g & 1) + (g >> 1)) * 16;
      __builtin_amdgcn_raw_buffer_store_b128(quad2chunk(op[0], op[1]), ro, (int)oo, 0, OUT_AUX);
      __builtin_amdgcn_raw_buffer_store_b128(quad2chunk(op[2], op[3]), ro, (int)oo + 64, 0, OUT_AUX);
      const u32x4 last = quad2chunk(op[4], op[4]);
      if ((g & 1) == 0) __builtin_amdgcn_raw_buffer_store_b128(last, ro, (int)oo + 128, 0, OUT_AUX);
    }
    STAMP(15);
  }
}

extern "C" void kernel_launch(void* const* d_in, const int* in_sizes, int n_in, void* d_out, int out_size, void* d_ws, size_t ws_size, hipStream_t stream) {
  (void)in_sizes; (void)n_in; (void)out_size; (void)ws_size;
  (void)hipFuncSetAttribute((const void*)fused_block, hipFuncAttributeMaxDynamicSharedMemorySize, LDS_BYTES);
  fused_block<<<256, 512, LDS_BYTES, stream>>>((const char*)d_in[0], (const char*)d_in[1], (const char*)d_in[2], (const char*)d_in[4], (const char*)d_in[5],
                                               (const char*)d_in[6], (char*)d_out, (char*)d_ws);
}
```

```cpp
#include <hip/hip_runtime.h>
#include <stdint.h>

typedef unsigned short u16;
typedef unsigned int u32;
typedef unsigned long long u64;
typedef __bf16 bf16x8 __attribute__((ext_vector_type(8)));
typedef __bf16 bf16x2 __attribute__((ext_vector_type(2)));
typedef short s16x4 __attribute__((ext_vector_type(4)));
typedef float f32x4 __attribute__((ext_vector_type(4)));
typedef float f32x2 __attribute__((ext_vector_type(2)));
typedef u32 u32x4 __attribute__((ext_vector_type(4)));
typedef u32 u32x2 __attribute__((ext_vector_type(2)));
#define LAS __attribute__((address_space(3)))
#define GAS __attribute__((address_space(1)))
#define DI __device__ __forceinline__
#define RLX_AGENT __ATOMIC_RELAXED, __HIP_MEMORY_SCOPE_AGENT

#define RG 5
#define OUT_AUX 0
#define W3BAR 0
#define DWARM 0
#define GMAP 0
#define DMAP 1
#define DROT 0
#define RSAT 8
#define X_A 0
#define X_B 0
#define X_C 0
#define X_D 0
#define POLLW 0
#define KSWAP 0
#define KROT 0
#define POLLV 8
#define FW 0
#define NEARLY 20
#define ATT_PRIO 0
#define NP 0
#define WPOS 1
#define XFIRST 2
#define XROT 0
#define WDEC 1
#define WDAT 1
#define WGAT 99
#define WGSP 0
#define KA 1
#define WH 3
#define SW 0
#define STAMPS 0

constexpr u32 WS_EPOCH = 0;
constexpr u32 WS_WOF = 1024;
constexpr u32 WS_DENSE = 2048;
constexpr u32 WS_FLAG = 4096;
constexpr u32 WS_QKV = WS_FLAG + 4 * 256 * 256;
constexpr u32 WS_O = WS_QKV + 16 * 240 * 2048;
constexpr u32 WS_H = WS_O + 16 * 80 * 2048;
constexpr u32 WS_HH = WS_H + 16 * 80 * 2048;
constexpr u32 WS_ST = WS_HH + 16 * 192 * 2048;
constexpr u32 WS_RS = WS_ST + 16 * 8 * 1024;
constexpr u32 WS_WB = WS_RS + 2048 * 16;
constexpr u32 WT_SLICE = 68 * 5120;
constexpr u32 WS_WT = WS_WB + 3072;
constexpr u32 WS_END = WS_WT + 8 * WT_SLICE;
constexpr u32 WS_STAMP = WS_END;
constexpr u64 TAG_STEP = 0x9E3779B97F4A7C15ull;

constexpr u32 LDS_BYTES = 163840;
constexpr u32 L_MISC = 161792;
constexpr u32 L_K = 0, KCH = 6144;
constexpr u32 L_V = 6 * KCH, VCH = 6208;
constexpr u32 L_WOB = L_MISC - 102400;
__device__ constexpr u32 gb_off(int c) { return (c & 3) == 2 ? 0 : ((c & 3) == 3 ? 24576 : ((c & 3) == 0 ? 65536 : 106496)); }
__device__ constexpr u32 ga_off(int c) { return c % 3 == 1 ? 49152 : (c % 3 == 2 ? 90112 : 131072); }
constexpr u32 L_ST = 147456;
constexpr u32 L_WB = 49152;
constexpr u32 L_RED = 24576;
constexpr u32 L_WARM = L_MISC + 1792;
constexpr u32 L_RT = 131072;
constexpr u32 SPIN_CAP = 1u << 15;
constexpr float LN_EPS = 1e-5f;

extern __shared__ __attribute__((aligned(16))) char smem[];

DI u32x4 lds128(u32 off) { return *(LAS const u32x4*)(smem + off); }
DI u32x2 ldstr(u32 off) { return __builtin_bit_cast(u32x2, __builtin_amdgcn_ds_read_tr16_b64_v4i16((LAS s16x4*)(smem + off))); }
DI u32x4 ldstr2(u32 lo, u32 hi) { const u32x2 a = ldstr(lo), b = ldstr(hi); return u32x4{a[0], a[1], b[0], b[1]}; }
DI f32x4 mfma(u32x4 a, u32x4 b, f32x4 c) { return __builtin_amdgcn_mfma_f32_16x16x32_bf16(__builtin_bit_cast(bf16x8, a), __builtin_bit_cast(bf16x8, b), c, 0, 0, 0); }
typedef float f32x16 __attribute__((ext_vector_type(16)));
DI f32x16 mfma32(u32x4 a, u32x4 b, f32x16 c) { return __builtin_amdgcn_mfma_f32_32x32x16_bf16(__builtin_bit_cast(bf16x8, a), __builtin_bit_cast(bf16x8, b), c, 0, 0, 0); }
DI float max3(float a, float b, float c) { return __builtin_fmaxf(__builtin_fmaxf(a, b), c); }
DI f32x4 mfma16(u32x2 a, u32x2 b, f32x4 c) { return __builtin_amdgcn_mfma_f32_16x16x16bf16_1k(__builtin_bit_cast(s16x4, a), __builtin_bit_cast(s16x4, b), c, 0, 0, 0); }
DI float dot2(u32 a, u32 b, float c) { return __builtin_amdgcn_fdot2_f32_bf16(__builtin_bit_cast(bf16x2, a), __builtin_bit_cast(bf16x2, b), c, false); }
DI u32 pack2(float lo, float hi) { const f32x2 v = {lo, hi}; return __builtin_bit_cast(u32, __builtin_convertvector(v, bf16x2)); }
DI float bflo(u32 v) { return __uint_as_float(v << 16); }
DI float bfhi(u32 v) { return __uint_as_float(v & 0xffff0000u); }
constexpr u32 ONES2 = 0x3f803f80u;
DI float sum8(u32x4 a, float s) { const u32 a0 = a[0], a1 = a[1], a2 = a[2], a3 = a[3]; return dot2(a3, ONES2, dot2(a2, ONES2, dot2(a1, ONES2, dot2(a0, ONES2, s)))); }
DI float sq8(u32x4 a, float s) { const u32 a0 = a[0], a1 = a[1], a2 = a[2], a3 = a[3]; return dot2(a3, a3, dot2(a2, a2, dot2(a1, a1, dot2(a0, a0, s)))); }
DI float gsum(float x) {
  auto a = __builtin_amdgcn_permlane16_swap(__float_as_uint(x), __float_as_uint(x), false, false);
  x = __uint_as_float(a[0]) + __uint_as_float(a[1]);
  auto b = __builtin_amdgcn_permlane32_swap(__float_as_uint(x), __float_as_uint(x), false, false);
  return __uint_as_float(b[0]) + __uint_as_float(b[1]);
}
DI float gmax(float x) {
  auto a = __builtin_amdgcn_permlane16_swap(__float_as_uint(x), __float_as_uint(x), false, false);
  x = __builtin_fmaxf(__uint_as_float(a[0]), __uint_as_float(a[1]));
  auto b = __builtin_amdgcn_permlane32_swap(__float_as_uint(x), __float_as_uint(x), false, false);
  return __builtin_fmaxf(__uint_as_float(b[0]), __uint_as_float(b[1]));
}
DI u32x4 quad2chunk(u32x2 A, u32x2 B) {
  const u32 a0 = A[0], a1 = A[1], b0 = B[0], b1 = B[1];
  auto s0 = __builtin_amdgcn_permlane16_swap(a0, b0, false, false);
  auto s1 = __builtin_amdgcn_permlane16_swap(a1, b1, false, false);
  return u32x4{s0[0], s1[0], s0[1], s1[1]};
}
DI void dma(const void* sbase, u32 voff, u32 lds) {
  asm volatile("s_mov_b32 m0, %2\n\ts_nop 0\n\tglobal_load_lds_dwordx4 %0, %1" ::"v"(voff), "s"(sbase), "s"(lds));
}
DI void dma_sc1(const void* sbase, u32 voff, u32 lds) {
  asm volatile("s_mov_b32 m0, %2\n\ts_nop 0\n\tglobal_load_lds_dwordx4 %0, %1 sc1" ::"v"(voff), "s"(sbase), "s"(lds));
}
DI void gld_sc1(u32x4& dst, const void* sbase, u32 voff) {
  asm volatile("global_load_dwordx4 %0, %1, %2 sc1" : "=v"(dst) : "v"(voff), "s"(sbase));
}
DI void gld(u32x4& dst, const void* sbase, u32 voff) {
  asm volatile("global_load_dwordx4 %0, %1, %2" : "=v"(dst) : "v"(voff), "s"(sbase));
}
DI void gld64(u32x2& dst, const void* sbase, u32 voff) {
  asm volatile("global_load_dwordx2 %0, %1, %2" : "=v"(dst) : "v"(voff), "s"(sbase));
}
template <int N> DI void vm_wait() { asm volatile("s_waitcnt vmcnt(%0)" ::"n"(N) : "memory"); }
template <int N> DI void vm_wait_use(u32x4& r) { asm volatile("s_waitcnt vmcnt(%1)" : "+v"(r) : "n"(N)); }
#define INL __attribute__((always_inline))
#define SB() __builtin_amdgcn_sched_barrier(0)
DI void warm(const void* sbase, u32 voff, u32 lds) {
  asm volatile("s_mov_b32 m0, %2\n\ts_nop 0\n\tglobal_load_lds_dword %0, %1" ::"v"(voff), "s"(sbase), "s"(lds));
}
DI void warm_sc1(const void* sbase, u32 voff, u32 lds) {
  asm volatile("s_mov_b32 m0, %2\n\ts_nop 0\n\tglobal_load_lds_dword %0, %1 sc1" ::"v"(voff), "s"(sbase), "s"(lds));
}
#define SL(o) "s_load_dword %0, %1, " #o "\n\t"
DI void swarm15(u32& sink, const void* base) {
  asm volatile(SL(0) SL(128) SL(256) SL(3072) SL(3200) SL(3328) SL(6144) SL(6272) SL(6400) SL(9216) SL(9344) SL(9472) SL(12288) SL(12416) SL(12544) : "=&s"(sink) : "s"(base));
}
DI void bar0() { asm volatile("s_barrier" ::: "memory"); }
DI u32x4 cat(u32x2 a, u32x2 b) { return u32x4{a[0], a[1], b[0], b[1]}; }
DI void bar() { asm volatile("s_waitcnt lgkmcnt(0)\n\ts_barrier" ::: "memory"); }
DI void st128_sc1(__amdgpu_buffer_rsrc_t rs, u32 off, u32x4 v) { __builtin_amdgcn_raw_buffer_store_b128(v, rs, (int)off, 0, 16); }
DI void st64_sc1(__amdgpu_buffer_rsrc_t rs, u32 off, u32x2 v) { __builtin_amdgcn_raw_buffer_store_b64(v, rs, (int)off, 0, 16); }

DI void poll_flags(const char* ws, u32 off, u64 tag) {
  GAS u64* a = (GAS u64*)(ws + off);
  __builtin_amdgcn_s_sleep(POLLV);
  for (u32 spins = 0; spins < SPIN_CAP; ++spins) {
    const u64 v = __hip_atomic_load(a, RLX_AGENT);
    if (__all(v == tag)) break;
    if (POLLW) __builtin_amdgcn_s_sleep(POLLW);
  }
  __builtin_amdgcn_fence(__ATOMIC_ACQUIRE, "wavefront");
}
DI u32 flag_off(int phase, int p, int j) { return WS_FLAG + (u32)((phase * 256 + p * 16 + j) * 256); }

#define STAMP(i) do {} while (0)

__global__ __launch_bounds__(512, 2) void fused_block(const char* __restrict__ x, const char* __restrict__ wqkv, const char* __restrict__ wo,
                                                      const char* __restrict__ wg, const char* __restrict__ wu, const char* __restrict__ wd,
                                                      char* __restrict__ out, char* ws) {
  const int tid = threadIdx.x, lane = tid & 63, w = __builtin_amdgcn_readfirstlane(tid >> 6);
  const int r16 = lane & 15, g = lane >> 4, q4 = r16 >> 2, pq = r16 & 3;
  const int bx = blockIdx.x, xcd = bx & 7, rr = bx >> 3;
  const int p = (xcd >> 1) * 4 + (rr >> 3), j = (xcd & 1) * 8 + (rr & 7);
  const int pa = rr >> 1, ja = 2 * xcd + (rr & 1);
  const int pg = GMAP ? pa : 8 * (xcd >> 2) + (rr >> 2), jg = GMAP ? ja : 4 * (xcd & 3) + (rr & 3);
  const int half = DMAP ? (rr >> 2) & 1 : j >> 3, slice = DMAP ? 4 * (xcd & 1) + (rr & 3) : j & 7;
  constexpr int PW = 3;
  const __amdgpu_buffer_rsrc_t rs = __builtin_amdgcn_make_buffer_rsrc(ws, 0, WS_END, 0x00020000);
  STAMP(0);
  if (KA) asm volatile("" ::"s"(x), "s"(wqkv), "s"(ws));
  u64 tag;
  auto load_epoch = [&](u32x2& e) { const u32 z = 0; asm volatile("global_load_dwordx2 %0, %1, %2 sc1" : "=v"(e) : "v"(z), "s"(ws + WS_EPOCH)); };
  auto make_tag = [&](u32x2& e) {
    asm volatile("" : "+v"(e));
    const u32 e0 = e[0], e1 = e[1];
    tag = (((u64)(u32)__builtin_amdgcn_readfirstlane(e1) << 32) | (u32)__builtin_amdgcn_readfirstlane(e0)) + TAG_STEP;
  };
  const char* xa = x + (size_t)p * 128 * 1280;
  const u32x4 ones8 = {ONES2, ONES2, ONES2, ONES2};
  const char* wt = ws + WS_WT + slice * WT_SLICE;
  auto rt_dst = [&](int piece, int blk, int gq) {
    const int tile = 4 * piece + g, sl = tile / 5, t = tile - sl * 5;
    return WS_WT + sl * WT_SLICE + blk * 5120 + t * 1024 + gq * 256 + r16 * 16;
  };
  auto warm_gu = [&]() INL {
    if (GMAP) {
      const int l3 = lane < 30 ? lane : 29, rw = l3 / 3;
      warm(((w & 1) ? wu : wg) + (size_t)(rr * 20 + (w >> 1) * 10) * 3072 + xcd * 384, rw * 3072 + (l3 - rw * 3) * 128, L_WARM);
      return;
    }
    if (WH == 1) {
      const int l6 = lane < 30 ? lane : 29, rw = l6 / 6;
      warm(((xcd & 4) ? wu : wg) + (size_t)(rr * 20 + (w & 3) * 5) * 3072 + (xcd & 3) * 768, rw * 3072 + (l6 - rw * 6) * 128, L_WARM);
      return;
    }
    if (WH == 2) {
      const int l6 = lane < 15 ? lane : 14, rw = l6 / 3;
      warm(((xcd & 4) ? wu : wg) + (size_t)(rr * 20 + (w & 3) * 5) * 3072 + (xcd & 3) * 768, rw * 3072 + (l6 - rw * 3) * 256, L_WARM);
      return;
    }
    if (WH == 3) {
      const int l6 = lane < 60 ? lane : 59, rw = l6 / 6;
      if ((w & 3) < 2) warm(((xcd & 4) ? wu : wg) + (size_t)(rr * 20 + (w & 1) * 10) * 3072 + (xcd & 3) * 768, rw * 3072 + (l6 - rw * 6) * 128, L_WARM);
      return;
    }
    const int l6 = lane < 60 ? lane : 59, rw = l6 / 6;
    warm(((w & 1) ? wu : wg) + (size_t)(rr * 20 + ((w >> 1) & 1) * 10) * 3072 + (xcd & 3) * 768, rw * 3072 + (l6 - rw * 6) * 128, L_WARM);
  };
  u32 kvsrc[8], kvdst[8], fl_get;
  auto setup = [&]() INL {
#pragma unroll
    for (int i = 0; i < 8; ++i) {
      const int id = w + 8 * i, kv = id >= 30, r = id - 30 * kv, c = r / 6, r2 = r - 6 * c;
      int pp = pa - 2 + (r2 >> 1); pp = pp < 0 ? 0 : pp;
      kvsrc[i] = WS_QKV + (pp * 240 + 80 * (1 + kv) + 5 * ja + c) * 2048 + (r2 & 1) * 1024;
      kvdst[i] = (kv ? L_V + c * VCH : L_K + c * KCH) + r2 * 1024;
      asm volatile("" : "+s"(kvsrc[i]), "+s"(kvdst[i]));
    }
    const int e = r16 < 14 ? r16 : 0, sec = e < 2 ? 0 : (e < 8 ? 1 : 2), i = e < 2 ? e : (e - 2) % 6;
    int pp = e < 2 ? pa : pa - (i >> 1); pp = pp < 0 ? 0 : pp;
    fl_get = (!WDEC && (g & 1)) ? flag_off(0, r16, 15) : flag_off(0, pp, (80 * sec + 5 * ja + 4 * (i & 1)) >> 4);
    asm volatile("" : "+v"(fl_get));
  };

  if (j < 15) {
    const int wm = w & 1, wn = w >> 1;
    u32 voffA[2], voffB[2];
#pragma unroll
    for (int i = 0; i < 2; ++i) {
      const int ra = 16 * w + 8 * i + (lane >> 3);
      voffA[i] = ra * 1280 + (((lane & 7) ^ ((ra >> 1) & 7)) << 4);
      const int rb = 8 * w + 4 * i + g, f = (rb & 3) | (((rb >> 3) & 1) << 2);
      voffB[i] = rb * 3840 + (((((r16 >> 1) ^ f) << 1) | (r16 & 1)) << 4);
    }
    const char* wb = wqkv + j * 256;
    const int rot = RG == 10 ? ((rr & 7) + ((rr & 7) >> 2) + ((5 * (rr >> 3)) >> 1)) % 10 : ((rr & 7) + (rr >> 3)) % RG;
    auto stage = [&](int c, int piece) {
      int kc = c % RG + rot; kc = (kc >= RG ? kc - RG : kc) + (c / RG) * RG;
      const u32 slot = (c & 3) * 32768;
      const char* a = xa + kc * 128; const char* b = wb + (size_t)kc * 64 * 3840;
      if (piece < 0 || piece == 0) dma(a, voffA[0], slot + w * 2048);
      if (piece < 0 || piece == 1) dma(a, voffA[1], slot + w * 2048 + 1024);
      if (piece < 0 || piece == 2) dma(b, voffB[0], slot + 16384 + w * 2048);
      if (piece < 0 || piece == 3) dma(b, voffB[1], slot + 16384 + w * 2048 + 1024);
    };
    stage(0, -1);
    u32x2 epoch;
    load_epoch(epoch);
    {
      u64 pc; asm volatile("s_getpc_b64 %0" : "=s"(pc));
      if (w == 0) warm((const char*)(pc & ~127ull) + rr * 1280, (lane < 10 ? lane : 9) * 128, L_WARM);
    }
    setup();
    stage(1, -1);
    u32 adA[2], adB[2];
#pragma unroll
    for (int ks = 0; ks < 2; ++ks) adA[ks] = (64 * wm + r16) * 128 + (((4 * ks + g) ^ ((r16 >> 1) & 7)) << 4);
#pragma unroll
    for (int n = 0; n < 2; ++n) adB[n] = 16384 + (8 * g + q4) * 256 + (((2 * wn + (n ^ wm)) ^ (q4 | ((g & 1) << 2))) << 5) + pq * 8;
    f32x4 acc[4][2], csum = {0, 0, 0, 0};
#pragma unroll
    for (int m = 0; m < 4; ++m) { acc[m][0] = f32x4{0, 0, 0, 0}; acc[m][1] = f32x4{0, 0, 0, 0}; }
    u32x4 fa[2][4]; u32x2 fb[2][2][2];
    u32x4 rsg[4];
    auto ld_rs = [&]() {
#pragma unroll
      for (int m = 0; m < 4; ++m) gld_sc1(rsg[m], ws + WS_RS + (p * 128 + 64 * wm + 16 * m) * 16, r16 * 16);
    };
    auto rdp = [&](int c, int ks, int i) {
      const u32 slot = (c & 3) * 32768;
      if (i == 0 || i == 1) fb[ks][0][i] = ldstr(slot + adB[0] + ks * 8192 + i * 1024);
      else if (i == 2) fa[ks][0] = lds128(slot + adA[ks]);
      else if (i == 3 || i == 4) fb[ks][1][i - 3] = ldstr(slot + adB[1] + ks * 8192 + (i - 3) * 1024);
      else if (i < 8) fa[ks][i - 4] = lds128(slot + adA[ks] + (i - 4) * 2048);
    };
    auto mm = [&](int ks, int cr, int cn) {
      const u32x4 b0 = cat(fb[ks][0][0], fb[ks][0][1]), b1 = cat(fb[ks][1][0], fb[ks][1][1]);
#pragma unroll
      for (int i = 0; i < 9; ++i) {
        if (i > 0) acc[(i - 1) >> 1][(i - 1) & 1] = mfma(((i - 1) & 1) ? b1 : b0, fa[ks][(i - 1) >> 1], acc[(i - 1) >> 1][(i - 1) & 1]);
        else csum = mfma(b0, ones8, csum);
        if (cr == 10 && i == 3) {
          if (r16 == 0) *(LAS f32x4*)(smem + L_MISC + 1024 + (32 * wn + 16 * wm + 4 * g) * 4) = csum;
          asm volatile("" ::: "memory");
          if (lane == 0) *(volatile LAS u32*)(smem + L_MISC + w * 4) = 1;
        }
        if (cr < 10) rdp(cr, ks ^ 1, i);
        if (cn < 10 && i == 4) stage(cn, 2 * (ks ^ 1));
        if (cn < 10 && i == 8) stage(cn, 2 * (ks ^ 1) + 1);
        SB();
      }
    };
    auto ld_wd = [&]() INL {
      if (j < 12 && w < 5) {
        dma(wd + (p * 12 + j) * 10240 + w * 256, (lane >> 3) * 1280 + (lane & 7) * 16, L_RT + w * 2048);
        dma(wd + (p * 12 + j) * 10240 + w * 256 + 128, (lane >> 3) * 1280 + (lane & 7) * 16, L_RT + w * 2048 + 1024);
      }
      SB();
    };
    if (tid < 8) *(LAS u32*)(smem + L_MISC + tid * 4) = 0;
    vm_wait<4>();
    make_tag(epoch);
    bar();
    STAMP(16);
#pragma unroll
    for (int i = 0; i < 8; ++i) rdp(0, 0, i);
    SB();
    if (WDAT == -1) ld_wd();
    stage(2, 0); stage(2, 1);
    SB();
#pragma unroll
    for (int c = 0; c < 10; ++c) {
      mm(0, c, c + 2);
      if (c < 9) {
        {
          constexpr int base[9] = {4, 4, 4, 4, 4, 4, 4, 4, 0};
          const int n = base[c] + (c == RSAT + 1 ? 4 : 0);
          if (WGAT < 9 && c >= WGAT + 1 && c <= WGAT + 1 + 3 * WGSP && w >= 4 && c == WGAT + 1 + (w - 4) * WGSP) { if (n == 0) vm_wait<1>(); else vm_wait<5>(); }
          else if (c == WDAT + 1 && j < 12 && w < 5) { if (n == 0) vm_wait<2>(); else if (n == 4) vm_wait<6>(); else vm_wait<10>(); }
          else { if (n == 0) vm_wait<0>(); else if (n == 4) vm_wait<4>(); else vm_wait<8>(); }
        }
        bar0();
        if (c == RSAT) {
          ld_rs(); SB();
        }
        if (c == WDAT) ld_wd();
        if (WGAT < 9 && c >= WGAT && c <= WGAT + 3 * WGSP && w >= 4 && c == WGAT + (w - 4) * WGSP) { warm_gu(); SB(); }
      }
      mm(1, c + 1, c + 3);
    }
    STAMP(1);
    if (WDAT == 8 && j < 12 && w < 5) vm_wait<2>(); else vm_wait<0>();
    for (u32 spins = 0; spins < SPIN_CAP; ++spins) {
      asm volatile("" : "+v"(rsg[0]), "+v"(rsg[1]), "+v"(rsg[2]), "+v"(rsg[3]));
      bool ok = true;
#pragma unroll
      for (int m = 0; m < 4; ++m) ok = ok && rsg[m][1] == (u32)tag && rsg[m][3] == (u32)(tag >> 32);
      if (__all(ok)) break;
      ld_rs();
      vm_wait<0>();
    }
    asm volatile("" : "+v"(rsg[0]), "+v"(rsg[1]), "+v"(rsg[2]), "+v"(rsg[3]));
    STAMP(27);
    while (*(volatile LAS u32*)(smem + L_MISC + (w ^ 1) * 4) == 0) {}
    asm volatile("" ::: "memory");
    STAMP(17);
    const float qs = j < 5 ? 0.22360679774997896f * 1.4426950408889634f : 1.f;
    f32x4 cs[2];
#pragma unroll
    for (int n = 0; n < 2; ++n) cs[n] = n == 0 ? csum : *(LAS const f32x4*)(smem + L_MISC + 1024 + (32 * wn + 16 * (1 ^ wm) + 4 * g) * 4);
    const u32 qb = WS_QKV + (p * 240 + 16 * j + 4 * wn + 2 * ((g & 1) ^ wm) + (g >> 1)) * 2048 + (64 * wm + r16) * 16;
#pragma unroll
    for (int m = 0; m < 4; ++m) {
      const u32 um = rsg[m][0], ur = rsg[m][2];
      const float mean = __uint_as_float(um), sc = __uint_as_float(ur) * qs;
      u32x2 pk[2];
#pragma unroll
      for (int n = 0; n < 2; ++n) {
        const f32x4 v = (acc[m][n] - mean * cs[n]) * sc;
        pk[n] = u32x2{pack2(v[0], v[1]), pack2(v[2], v[3])};
      }
      st128_sc1(rs, qb + m * 256, quad2chunk(pk[0], pk[1]));
    }
    asm volatile("" ::: "memory");
    if (WDAT == 9) ld_wd();
  } else {
    u32x2 epoch;
    load_epoch(epoch);
    auto ld_wo = [&]() INL {
#pragma unroll
      for (int i = 0; i < 7; ++i) { const int id = w + 8 * i < 50 ? w + 8 * i : 49; dma(wo + p * 51200 + id * 1024, lane * 16, id * 1024); }
    };
    if (!XFIRST) ld_wo();
    u32x4 xv[2][10];
#pragma unroll
    for (int t = 0; t < 10; ++t)
#pragma unroll
      for (int s2 = 0; s2 < 2; ++s2) gld(xv[s2][t], xa + (16 * w + 8 * s2) * 1280 + ((t + XROT) % 10) * 128, (lane >> 3) * 1280 + (lane & 7) * 16);
    if (XFIRST == 1) ld_wo();
    float s1[2] = {0.f, 0.f}, sq[2] = {0.f, 0.f};
#pragma unroll
    for (int t = 0; t < 10; ++t) {
      if (t == 0) vm_wait<10 + 7 * (XFIRST == 1)>();
      if (t == 5) vm_wait<4 + 7 * (XFIRST == 1)>();
      if (t == 8) vm_wait<7 * (XFIRST == 1)>();
#pragma unroll
      for (int s2 = 0; s2 < 2; ++s2) { asm volatile("" : "+v"(xv[s2][t])); s1[s2] = sum8(xv[s2][t], s1[s2]); sq[s2] = sq8(xv[s2][t], sq[s2]); }
    }
    make_tag(epoch);
#pragma unroll
    for (int o = 1; o < 8; o <<= 1) { s1[0] += __shfl_xor(s1[0], o); sq[0] += __shfl_xor(sq[0], o); s1[1] += __shfl_xor(s1[1], o); sq[1] += __shfl_xor(sq[1], o); }
    {
      const float a1 = (lane & 1) ? s1[1] : s1[0], a2 = (lane & 1) ? sq[1] : sq[0];
      const float mean = a1 * (1.f / 640.f), var = __builtin_fmaxf(a2 * (1.f / 640.f) - mean * mean, 0.f);
      if ((lane & 7) < 2)
        st128_sc1(rs, WS_RS + (p * 128 + 16 * w + 8 * (lane & 1) + (lane >> 3)) * 16, u32x4{__float_as_uint(mean), (u32)tag, __float_as_uint(__builtin_amdgcn_rsqf(var + LN_EPS)), (u32)(tag >> 32)});
    }
    STAMP(16);
    asm volatile("" ::: "memory");
    if (XFIRST == 2) ld_wo();
    setup();
    if (XFIRST == 2) vm_wait<0>(); else vm_wait<1>();
    bar();
    {
#pragma unroll
      for (int i = 0; i < 7; ++i) {
        const int id = w + 8 * i, gr = id / 10;
        if (id < 50) {
          const u32 l = gr * 10240 + q4 * 1280 + (id - 10 * gr) * 128 + g * 32 + pq * 8;
          st128_sc1(rs, rt_dst(id - 10 * gr, (p * 5 + gr) >> 2, (p * 5 + gr) & 3), ldstr2(l, l + 5120));
        }
      }
      if (w >= 3) {
        float a1 = 0.f;
#pragma unroll
        for (int t = 0; t < 10; ++t) a1 = sum8(lds128((w - 3) * 10240 + (lane >> 3) * 1280 + t * 128 + (lane & 7) * 16), a1);
#pragma unroll
        for (int o = 1; o < 8; o <<= 1) a1 += __shfl_xor(a1, o);
        a1 *= (1.f / 640.f);
        const u32 hi = pack2(a1, 0.f), lo = pack2(a1 - bflo(hi), 0.f);
        if ((lane & 7) < 2) __builtin_amdgcn_raw_buffer_store_b16((u16)((lane & 1) ? lo : hi), rs, (int)(WS_WB + (lane & 1) * 1280 + ((p * 5 + w - 3) * 8 + (lane >> 3)) * 2), 0, 16);
      }
    }
  }
  STAMP(18);
  u32 sink = 0;
  if (SW) {
    asm volatile("s_waitcnt lgkmcnt(0)" ::: "memory");
    swarm15(sink, ((xcd & 4) ? wu : wg) + (size_t)(rr * 20 + (w >> 1) * 5) * 3072 + (xcd & 3) * 768 + (w & 1) * 384);
  }
  if (WDAT == 9 && j < 12 && w < 5) vm_wait<2>(); else vm_wait<0>();
  if (SW) bar0(); else { bar(); STAMP(2); }
  if (w == FW && lane < (j == 15 ? 2 : 1)) __hip_atomic_store((GAS u64*)(ws + (lane == 0 ? flag_off(0, p, j) : WS_WOF + p * 8)), tag, RLX_AGENT);
  asm volatile("" ::: "memory");
  if (w != PW && w != 7) {
    const int kr = (w < PW ? w : w - 1) * 64 + lane;
    const bool valid = 128 * (pa - 2) + kr >= 0;
    *(LAS u32x4*)(smem + L_K + 5 * KCH + kr * 16) = u32x4{valid ? 0u : 0xf149u, 0u, ONES2, ONES2};
  }
  if (w == PW) poll_flags(ws, fl_get, tag);
  if (SW) bar0(); else { bar(); STAMP(3); }

  u32x2 xr[6];
  u32 voffG[3];
  {
    const int h = w & 1, qt = w >> 1, l32 = lane & 31, hh = lane >> 5;
    u32x4 qf[2];
    gld_sc1(qf[0], ws + WS_QKV + (pa * 240 + 5 * ja + 2 * h) * 2048 + qt * 512, hh * 2048 + l32 * 16);
    gld_sc1(qf[1], ws + WS_QKV + (pa * 240 + 5 * ja + 2 * h + 2) * 2048 + qt * 512, l32 * 16);
#pragma unroll
    for (int i = 0; i < 5; ++i) dma_sc1(ws + kvsrc[i], lane * 16, kvdst[i]);
    u32 dm[8];
#pragma unroll
    for (int m = 0; m < 8; ++m) {
      const int k0 = ((2 * m) & 3) + 8 * (m >> 1) + 4 * hh;
      dm[m] = (k0 <= l32 ? 0xffffu : 0u) | (k0 + 1 <= l32 ? 0xffff0000u : 0u);
      asm volatile("" : "+v"(dm[m]));
    }
    u32 adK0 = L_K + (2 * h + hh) * KCH + (32 * qt + l32) * 16, adK1 = L_K + (hh ? 5 : 2 * h + 2) * KCH + (32 * qt + l32) * 16;
    const u32 vrow = 32 * qt + 4 * hh + q4;
    u32 adV = (g & 1) == 0 ? L_V + (3 * h + (pq >> 1)) * VCH + vrow * 16 + (pq & 1) * 8
                           : (pq == 0 ? L_V + 2 * VCH + vrow * 16 + 8 * h : L_K + 5 * KCH + ((4 * hh + (h ? 4 : 12)) & 15) * 16 + 8);
#pragma unroll
    for (int i = 0; i < 3; ++i) {
      const int s = 64 * i + lane, rw = s / 12, cp = s - rw * 12;
      int c = cp - (w & 1) - 2 * ((rw >> 3) & 1); c = c < 0 ? c + 12 : c;
      voffG[i] = rw * 3072 + c * 16;
    }
    asm volatile("" : "+v"(adK0), "+v"(adK1), "+v"(adV), "+v"(voffG[0]), "+v"(voffG[1]), "+v"(voffG[2]));
    vm_wait<1>();
    asm volatile("" : "+v"(qf[0]), "+v"(qf[1]));
    bar();
    asm volatile("" : "+s"(sink));
    STAMP(4);
    const f32x16 zero16 = {0, 0, 0, 0, 0, 0, 0, 0, 0, 0, 0, 0, 0, 0, 0, 0};
    u32x4 kf[9][2];
    constexpr int ord[9] = {0, 8, 1, 2, 3, 4, 5, 6, 7};
#pragma unroll
    for (int n = 0; n < 4; ++n) { kf[ord[n]][0] = lds128(adK0 + 512 * ord[n]); kf[ord[n]][1] = lds128(adK1 + 512 * ord[n]); }
    SB();
    dma_sc1(ws + kvsrc[5], lane * 16, kvdst[5]);
    if (h == 1 && hh == 0) { qf[0][0] = 0; qf[0][1] = 0; }
    if (h == 0) { qf[1][2] = 0; qf[1][3] = 0; }
    if (hh == 1) qf[1] = u32x4{0x3f80u, 0, 0, 0};
    SB();
    f32x16 sc[9];
    float m4[4];
    auto qk = [&](int i, int st) { sc[i] = mfma32(kf[i][st], qf[st], st == 0 ? zero16 : sc[i]); };
    qk(0, 0); qk(8, 0);
    SB();
    dma_sc1(ws + kvsrc[6], lane * 16, kvdst[6]);
    SB();
    qk(0, 1); qk(8, 1);
    SB();
    u32x2 wof;
    if (w < 4) {
      dma_sc1(ws + kvsrc[7], lane * 16, kvdst[7]);
      asm volatile("global_load_dwordx2 %0, %1, %2 sc1" : "=v"(wof) : "v"(r16 * 8), "s"(ws + WS_WOF));
    } else asm volatile("v_mov_b64 %0, 0" : "=v"(wof));
    SB();
#pragma unroll
    for (int n = 2; n < 11; ++n) {
      if (n < 9) { qk(ord[n], 0); SB(); }
      if (n + 2 < 9) { kf[ord[n + 2]][0] = lds128(adK0 + 512 * ord[n + 2]); SB(); }
      if (n == 3) {
#pragma unroll
        for (int r = 6; r < 16; ++r) sc[0][r] = ((r & 3) + 8 * (r >> 2) + 4 * hh <= l32) ? sc[8][r] : sc[0][r];
      } else if (n > 3) {
#pragma unroll
        for (int c = 0; c < 4; ++c) m4[c] = max3(m4[c], sc[n - 3][2 * c], sc[n - 3][2 * c + 1]);
      }
      SB();
      if (n < 9) { qk(ord[n], 1); SB(); }
      if (n + 2 < 9) { kf[ord[n + 2]][1] = lds128(adK1 + 512 * ord[n + 2]); SB(); }
      if (n == 2) {
#pragma unroll
        for (int r = 0; r < 6; ++r) sc[0][r] = ((r & 3) + 8 * (r >> 2) + 4 * hh <= l32) ? sc[8][r] : sc[0][r];
      } else if (n == 3) {
#pragma unroll
        for (int c = 0; c < 4; ++c) m4[c] = max3(max3(sc[0][4 * c], sc[0][4 * c + 1], sc[0][4 * c + 2]), sc[0][4 * c + 3], sc[0][4 * c + 3]);
      } else {
#pragma unroll
        for (int c = 0; c < 4; ++c) m4[c] = max3(m4[c], sc[n - 3][8 + 2 * c], sc[n - 3][9 + 2 * c]);
      }
      SB();
    }
    float mx = __builtin_fmaxf(max3(m4[0], m4[1], m4[2]), m4[3]);
    { auto sw = __builtin_amdgcn_permlane32_swap(__float_as_uint(mx), __float_as_uint(mx), false, false); mx = __builtin_fmaxf(__uint_as_float(sw[0]), __uint_as_float(sw[1])); }
    u32x4 nmf;
    {
      const float nmx = -mx;
      const u32 p0 = __float_as_uint(nmx) & 0xffff0000u;
      const float r1 = nmx - __uint_as_float(p0);
      const u32 p1 = __float_as_uint(r1) & 0xffff0000u;
      const u32 p2 = __float_as_uint(r1 - __uint_as_float(p1));
      nmf = u32x4{hh ? p2 >> 16 : (p0 >> 16) | p1, 0, 0, 0};
    }
    auto submax = [&](int i) { sc[i] = mfma32(ones8, nmf, sc[i]); };
    submax(0); submax(1);
    SB();
    u32x4 rtv[3];
    if (w < 4) {
#pragma unroll
      for (int i = 0; i < 3; ++i) { const u32 l = L_RT + (w + 4 * i) * 1024 + q4 * 128 + g * 32 + pq * 8; rtv[i] = ldstr2(l, l + 512); }
    }
    STAMP(29);
    vm_wait<0>();
    bar();
    STAMP(26);
    if (w < 4) {
      if (WPOS == 2) warm_gu();
      if (j < 12) {
        const int gi = p * 12 + j;
#pragma unroll
        for (int i = 0; i < 3; ++i)
          if (w + 4 * i < 10) st128_sc1(rs, rt_dst(w + 4 * i, 20 + (gi >> 2), gi & 3), rtv[i]);
      }
      asm volatile("" : "+v"(wof));
      if (WDEC && !__all(wof[0] == (u32)tag && wof[1] == (u32)(tag >> 32))) poll_flags(ws, WS_WOF + r16 * 8, tag);
#pragma unroll
      for (int i = 0; i < 20 - 2 * NP; ++i) {
        const u32 o = 20480 + (8 * NP + w * (20 - 2 * NP) + i) * 1024;
        dma_sc1(wt + o, lane * 16, L_WOB + o);
      }
#pragma unroll
      for (int t = 0; t < 5; ++t) gld64(xr[t], xa + (64 * half + 16 * w) * 1280 + slice * 160 + t * 32, r16 * 1280 + g * 8);
      asm volatile("global_load_dwordx2 %0, %1, %2 sc1" : "=v"(xr[5]) : "v"(r16 * 16), "s"(ws + WS_RS + (p * 128 + 64 * half + 16 * w) * 16));
      if (!SW && WPOS == 1 && (WGAT > 9 || j == 15)) warm_gu();
    } else {
#pragma unroll
      for (int t = 0; t < 6; ++t) asm volatile("v_mov_b64 %0, 0" : "=v"(xr[t]));
    }
    SB();
    f32x16 o = zero16;
    u32 pw[2][8];
    u32x2 vf[2][4], vx[4];
    auto rdV = [&](int i, int e) { return ldstr(adV + 512 * i + 128 * e); };
    auto pfrag = [&](const u32* q, int st) { return u32x4{q[4 * st], q[4 * st + 1], q[4 * st + 2], q[4 * st + 3]}; };
    u32 p8[8];
#pragma unroll
    for (int n = 0; n < 8; ++n) {
      float ex[16];
#pragma unroll
      for (int e = 0; e < 8; ++e) {
        ex[2 * e] = __builtin_amdgcn_exp2f(sc[n][2 * e]); SB();
        if (e < 4) { vf[n & 1][e] = rdV(n, e); SB(); }
        else if (n == 0) { vx[e - 4] = rdV(8, e - 4); SB(); }
        ex[2 * e + 1] = __builtin_amdgcn_exp2f(sc[n][2 * e + 1]); SB();
        if (e > 0) { pw[n & 1][e - 1] = pack2(ex[2 * e - 2], ex[2 * e - 1]); SB(); }
        if (n >= 1) {
          if (e == 0 && n < 7) { submax(n + 1); SB(); }
          if (e == 3) { o = mfma32(cat(vf[(n - 1) & 1][0], vf[(n - 1) & 1][1]), pfrag(pw[(n - 1) & 1], 0), o); SB(); }
          if (e == 6) { o = mfma32(cat(vf[(n - 1) & 1][2], vf[(n - 1) & 1][3]), pfrag(pw[(n - 1) & 1], 1), o); SB(); }
          if (n == 2 && e == 2) { o = mfma32(cat(vx[0], vx[1]), pfrag(p8, 0), o); SB(); }
          if (n == 2 && e == 5) { o = mfma32(cat(vx[2], vx[3]), pfrag(p8, 1), o); SB(); }
        }
      }
      pw[n & 1][7] = pack2(ex[14], ex[15]);
      if (n == 0) {
#pragma unroll
        for (int m = 0; m < 8; ++m) { p8[m] = pw[0][m] & dm[m]; pw[0][m] &= ~dm[m]; }
      }
      SB();
    }
    o = mfma32(cat(vf[1][0], vf[1][1]), pfrag(pw[1], 0), o);
    o = mfma32(cat(vf[1][2], vf[1][3]), pfrag(pw[1], 1), o);
    const float inv = __builtin_amdgcn_rcpf(o[12]);
    const u32 x0 = pack2(o[0] * inv, o[1] * inv), x1 = pack2(o[2] * inv, o[3] * inv), y0 = pack2(o[4] * inv, o[5] * inv), y1 = pack2(o[6] * inv, o[7] * inv);
    auto s0 = __builtin_amdgcn_permlane32_swap(x0, y0, false, false);
    auto s1 = __builtin_amdgcn_permlane32_swap(x1, y1, false, false);
    const u32 ob = WS_O + (pa * 80 + 5 * ja) * 2048 + (32 * qt + l32) * 16;
    st128_sc1(rs, ob + (3 * h + hh) * 2048, u32x4{s0[0], s1[0], s0[1], s1[1]});
    if (hh == 0) st64_sc1(rs, ob + 2 * 2048 + 8 * h, u32x2{pack2(o[8] * inv, o[9] * inv), pack2(o[10] * inv, o[11] * inv)});
    STAMP(5);
  }
  const char* gsrc = ((w & 1) ? wu : wg) + jg * 192;
  auto stageGBp = [&](int c, int rb, int i) {
    dma(gsrc + (size_t)(c * 64 + rb * 16) * 3072, voffG[i], gb_off(c) + (w & 1) * 12288 + rb * 3072 + i * 1024);
  };
  auto stageGB = [&](int c, int rb) { stageGBp(c, rb, 0); stageGBp(c, rb, 1); stageGBp(c, rb, 2); };
  const int wm4 = w & 3, wk = w >> 2, kh = KSWAP ? wk ^ (slice & 1) : wk;
  u32 voffR = g * 2048 + (64 * half + 16 * wm4 + r16) * 16;
  fl_get = flag_off(1, p, r16);
  u32 fl_get2 = flag_off(2, pg, r16), fl_put = lane == 0 ? flag_off(1, pa, ja) : WS_DENSE + bx * 8;
  asm volatile("" : "+v"(fl_put));
  asm volatile("" : "+v"(voffR), "+v"(fl_get), "+v"(fl_get2));
  asm volatile("" ::: "memory");
  bar();
  if (w < 4) {
#pragma unroll
    for (int i = 0; i < 5; ++i) dma_sc1(wt + 5120 * w + i * 1024, lane * 16, L_WOB + 5120 * w + i * 1024);
    dma_sc1(ws + WS_WB + (w < 3 ? w : 2) * 1024, lane * 16, L_WB + (w < 3 ? w : 2) * 1024);
    vm_wait<6>();
  } else {
    if (X_A) vm_wait<0>(); else {
    stageGB(2, w >> 1); stageGB(2, (w >> 1) - 2);
    vm_wait<6>(); }
  }
  bar0();
  STAMP(6);
  u32x2 hp[5];
  {
    if (w == FW && lane < 2) __hip_atomic_store((GAS u64*)(ws + fl_put), tag, RLX_AGENT);
    u32x4 a[10], b[6][5];
    const u32 ad0 = L_WOB + kh * 10 * 5120 + lane * 16, adm = L_WB + (r16 & 1) * 1280 + kh * 640 + g * 16;
    const bool alt = KROT && (slice & 2);
    auto kso = [&](int s) { return alt ? (s < 6 ? 4 + (s + 3) % 6 : (s - 4) & 3) : (s + 4) % 10; };
    auto rdB = [&](int s, int t) { b[s % 6][t] = lds128(ad0 + kso(s) * 5120 + t * 1024); };
#pragma unroll
    for (int s = 0; s < 6; ++s)
#pragma unroll
      for (int t = 0; t < 5; ++t) rdB(s, t);
    asm volatile("" : "+v"(xr[0]), "+v"(xr[1]), "+v"(xr[2]), "+v"(xr[3]), "+v"(xr[4]), "+v"(xr[5]));
    f32x4 hacc[5], macc;
    { const u32 xm = xr[5][0]; macc = f32x4{__uint_as_float(xm), 0.f, 0.f, 0.f}; }
#pragma unroll
    for (int t = 0; t < 5; ++t) { const u32 a0 = xr[t][0], a1 = xr[t][1]; hacc[t] = f32x4{bflo(a0), bfhi(a0), bflo(a1), bfhi(a1)}; }
    SB();
    if (w == PW) poll_flags(ws, fl_get, tag); else if (w < 4) vm_wait<0>();
    bar();
    STAMP(7);
    const char* osrc = ws + WS_O + (p * 80 + 40 * kh) * 2048;
#pragma unroll
    for (int s = 0; s < 10; ++s) gld_sc1(a[s], osrc + kso(s) * 8192, voffR);
    u32x4 bm[2];
    bm[0] = lds128(adm + kso(0) * 64);
    SB();
#pragma unroll
    for (int s = 0; s < 10; ++s) {
      switch (s) {
        case 0: vm_wait_use<9>(a[0]); break; case 1: vm_wait_use<8>(a[1]); break; case 2: vm_wait_use<7>(a[2]); break;
        case 3: vm_wait_use<6>(a[3]); break; case 4: vm_wait_use<5>(a[4]); break; case 5: vm_wait_use<4>(a[5]); break;
        case 6: vm_wait_use<3>(a[6]); break; case 7: vm_wait_use<2>(a[7]); break; case 8: vm_wait_use<1>(a[8]); break;
        default: vm_wait_use<0>(a[9]); break;
      }
      if (s == 0) STAMP(28);
#pragma unroll
      for (int t = 0; t < 5; ++t) {
        hacc[t] = mfma(b[s % 6][t], a[s], hacc[t]);
        SB();
        if (s + 6 < 10) { rdB(s + 6, t); SB(); }
        if (t == 0 && s < 9) { bm[(s + 1) & 1] = lds128(adm + kso(s + 1) * 64); SB(); }
      }
      macc = mfma(bm[s & 1], a[s], macc);
      SB();
    }
    STAMP(8);
    if (wk == 0) {
#pragma unroll
      for (int t = 0; t < 5; ++t) *(LAS f32x4*)(smem + L_RED + (wm4 * 5 + t) * 1024 + lane * 16) = hacc[t];
      *(LAS float*)(smem + L_RED + 20480 + wm4 * 256 + lane * 4) = macc[0] + macc[1];
    }
    bar();
    if (wk == 1) {
      float s1 = 0.f, s2 = 0.f;
      const float mean = macc[0] + macc[1] + *(LAS const float*)(smem + L_RED + 20480 + wm4 * 256 + lane * 4);
      u32x2 hc[5];
#pragma unroll
      for (int t = 0; t < 5; ++t) {
        const f32x4 v = hacc[t] + *(LAS const f32x4*)(smem + L_RED + (wm4 * 5 + t) * 1024 + lane * 16);
        const u32 lo = pack2(v[0], v[1]), hi = pack2(v[2], v[3]);
        hp[t] = u32x2{lo, hi};
        hc[t] = u32x2{pack2(bflo(lo) - mean, bfhi(lo) - mean), pack2(bflo(hi) - mean, bfhi(hi) - mean)};
      }
      const u32 hb = WS_H + (p * 80 + 10 * slice + 2 * (g & 1) + (g >> 1)) * 2048 + (64 * half + 16 * wm4 + r16) * 16;
      st128_sc1(rs, hb, quad2chunk(hc[0], hc[1]));
      st128_sc1(rs, hb + 4 * 2048, quad2chunk(hc[2], hc[3]));
      const u32x4 last = quad2chunk(hc[4], hc[4]);
      if ((g & 1) == 0) st128_sc1(rs, hb + 8 * 2048, last);
#pragma unroll
      for (int t = 0; t < 5; ++t) { const u32 lo = hc[t][0], hi = hc[t][1]; s1 = dot2(hi, ONES2, dot2(lo, ONES2, s1)); s2 = dot2(hi, hi, dot2(lo, lo, s2)); }
      s1 = gsum(s1); s2 = gsum(s2);
      if (g == 0) st64_sc1(rs, WS_ST + (p * 8 + slice) * 1024 + (64 * half + 16 * wm4 + r16) * 8, u32x2{__float_as_uint(s1), __float_as_uint(s2)});
      if (W3BAR) bar();
      vm_wait<0>();
    } else {
#pragma unroll
      for (int t = 0; t < 5; ++t) hp[t] = u32x2{0, 0};
      if (W3BAR) bar();
      if (!X_B) { stageGB(0, w >> 1); stageGB(0, 2 + (w >> 1)); stageGB(1, w >> 1); stageGB(1, 2 + (w >> 1)); }
    }
  }
  bar();
  STAMP(9);
  if (w == FW && lane == 0) __hip_atomic_store((GAS u64*)(ws + flag_off(2, p, j)), tag, RLX_AGENT);
  if (w == PW) {
    poll_flags(ws, fl_get2, tag);
  } else if (w == 2) {
    GAS u64* a = (GAS u64*)(ws + WS_DENSE + lane * 8);
    for (u32 spins = 0; spins < SPIN_CAP; ++spins) {
      const u64 v0 = __hip_atomic_load(a, RLX_AGENT), v1 = __hip_atomic_load(a + 64, RLX_AGENT), v2 = __hip_atomic_load(a + 128, RLX_AGENT), v3 = __hip_atomic_load(a + 192, RLX_AGENT);
      if (__all(v0 == tag && v1 == tag && v2 == tag && v3 == tag)) break;
    }
    if (bx == 0 && lane == 0) __hip_atomic_store((GAS u64*)(ws + WS_EPOCH), tag, RLX_AGENT);
  } else if (wk == 1) {
    if (!X_C) { stageGB(3, w >> 1); stageGB(3, (w >> 1) - 2); }
  }
  bar();
  STAMP(10);

  const int rotd = DROT == 0 ? 0 : (DMAP ? (((3 * (rr & 3)) >> 1) + (DROT == 2 ? ((6 * (rr >> 2)) >> 3) : 0)) % 6 : (((slice * 6) >> 3) + (DROT == 2 ? ((3 * (rr >> 3)) >> 1) : 0)) % 6);
  auto stageDq = [&](int c, int blk, int i) {
    int kc = c + rotd; kc = kc >= 6 ? kc - 6 : kc;
    dma_sc1(wt + (20 + kc * 8 + blk) * 5120 + i * 1024, lane * 16, (c & 3) * 40960 + blk * 5120 + i * 1024);
  };
  auto stageDp = [&](int c, int i) { stageDq(c, w, i); };
  auto stageD = [&](int c) {
#pragma unroll
    for (int i = 0; i < 5; ++i) stageDp(c, i);
  };

  {
    const int wm = w >> 2, wn = w & 3;
    const char* hsrc = ws + WS_H + (pg * 80 + w) * 2048;
    auto stageGAp = [&](int c, int i) { dma_sc1(hsrc + c * 16384 + i * 1024, lane * 16, ga_off(c) + w * 2048 + i * 1024); };
    auto stageGA = [&](int c) { stageGAp(c, 0); stageGAp(c, 1); };
    stageGA(0);
    dma_sc1(ws + WS_ST + (pg * 8 + w) * 1024, lane * 16, L_ST + w * 1024);
    stageGA(1);
    const u32 adA = g * 2048 + (64 * wm + r16) * 16;
    u32 adB[3];
#pragma unroll
    for (int t = 0; t < 3; ++t) {
      const int mat = t == 0 ? 0 : (t == 1 ? 1 : (pq >> 1)), c = t < 2 ? 3 * wn + (pq >> 1) : 3 * wn + 2;
      int cp = c + mat + 2 * (g & 1); cp = cp >= 12 ? cp - 12 : cp;
      adB[t] = mat * 12288 + (8 * g + q4) * 192 + cp * 16 + (pq & 1) * 8;
    }
    f32x4 acc[4][3];
#pragma unroll
    for (int m = 0; m < 4; ++m)
#pragma unroll
      for (int t = 0; t < 3; ++t) acc[m][t] = f32x4{0, 0, 0, 0};
    u32x4 fa[2][4]; u32x2 fb[2][3][2];
    auto rdp = [&](int c, int s, int i) {
      const u32 sa = ga_off(c) + s * 8192, sb = gb_off(c) + s * 6144;
      if (i < 2) fb[s][0][i] = ldstr(sb + adB[0] + i * 768);
      else if (i == 2) fa[s][0] = lds128(sa + adA);
      else if (i < 7) fb[s][1 + ((i - 3) >> 1)][(i - 3) & 1] = ldstr(sb + adB[1 + ((i - 3) >> 1)] + ((i - 3) & 1) * 768);
      else if (i < 10) fa[s][i - 6] = lds128(sa + adA + (i - 6) * 256);
    };
    auto mm = [&](int s, int c) {
      u32x4 b[3];
#pragma unroll
      for (int t = 0; t < 3; ++t) b[t] = cat(fb[s][t][0], fb[s][t][1]);
      const int cr = s == 0 ? c : c + 1;
#pragma unroll
      for (int i = 0; i < 12; ++i) {
        acc[i / 3][i % 3] = mfma(b[i % 3], fa[s][i / 3], acc[i / 3][i % 3]);
        if (cr < 10) rdp(cr, s ^ 1, i);
        if (s == 1 && (i == 4 || i == 10) && c + 3 < 10) stageGAp(c + 3, i == 10);
        if (s == 0 && i % 4 == 3 && c >= 1 && c + 3 < 10) stageGBp(c + 3, w >> 1, i >> 2);
        if (DWARM && s == 1 && i == 7 && c >= 1 && c <= 4 && (w >> 1) == c - 1)
          warm_sc1(ws + WS_WT + w * WT_SLICE + 102400 + rr * 7680, (lane < 60 ? lane : 59) * 128, L_WARM);
        if (s == 1 && (c == 7 || c == 8) && i % 2 == 1 && i < 10) stageDp(c - 7, i >> 1);
        SB();
      }
    };
    vm_wait<3>();
    bar();
    STAMP(19);
#pragma unroll
    for (int i = 0; i < 10; ++i) rdp(0, 0, i);
    SB();
    stageGA(2);
    SB();
#pragma unroll
    for (int c = 0; c < 10; ++c) {
      if (c == 1) {
        const f32x2 u0 = *(LAS const f32x2*)(smem + L_ST + (2 * g) * 1024 + (16 * w + r16) * 8), u1 = *(LAS const f32x2*)(smem + L_ST + (2 * g + 1) * 1024 + (16 * w + r16) * 8);
        const float s1 = gsum(u0[0] + u1[0]), s2 = gsum(u0[1] + u1[1]);
        const float mean = s1 * (1.f / 640.f), var = __builtin_fmaxf(s2 * (1.f / 640.f) - mean * mean, 0.f);
        if (g == 0) *(LAS float*)(smem + L_MISC + (16 * w + r16) * 4) = __builtin_amdgcn_rcpf(var + LN_EPS);
      }
      mm(0, c);
      if (c < 9) {
        if (c == 0) vm_wait<2>(); else if (c == 1) vm_wait<5>(); else if (c <= 6) vm_wait<8>(); else vm_wait<5>();
        bar();
        if (c == 4) STAMP(20);
        if (c == 7) STAMP(30);
      }
      mm(1, c);
    }
    STAMP(11);
    u32x2 p01[4];
    float r2[4];
#pragma unroll
    for (int m = 0; m < 4; ++m) r2[m] = *(LAS const float*)(smem + L_MISC + (64 * wm + 16 * m + r16) * 4);
    if (wm == 1) { vm_wait<5>(); bar(); }
#pragma unroll
    for (int m = 0; m < 4; ++m) {
      const f32x4 v = acc[m][0] * acc[m][1] * r2[m];
      p01[m] = u32x2{pack2(v[0], v[1]), pack2(v[2], v[3])};
    }
    const u32 hb = WS_HH + (pg * 192 + 12 * jg + 3 * wn) * 2048 + (64 * wm + r16) * 16;
    st128_sc1(rs, hb + (g >> 1) * 2048 + (g & 1) * 256, quad2chunk(p01[0], p01[1]));
    st128_sc1(rs, hb + (g >> 1) * 2048 + (2 + (g & 1)) * 256, quad2chunk(p01[2], p01[3]));
    u32x2 p2[2];
#pragma unroll
    for (int mm2 = 0; mm2 < 2; ++mm2) {
      f32x4 v;
#pragma unroll
      for (int r = 0; r < 4; ++r) {
        auto sw = __builtin_amdgcn_permlane32_swap(__float_as_uint(acc[2 * mm2][2][r]), __float_as_uint(acc[2 * mm2 + 1][2][r]), false, false);
        v[r] = __uint_as_float(sw[0]) * __uint_as_float(sw[1]) * (lane < 32 ? r2[2 * mm2] : r2[2 * mm2 + 1]);
      }
      p2[mm2] = u32x2{pack2(v[0], v[1]), pack2(v[2], v[3])};
    }
    st128_sc1(rs, hb + 2 * 2048 + (2 * (g & 1) + (g >> 1)) * 256, quad2chunk(p2[0], p2[1]));
    STAMP(31);
    if (wm == 0) {
      vm_wait<8>();
      bar();
#pragma unroll
      for (int i = 0; i < 10; ++i) stageDq(2, w + 4 * (i / 5), i % 5);
    }
  }

  {
    u32x4 b[6][5];
    const u32 ad0 = kh * 4 * 5120 + lane * 16;
    auto rdB = [&](int s, int t) { b[s % 6][t] = lds128(ad0 + ((s >> 2) & 3) * 40960 + (s & 3) * 5120 + t * 1024); };
#pragma unroll
    for (int s = 0; s < 4; ++s)
#pragma unroll
      for (int t = 0; t < 5; ++t) rdB(s, t);
    SB();
    if (wk == 0) vm_wait<10>(); else vm_wait<0>();
    bar();
    STAMP(12);
    if (w == FW && lane == 0) __hip_atomic_store((GAS u64*)(ws + flag_off(3, pg, jg)), tag, RLX_AGENT);
    if (wk == 1) {
#pragma unroll
      for (int i = 0; i < 10; ++i) if (!X_D) stageDq(3, w - 4 * (i / 5), i % 5);
    }
#pragma unroll
    for (int s = 4; s < 6; ++s)
#pragma unroll
      for (int t = 0; t < 5; ++t) rdB(s, t);
    f32x4 acc[5];
#pragma unroll
    for (int t = 0; t < 5; ++t) { const u32 a = hp[t][0], b2 = hp[t][1]; acc[t] = f32x4{bflo(a), bfhi(a), bflo(b2), bfhi(b2)}; }
    SB();
    if (w == PW) poll_flags(ws, fl_get + 2 * 65536, tag); else if (wk == 0) vm_wait<0>();
    bar();
    STAMP(13);
    u32x4 a[6][4];
    const char* asrc = ws + WS_HH + (p * 192 + 16 * kh) * 2048;
    auto loadAp = [&](int c, int s) {
      int kc = c + rotd; kc = kc >= 6 ? kc - 6 : kc;
      gld_sc1(a[c][s], asrc + kc * 65536 + s * 8192, voffR);
    };
    auto issue = [&](int n) {
      if (n < 4) loadAp(2, n); else if (n < 9) stageDp(4, n - 4); else if (n < 13) loadAp(3, n - 9); else if (n < 18) stageDp(5, n - 13);
      else if (n < 22) loadAp(4, n - 18); else if (n < 26) loadAp(5, n - 22);
    };
#pragma unroll
    for (int s = 0; s < 4; ++s) loadAp(0, s);
#pragma unroll
    for (int s = 0; s < 4; ++s) loadAp(1, s);
#pragma unroll
    for (int s = 0; s < 24; ++s) {
      if (s == 0) vm_wait<4>();
      if (s == 4) vm_wait<8>();
      if (s == 8) vm_wait<12>();
      if (s == 10) vm_wait<11>();
      if (s == 12) vm_wait<11>();
      if (s == 14) vm_wait<8>();
      if (s == 16) vm_wait<4>();
      if (s == 20) vm_wait<0>();
      if (s % 4 == 0) asm volatile("" : "+v"(a[s >> 2][0]), "+v"(a[s >> 2][1]), "+v"(a[s >> 2][2]), "+v"(a[s >> 2][3]));
      if (s == 6) bar();
      if (s == 10 || s == 14) bar0();
      if (s == 0) STAMP(21);
      if (s == 4) STAMP(22); if (s == 8) STAMP(23); if (s == 12) STAMP(24); if (s == 16) STAMP(25);
#pragma unroll
      for (int t = 0; t < 5; ++t) {
        acc[t] = mfma(b[s % 6][t], a[s >> 2][s & 3], acc[t]);
        SB();
        if (s + 6 < 24) { rdB(s + 6, t); SB(); }
        if (t == 1) { issue(2 * s); SB(); }
        if (t == 4) { issue(2 * s + 1); SB(); }
      }
    }
    STAMP(14);
    constexpr u32 L_RED2 = 2 * 40960;
    if (wk == 0) {
#pragma unroll
      for (int t = 0; t < 5; ++t) *(LAS f32x4*)(smem + L_RED2 + (wm4 * 5 + t) * 1024 + lane * 16) = acc[t];
    }
    bar();
    if (wk == 1) {
      u32x2 op[5];
#pragma unroll
      for (int t = 0; t < 5; ++t) {
        const f32x4 v = acc[t] + *(LAS const f32x4*)(smem + L_RED2 + (wm4 * 5 + t) * 1024 + lane * 16);
        op[t] = u32x2{pack2(v[0], v[1]), pack2(v[2], v[3])};
      }
      const __amdgpu_buffer_rsrc_t ro = __builtin_amdgcn_make_buffer_rsrc(out, 0, 2048 * 1280, 0x00020000);
      const u32 oo = (128 * p + 64 * half + 16 * wm4 + r16) * 1280 + slice * 160 + (2 * (g & 1) + (g >> 1)) * 16;
      __builtin_amdgcn_raw_buffer_store_b128(quad2chunk(op[0], op[1]), ro, (int)oo, 0, OUT_AUX);
      __builtin_amdgcn_raw_buffer_store_b128(quad2chunk(op[2], op[3]), ro, (int)oo + 64, 0, OUT_AUX);
      const u32x4 last = quad2chunk(op[4], op[4]);
      if ((g & 1) == 0) __builtin_amdgcn_raw_buffer_store_b128(last, ro, (int)oo + 128, 0, OUT_AUX);
    }
    STAMP(15);
  }
}

extern "C" void kernel_launch(void* const* d_in, const int* in_sizes, int n_in, void* d_out, int out_size, void* d_ws, size_t ws_size, hipStream_t stream) {
  (void)in_sizes; (void)n_in; (void)out_size; (void)ws_size;
  (void)hipFuncSetAttribute((const void*)fused_block, hipFuncAttributeMaxDynamicSharedMemorySize, LDS_BYTES);
  fused_block<<<256, 512, LDS_BYTES, stream>>>((const char*)d_in[0], (const char*)d_in[1], (const char*)d_in[2], (const char*)d_in[4], (const char*)d_in[5],
                                               (const char*)d_in[6], (char*)d_out, (char*)d_ws);
}
```

```cpp
#include <hip/hip_runtime.h>
#include <stdint.h>

typedef unsigned short u16;
typedef unsigned int u32;
typedef unsigned long long u64;
typedef __bf16 bf16x8 __attribute__((ext_vector_type(8)));
typedef __bf16 bf16x2 __attribute__((ext_vector_type(2)));
typedef short s16x4 __attribute__((ext_vector_type(4)));
typedef float f32x4 __attribute__((ext_vector_type(4)));
typedef float f32x2 __attribute__((ext_vector_type(2)));
typedef u32 u32x4 __attribute__((ext_vector_type(4)));
typedef u32 u32x2 __attribute__((ext_vector_type(2)));
#define LAS __attribute__((address_space(3)))
#define GAS __attribute__((address_space(1)))
#define DI __device__ __forceinline__
#define RLX_AGENT __ATOMIC_RELAXED, __HIP_MEMORY_SCOPE_AGENT

#define RG 5
#define OUT_AUX 0
#define W3BAR 0
#define DWARM 0
#define GMAP 0
#define DMAP 1
#define DROT 0
#define RSAT 8
#define X_A 0
#define X_B 0
#define X_C 0
#define X_D 0
#define POLLW 0
#define KSWAP 0
#define KROT 0
#define POLLV 8
#define FW 0
#define NEARLY 20
#define ATT_PRIO 0
#define NP 0
#define WPOS 1
#define XFIRST 2
#define XROT 0
#define WDEC 1
#define WDAT 1
#define WGAT 99
#define WGSP 0
#define KA 1
#define WH 3
#define SW 0
#define XL 1
#define XLAT 7
#define S1POS 2
#define STAMPS 0

constexpr u32 WS_EPOCH = 0;
constexpr u32 WS_WOF = 1024;
constexpr u32 WS_DENSE = 2048;
constexpr u32 WS_FLAG = 4096;
constexpr u32 WS_QKV = WS_FLAG + 4 * 256 * 256;
constexpr u32 WS_O = WS_QKV + 16 * 240 * 2048;
constexpr u32 WS_H = WS_O + 16 * 80 * 2048;
constexpr u32 WS_HH = WS_H + 16 * 80 * 2048;
constexpr u32 WS_ST = WS_HH + 16 * 192 * 2048;
constexpr u32 WS_RS = WS_ST + 16 * 8 * 1024;
constexpr u32 WS_WB = WS_RS + 2048 * 16;
constexpr u32 WT_SLICE = 68 * 5120;
constexpr u32 WS_WT = WS_WB + 3072;
constexpr u32 WS_END = WS_WT + 8 * WT_SLICE;
constexpr u32 WS_STAMP = WS_END;
constexpr u64 TAG_STEP = 0x9E3779B97F4A7C15ull;

constexpr u32 LDS_BYTES = 163840;
constexpr u32 L_MISC = 161792;
constexpr u32 L_K = 0, KCH = 6144;
constexpr u32 L_V = 6 * KCH, VCH = 6208;
constexpr u32 L_WOB = L_MISC - 102400;
__device__ constexpr u32 gb_off(int c) { return (c & 3) == 2 ? 0 : ((c & 3) == 3 ? 24576 : ((c & 3) == 0 ? 65536 : 106496)); }
__device__ constexpr u32 ga_off(int c) { return c % 3 == 1 ? 49152 : (c % 3 == 2 ? 90112 : 131072); }
constexpr u32 L_ST = 147456;
constexpr u32 L_WB = 49152;
constexpr u32 L_RED = 24576;
constexpr u32 L_WARM = L_MISC + 1792;
constexpr u32 L_XT = L_V + 5 * VCH;
constexpr u32 L_RT = 131072;
constexpr u32 SPIN_CAP = 1u << 15;
constexpr float LN_EPS = 1e-5f;

extern __shared__ __attribute__((aligned(16))) char smem[];

DI u32x4 lds128(u32 off) { return *(LAS const u32x4*)(smem + off); }
DI u32x2 ldstr(u32 off) { return __builtin_bit_cast(u32x2, __builtin_amdgcn_ds_read_tr16_b64_v4i16((LAS s16x4*)(smem + off))); }
DI u32x4 ldstr2(u32 lo, u32 hi) { const u32x2 a = ldstr(lo), b = ldstr(hi); return u32x4{a[0], a[1], b[0], b[1]}; }
DI f32x4 mfma(u32x4 a, u32x4 b, f32x4 c) { return __builtin_amdgcn_mfma_f32_16x16x32_bf16(__builtin_bit_cast(bf16x8, a), __builtin_bit_cast(bf16x8, b), c, 0, 0, 0); }
typedef float f32x16 __attribute__((ext_vector_type(16)));
DI f32x16 mfma32(u32x4 a, u32x4 b, f32x16 c) { return __builtin_amdgcn_mfma_f32_32x32x16_bf16(__builtin_bit_cast(bf16x8, a), __builtin_bit_cast(bf16x8, b), c, 0, 0, 0); }
DI float max3(float a, float b, float c) { return __builtin_fmaxf(__builtin_fmaxf(a, b), c); }
DI f32x4 mfma16(u32x2 a, u32x2 b, f32x4 c) { return __builtin_amdgcn_mfma_f32_16x16x16bf16_1k(__builtin_bit_cast(s16x4, a), __builtin_bit_cast(s16x4, b), c, 0, 0, 0); }
DI float dot2(u32 a, u32 b, float c) { return __builtin_amdgcn_fdot2_f32_bf16(__builtin_bit_cast(bf16x2, a), __builtin_bit_cast(bf16x2, b), c, false); }
DI u32 pack2(float lo, float hi) { const f32x2 v = {lo, hi}; return __builtin_bit_cast(u32, __builtin_convertvector(v, bf16x2)); }
DI float bflo(u32 v) { return __uint_as_float(v << 16); }
DI float bfhi(u32 v) { return __uint_as_float(v & 0xffff0000u); }
constexpr u32 ONES2 = 0x3f803f80u;
DI float sum8(u32x4 a, float s) { const u32 a0 = a[0], a1 = a[1], a2 = a[2], a3 = a[3]; return dot2(a3, ONES2, dot2(a2, ONES2, dot2(a1, ONES2, dot2(a0, ONES2, s)))); }
DI float sq8(u32x4 a, float s) { const u32 a0 = a[0], a1 = a[1], a2 = a[2], a3 = a[3]; return dot2(a3, a3, dot2(a2, a2, dot2(a1, a1, dot2(a0, a0, s)))); }
DI float gsum(float x) {
  auto a = __builtin_amdgcn_permlane16_swap(__float_as_uint(x), __float_as_uint(x), false, false);
  x = __uint_as_float(a[0]) + __uint_as_float(a[1]);
  auto b = __builtin_amdgcn_permlane32_swap(__float_as_uint(x), __float_as_uint(x), false, false);
  return __uint_as_float(b[0]) + __uint_as_float(b[1]);
}
DI float gmax(float x) {
  auto a = __builtin_amdgcn_permlane16_swap(__float_as_uint(x), __float_as_uint(x), false, false);
  x = __builtin_fmaxf(__uint_as_float(a[0]), __uint_as_float(a[1]));
  auto b = __builtin_amdgcn_permlane32_swap(__float_as_uint(x), __float_as_uint(x), false, false);
  return __builtin_fmaxf(__uint_as_float(b[0]), __uint_as_float(b[1]));
}
DI u32x4 quad2chunk(u32x2 A, u32x2 B) {
  const u32 a0 = A[0], a1 = A[1], b0 = B[0], b1 = B[1];
  auto s0 = __builtin_amdgcn_permlane16_swap(a0, b0, false, false);
  auto s1 = __builtin_amdgcn_permlane16_swap(a1, b1, false, false);
  return u32x4{s0[0], s1[0], s0[1], s1[1]};
}
DI void dma(const void* sbase, u32 voff, u32 lds) {
  asm volatile("s_mov_b32 m0, %2\n\ts_nop 0\n\tglobal_load_lds_dwordx4 %0, %1" ::"v"(voff), "s"(sbase), "s"(lds));
}
DI void dma_sc1(const void* sbase, u32 voff, u32 lds) {
  asm volatile("s_mov_b32 m0, %2\n\ts_nop 0\n\tglobal_load_lds_dwordx4 %0, %1 sc1" ::"v"(voff), "s"(sbase), "s"(lds));
}
DI void gld_sc1(u32x4& dst, const void* sbase, u32 voff) {
  asm volatile("global_load_dwordx4 %0, %1, %2 sc1" : "=v"(dst) : "v"(voff), "s"(sbase));
}
DI void gld(u32x4& dst, const void* sbase, u32 voff) {
  asm volatile("global_load_dwordx4 %0, %1, %2" : "=v"(dst) : "v"(voff), "s"(sbase));
}
DI void gld64(u32x2& dst, const void* sbase, u32 voff) {
  asm volatile("global_load_dwordx2 %0, %1, %2" : "=v"(dst) : "v"(voff), "s"(sbase));
}
template <int N> DI void vm_wait() { asm volatile("s_waitcnt vmcnt(%0)" ::"n"(N) : "memory"); }
template <int N> DI void vm_wait_use(u32x4& r) { asm volatile("s_waitcnt vmcnt(%1)" : "+v"(r) : "n"(N)); }
#define INL __attribute__((always_inline))
#define SB() __builtin_amdgcn_sched_barrier(0)
DI void warm(const void* sbase, u32 voff, u32 lds) {
  asm volatile("s_mov_b32 m0, %2\n\ts_nop 0\n\tglobal_load_lds_dword %0, %1" ::"v"(voff), "s"(sbase), "s"(lds));
}
DI void warm_sc1(const void* sbase, u32 voff, u32 lds) {
  asm volatile("s_mov_b32 m0, %2\n\ts_nop 0\n\tglobal_load_lds_dword %0, %1 sc1" ::"v"(voff), "s"(sbase), "s"(lds));
}
#define SL(o) "s_load_dword %0, %1, " #o "\n\t"
DI void swarm15(u32& sink, const void* base) {
  asm volatile(SL(0) SL(128) SL(256) SL(3072) SL(3200) SL(3328) SL(6144) SL(6272) SL(6400) SL(9216) SL(9344) SL(9472) SL(12288) SL(12416) SL(12544) : "=&s"(sink) : "s"(base));
}
DI void bar0() { asm volatile("s_barrier" ::: "memory"); }
DI u32x4 cat(u32x2 a, u32x2 b) { return u32x4{a[0], a[1], b[0], b[1]}; }
DI void bar() { asm volatile("s_waitcnt lgkmcnt(0)\n\ts_barrier" ::: "memory"); }
DI void st128_sc1(__amdgpu_buffer_rsrc_t rs, u32 off, u32x4 v) { __builtin_amdgcn_raw_buffer_store_b128(v, rs, (int)off, 0, 16); }
DI void st64_sc1(__amdgpu_buffer_rsrc_t rs, u32 off, u32x2 v) { __builtin_amdgcn_raw_buffer_store_b64(v, rs, (int)off, 0, 16); }

DI void poll_flags(const char* ws, u32 off, u64 tag) {
  GAS u64* a = (GAS u64*)(ws + off);
  __builtin_amdgcn_s_sleep(POLLV);
  for (u32 spins = 0; spins < SPIN_CAP; ++spins) {
    const u64 v = __hip_atomic_load(a, RLX_AGENT);
    if (__all(v == tag)) break;
    if (POLLW) __builtin_amdgcn_s_sleep(POLLW);
  }
  __builtin_amdgcn_fence(__ATOMIC_ACQUIRE, "wavefront");
}
DI u32 flag_off(int phase, int p, int j) { return WS_FLAG + (u32)((phase * 256 + p * 16 + j) * 256); }

#define STAMP(i) do {} while (0)

__global__ __launch_bounds__(512, 2) void fused_block(const char* __restrict__ x, const char* __restrict__ wqkv, const char* __restrict__ wo,
                                                      const char* __restrict__ wg, const char* __restrict__ wu, const char* __restrict__ wd,
                                                      char* __restrict__ out, char* ws) {
  const int tid = threadIdx.x, lane = tid & 63, w = __builtin_amdgcn_readfirstlane(tid >> 6);
  const int r16 = lane & 15, g = lane >> 4, q4 = r16 >> 2, pq = r16 & 3;
  const int bx = blockIdx.x, xcd = bx & 7, rr = bx >> 3;
  const int p = (xcd >> 1) * 4 + (rr >> 3), j = (xcd & 1) * 8 + (rr & 7);
  const int pa = rr >> 1, ja = 2 * xcd + (rr & 1);
  const int pg = GMAP ? pa : 8 * (xcd >> 2) + (rr >> 2), jg = GMAP ? ja : 4 * (xcd & 3) + (rr & 3);
  const int half = DMAP ? (rr >> 2) & 1 : j >> 3, slice = DMAP ? 4 * (xcd & 1) + (rr & 3) : j & 7;
  constexpr int PW = 3;
  const __amdgpu_buffer_rsrc_t rs = __builtin_amdgcn_make_buffer_rsrc(ws, 0, WS_END, 0x00020000);
  STAMP(0);
  if (KA) asm volatile("" ::"s"(x), "s"(wqkv), "s"(ws));
  u64 tag;
  auto load_epoch = [&](u32x2& e) { const u32 z = 0; asm volatile("global_load_dwordx2 %0, %1, %2 sc1" : "=v"(e) : "v"(z), "s"(ws + WS_EPOCH)); };
  auto make_tag = [&](u32x2& e) {
    asm volatile("" : "+v"(e));
    const u32 e0 = e[0], e1 = e[1];
    tag = (((u64)(u32)__builtin_amdgcn_readfirstlane(e1) << 32) | (u32)__builtin_amdgcn_readfirstlane(e0)) + TAG_STEP;
  };
  const char* xa = x + (size_t)p * 128 * 1280;
  const u32x4 ones8 = {ONES2, ONES2, ONES2, ONES2};
  const char* wt = ws + WS_WT + slice * WT_SLICE;
  auto rt_dst = [&](int piece, int blk, int gq) {
    const int tile = 4 * piece + g, sl = tile / 5, t = tile - sl * 5;
    return WS_WT + sl * WT_SLICE + blk * 5120 + t * 1024 + gq * 256 + r16 * 16;
  };
  auto warm_gu = [&]() INL {
    if (GMAP) {
      const int l3 = lane < 30 ? lane : 29, rw = l3 / 3;
      warm(((w & 1) ? wu : wg) + (size_t)(rr * 20 + (w >> 1) * 10) * 3072 + xcd * 384, rw * 3072 + (l3 - rw * 3) * 128, L_WARM);
      return;
    }
    if (WH == 1) {
      const int l6 = lane < 30 ? lane : 29, rw = l6 / 6;
      warm(((xcd & 4) ? wu : wg) + (size_t)(rr * 20 + (w & 3) * 5) * 3072 + (xcd & 3) * 768, rw * 3072 + (l6 - rw * 6) * 128, L_WARM);
      return;
    }
    if (WH == 2) {
      const int l6 = lane < 15 ? lane : 14, rw = l6 / 3;
      warm(((xcd & 4) ? wu : wg) + (size_t)(rr * 20 + (w & 3) * 5) * 3072 + (xcd & 3) * 768, rw * 3072 + (l6 - rw * 3) * 256, L_WARM);
      return;
    }
    if (WH == 3) {
      const int l6 = lane < 60 ? lane : 59, rw = l6 / 6;
      if ((w & 3) < 2) warm(((xcd & 4) ? wu : wg) + (size_t)(rr * 20 + (w & 1) * 10) * 3072 + (xcd & 3) * 768, rw * 3072 + (l6 - rw * 6) * 128, L_WARM);
      return;
    }
    const int l6 = lane < 60 ? lane : 59, rw = l6 / 6;
    warm(((w & 1) ? wu : wg) + (size_t)(rr * 20 + ((w >> 1) & 1) * 10) * 3072 + (xcd & 3) * 768, rw * 3072 + (l6 - rw * 6) * 128, L_WARM);
  };
  u32 kvsrc[8], kvdst[8], fl_get;
  auto setup = [&]() INL {
#pragma unroll
    for (int i = 0; i < 8; ++i) {
      const int id = w + 8 * i, kv = id >= 30, r = id - 30 * kv, c = r / 6, r2 = r - 6 * c;
      int pp = pa - 2 + (r2 >> 1); pp = pp < 0 ? 0 : pp;
      kvsrc[i] = WS_QKV + (pp * 240 + 80 * (1 + kv) + 5 * ja + c) * 2048 + (r2 & 1) * 1024;
      kvdst[i] = (kv ? L_V + c * VCH : L_K + c * KCH) + r2 * 1024;
      asm volatile("" : "+s"(kvsrc[i]), "+s"(kvdst[i]));
    }
    const int e = r16 < 14 ? r16 : 0, sec = e < 2 ? 0 : (e < 8 ? 1 : 2), i = e < 2 ? e : (e - 2) % 6;
    int pp = e < 2 ? pa : pa - (i >> 1); pp = pp < 0 ? 0 : pp;
    fl_get = (!WDEC && (g & 1)) ? flag_off(0, r16, 15) : flag_off(0, pp, (80 * sec + 5 * ja + 4 * (i & 1)) >> 4);
    asm volatile("" : "+v"(fl_get));
  };

  auto ld_xt = [&]() INL {
    const int rw = lane / 10;
    if (XLAT == 10) {
      const char* s4 = xa + (64 * half + 16 * (w & 3)) * 1280 + slice * 160;
      if (w >= 4 && lane < 40) {
#pragma unroll
        for (int i = 0; i < 4; ++i) dma(s4 + i * 5120, rw * 1280 + (lane - 10 * rw) * 16, L_XT + (w & 3) * 2560 + i * 640);
      }
      return;
    }
    const char* src = xa + (64 * half + 8 * w) * 1280 + slice * 160;
    if (lane < 40) {
      dma(src, rw * 1280 + (lane - 10 * rw) * 16, L_XT + w * 1280);
      dma(src + 4 * 1280, rw * 1280 + (lane - 10 * rw) * 16, L_XT + w * 1280 + 640);
    }
    SB();
  };
  if (__builtin_expect(j < 15, 1)) {
    const int wm = w & 1, wn = w >> 1;
    u32 voffA[2], voffB[2];
#pragma unroll
    for (int i = 0; i < 2; ++i) {
      const int ra = 16 * w + 8 * i + (lane >> 3);
      voffA[i] = ra * 1280 + (((lane & 7) ^ ((ra >> 1) & 7)) << 4);
      const int rb = 8 * w + 4 * i + g, f = (rb & 3) | (((rb >> 3) & 1) << 2);
      voffB[i] = rb * 3840 + (((((r16 >> 1) ^ f) << 1) | (r16 & 1)) << 4);
    }
    const char* wb = wqkv + j * 256;
    const int rot = RG == 10 ? ((rr & 7) + ((rr & 7) >> 2) + ((5 * (rr >> 3)) >> 1)) % 10 : ((rr & 7) + (rr >> 3)) % RG;
    auto stage = [&](int c, int piece) {
      int kc = c % RG + rot; kc = (kc >= RG ? kc - RG : kc) + (c / RG) * RG;
      const u32 slot = (c & 3) * 32768;
      const char* a = xa + kc * 128; const char* b = wb + (size_t)kc * 64 * 3840;
      if (piece < 0 || piece == 0) dma(a, voffA[0], slot + w * 2048);
      if (piece < 0 || piece == 1) dma(a, voffA[1], slot + w * 2048 + 1024);
      if (piece < 0 || piece == 2) dma(b, voffB[0], slot + 16384 + w * 2048);
      if (piece < 0 || piece == 3) dma(b, voffB[1], slot + 16384 + w * 2048 + 1024);
    };
    STAMP(22);
    stage(0, -1);
    STAMP(23);
    u32x2 epoch;
    load_epoch(epoch);
    {
      u64 pc; asm volatile("s_getpc_b64 %0" : "=s"(pc));
      if (w == 0) warm((const char*)(pc & ~127ull) + rr * 1280, (lane < 10 ? lane : 9) * 128, L_WARM);
    }
    if (S1POS == 0 || S1POS == 4) stage(1, -1);
    if (S1POS == 1) { stage(1, 0); stage(1, 2); }
    if (S1POS == 3) { stage(1, -1); stage(2, 0); stage(2, 1); }
    SB();
    setup();
    STAMP(24);
    if (S1POS == 2) stage(1, -1);
    if (S1POS == 1) { stage(1, 1); stage(1, 3); }
    if (S1POS == 4) { stage(2, 0); stage(2, 1); }
    STAMP(25);
    u32 adA[2], adB[2];
#pragma unroll
    for (int ks = 0; ks < 2; ++ks) adA[ks] = (64 * wm + r16) * 128 + (((4 * ks + g) ^ ((r16 >> 1) & 7)) << 4);
#pragma unroll
    for (int n = 0; n < 2; ++n) adB[n] = 16384 + (8 * g + q4) * 256 + (((2 * wn + (n ^ wm)) ^ (q4 | ((g & 1) << 2))) << 5) + pq * 8;
    f32x4 acc[4][2], csum = {0, 0, 0, 0};
#pragma unroll
    for (int m = 0; m < 4; ++m) { acc[m][0] = f32x4{0, 0, 0, 0}; acc[m][1] = f32x4{0, 0, 0, 0}; }
    u32x4 fa[2][4]; u32x2 fb[2][2][2];
    u32x4 rsg[4];
    auto ld_rs = [&]() {
#pragma unroll
      for (int m = 0; m < 4; ++m) gld_sc1(rsg[m], ws + WS_RS + (p * 128 + 64 * wm + 16 * m) * 16, r16 * 16);
    };
    auto rdp = [&](int c, int ks, int i) {
      const u32 slot = (c & 3) * 32768;
      if (i == 0 || i == 1) fb[ks][0][i] = ldstr(slot + adB[0] + ks * 8192 + i * 1024);
      else if (i == 2) fa[ks][0] = lds128(slot + adA[ks]);
      else if (i == 3 || i == 4) fb[ks][1][i - 3] = ldstr(slot + adB[1] + ks * 8192 + (i - 3) * 1024);
      else if (i < 8) fa[ks][i - 4] = lds128(slot + adA[ks] + (i - 4) * 2048);
    };
    auto mm = [&](int ks, int cr, int cn) {
      const u32x4 b0 = cat(fb[ks][0][0], fb[ks][0][1]), b1 = cat(fb[ks][1][0], fb[ks][1][1]);
#pragma unroll
      for (int i = 0; i < 9; ++i) {
        if (i > 0) acc[(i - 1) >> 1][(i - 1) & 1] = mfma(((i - 1) & 1) ? b1 : b0, fa[ks][(i - 1) >> 1], acc[(i - 1) >> 1][(i - 1) & 1]);
        else csum = mfma(b0, ones8, csum);
        if (cr == 10 && i == 3) {
          if (r16 == 0) *(LAS f32x4*)(smem + L_MISC + 1024 + (32 * wn + 16 * wm + 4 * g) * 4) = csum;
          asm volatile("" ::: "memory");
          if (lane == 0) *(volatile LAS u32*)(smem + L_MISC + w * 4) = 1;
        }
        if (cr < 10) rdp(cr, ks ^ 1, i);
        if (cn < 10 && i == 4) stage(cn, 2 * (ks ^ 1));
        if (cn < 10 && i == 8) stage(cn, 2 * (ks ^ 1) + 1);
        SB();
      }
    };
    auto ld_wd = [&]() INL {
      if (j < 12 && w < 5) {
        dma(wd + (p * 12 + j) * 10240 + w * 256, (lane >> 3) * 1280 + (lane & 7) * 16, L_RT + w * 2048);
        dma(wd + (p * 12 + j) * 10240 + w * 256 + 128, (lane >> 3) * 1280 + (lane & 7) * 16, L_RT + w * 2048 + 1024);
      }
      SB();
    };
    if (tid < 8) *(LAS u32*)(smem + L_MISC + tid * 4) = 0;
    if (S1POS >= 3) vm_wait<6>(); else vm_wait<4>();
    make_tag(epoch);
    bar();
    STAMP(16);
#pragma unroll
    for (int i = 0; i < 8; ++i) rdp(0, 0, i);
    SB();
    if (WDAT == -1) ld_wd();
    if (S1POS < 3) { stage(2, 0); stage(2, 1); }
    SB();
#pragma unroll
    for (int c = 0; c < 10; ++c) {
      mm(0, c, c + 2);
      if (c < 9) {
        {
          constexpr int base[9] = {4, 4, 4, 4, 4, 4, 4, 4, 0};
          const int n = base[c] + (c == RSAT + 1 ? 4 : 0);
          if (XL && c == XLAT + 1) { if (n == 0) vm_wait<2>(); else vm_wait<6>(); } else
          if (WGAT < 9 && c >= WGAT + 1 && c <= WGAT + 1 + 3 * WGSP && w >= 4 && c == WGAT + 1 + (w - 4) * WGSP) { if (n == 0) vm_wait<1>(); else vm_wait<5>(); }
          else if (c == WDAT + 1 && j < 12 && w < 5) { if (n == 0) vm_wait<2>(); else if (n == 4) vm_wait<6>(); else vm_wait<10>(); }
          else { if (n == 0) vm_wait<0>(); else if (n == 4) vm_wait<4>(); else vm_wait<8>(); }
        }
        bar0();
        if (c == RSAT) {
          ld_rs(); SB();
        }
        if (c == WDAT) ld_wd();
        if (XL && c == XLAT) ld_xt();
        if (WGAT < 9 && c >= WGAT && c <= WGAT + 3 * WGSP && w >= 4 && c == WGAT + (w - 4) * WGSP) { warm_gu(); SB(); }
      }
      mm(1, c + 1, c + 3);
    }
    STAMP(1);
    if (XL && XLAT == 8) vm_wait<2>(); else vm_wait<0>();
    for (u32 spins = 0; spins < SPIN_CAP; ++spins) {
      asm volatile("" : "+v"(rsg[0]), "+v"(rsg[1]), "+v"(rsg[2]), "+v"(rsg[3]));
      bool ok = true;
#pragma unroll
      for (int m = 0; m < 4; ++m) ok = ok && rsg[m][1] == (u32)tag && rsg[m][3] == (u32)(tag >> 32);
      if (__all(ok)) break;
      ld_rs();
      vm_wait<0>();
    }
    asm volatile("" : "+v"(rsg[0]), "+v"(rsg[1]), "+v"(rsg[2]), "+v"(rsg[3]));
    STAMP(27);
    while (*(volatile LAS u32*)(smem + L_MISC + (w ^ 1) * 4) == 0) {}
    asm volatile("" ::: "memory");
    STAMP(17);
    const float qs = j < 5 ? 0.22360679774997896f * 1.4426950408889634f : 1.f;
    f32x4 cs[2];
#pragma unroll
    for (int n = 0; n < 2; ++n) cs[n] = n == 0 ? csum : *(LAS const f32x4*)(smem + L_MISC + 1024 + (32 * wn + 16 * (1 ^ wm) + 4 * g) * 4);
    const u32 qb = WS_QKV + (p * 240 + 16 * j + 4 * wn + 2 * ((g & 1) ^ wm) + (g >> 1)) * 2048 + (64 * wm + r16) * 16;
#pragma unroll
    for (int m = 0; m < 4; ++m) {
      const u32 um = rsg[m][0], ur = rsg[m][2];
      const float mean = __uint_as_float(um), sc = __uint_as_float(ur) * qs;
      u32x2 pk[2];
#pragma unroll
      for (int n = 0; n < 2; ++n) {
        const f32x4 v = (acc[m][n] - mean * cs[n]) * sc;
        pk[n] = u32x2{pack2(v[0], v[1]), pack2(v[2], v[3])};
      }
      st128_sc1(rs, qb + m * 256, quad2chunk(pk[0], pk[1]));
    }
    asm volatile("" ::: "memory");
    if (XL && XLAT >= 9) ld_xt();
  } else {
    u32x2 epoch;
    load_epoch(epoch);
    auto ld_wo = [&]() INL {
#pragma unroll
      for (int i = 0; i < 7; ++i) { const int id = w + 8 * i < 50 ? w + 8 * i : 49; dma(wo + p * 51200 + id * 1024, lane * 16, id * 1024); }
    };
    if (!XFIRST) ld_wo();
    u32x4 xv[2][10];
#pragma unroll
    for (int t = 0; t < 10; ++t)
#pragma unroll
      for (int s2 = 0; s2 < 2; ++s2) gld(xv[s2][t], xa + (16 * w + 8 * s2) * 1280 + ((t + XROT) % 10) * 128, (lane >> 3) * 1280 + (lane & 7) * 16);
    if (XFIRST == 1) ld_wo();
    float s1[2] = {0.f, 0.f}, sq[2] = {0.f, 0.f};
#pragma unroll
    for (int t = 0; t < 10; ++t) {
      if (t == 0) vm_wait<10 + 7 * (XFIRST == 1)>();
      if (t == 5) vm_wait<4 + 7 * (XFIRST == 1)>();
      if (t == 8) vm_wait<7 * (XFIRST == 1)>();
#pragma unroll
      for (int s2 = 0; s2 < 2; ++s2) { asm volatile("" : "+v"(xv[s2][t])); s1[s2] = sum8(xv[s2][t], s1[s2]); sq[s2] = sq8(xv[s2][t], sq[s2]); }
    }
    make_tag(epoch);
#pragma unroll
    for (int o = 1; o < 8; o <<= 1) { s1[0] += __shfl_xor(s1[0], o); sq[0] += __shfl_xor(sq[0], o); s1[1] += __shfl_xor(s1[1], o); sq[1] += __shfl_xor(sq[1], o); }
    {
      const float a1 = (lane & 1) ? s1[1] : s1[0], a2 = (lane & 1) ? sq[1] : sq[0];
      const float mean = a1 * (1.f / 640.f), var = __builtin_fmaxf(a2 * (1.f / 640.f) - mean * mean, 0.f);
      if ((lane & 7) < 2)
        st128_sc1(rs, WS_RS + (p * 128 + 16 * w + 8 * (lane & 1) + (lane >> 3)) * 16, u32x4{__float_as_uint(mean), (u32)tag, __float_as_uint(__builtin_amdgcn_rsqf(var + LN_EPS)), (u32)(tag >> 32)});
    }
    STAMP(16);
    asm volatile("" ::: "memory");
    if (XFIRST == 2) ld_wo();
    if (XL) ld_xt();
    setup();
    if (XFIRST == 2) vm_wait<0>(); else vm_wait<1>();
    bar();
    {
#pragma unroll
      for (int i = 0; i < 7; ++i) {
        const int id = w + 8 * i, gr = id / 10;
        if (id < 50) {
          const u32 l = gr * 10240 + q4 * 1280 + (id - 10 * gr) * 128 + g * 32 + pq * 8;
          st128_sc1(rs, rt_dst(id - 10 * gr, (p * 5 + gr) >> 2, (p * 5 + gr) & 3), ldstr2(l, l + 5120));
        }
      }
      if (w >= 3) {
        float a1 = 0.f;
#pragma unroll
        for (int t = 0; t < 10; ++t) a1 = sum8(lds128((w - 3) * 10240 + (lane >> 3) * 1280 + t * 128 + (lane & 7) * 16), a1);
#pragma unroll
        for (int o = 1; o < 8; o <<= 1) a1 += __shfl_xor(a1, o);
        a1 *= (1.f / 640.f);
        const u32 hi = pack2(a1, 0.f), lo = pack2(a1 - bflo(hi), 0.f);
        if ((lane & 7) < 2) __builtin_amdgcn_raw_buffer_store_b16((u16)((lane & 1) ? lo : hi), rs, (int)(WS_WB + (lane & 1) * 1280 + ((p * 5 + w - 3) * 8 + (lane >> 3)) * 2), 0, 16);
      }
    }
  }
  STAMP(18);
  u32 sink = 0;
  if (SW) {
    asm volatile("s_waitcnt lgkmcnt(0)" ::: "memory");
    swarm15(sink, ((xcd & 4) ? wu : wg) + (size_t)(rr * 20 + (w >> 1) * 5) * 3072 + (xcd & 3) * 768 + (w & 1) * 384);
  }
  if (XL && XLAT == 9 && j < 15) vm_wait<2>(); else if (XL && XLAT == 10 && j < 15 && w >= 4) vm_wait<4>(); else vm_wait<0>();
  if (SW) bar0(); else { bar(); STAMP(2); }
  if (w == FW && lane < (j == 15 ? 2 : 1)) __hip_atomic_store((GAS u64*)(ws + (lane == 0 ? flag_off(0, p, j) : WS_WOF + p * 8)), tag, RLX_AGENT);
  asm volatile("" ::: "memory");
  if (w != PW && w != 7) {
    const int kr = (w < PW ? w : w - 1) * 64 + lane;
    const bool valid = 128 * (pa - 2) + kr >= 0;
    *(LAS u32x4*)(smem + L_K + 5 * KCH + kr * 16) = u32x4{valid ? 0u : 0xf149u, 0u, ONES2, ONES2};
  }
  if (w == PW) poll_flags(ws, fl_get, tag);
  if (SW) bar0(); else { bar(); STAMP(3); }

  u32x2 xr[6];
  u32 voffG[3];
  {
    const int h = w & 1, qt = w >> 1, l32 = lane & 31, hh = lane >> 5;
    u32x4 qf[2];
    gld_sc1(qf[0], ws + WS_QKV + (pa * 240 + 5 * ja + 2 * h) * 2048 + qt * 512, hh * 2048 + l32 * 16);
    gld_sc1(qf[1], ws + WS_QKV + (pa * 240 + 5 * ja + 2 * h + 2) * 2048 + qt * 512, l32 * 16);
#pragma unroll
    for (int i = 0; i < 5; ++i) dma_sc1(ws + kvsrc[i], lane * 16, kvdst[i]);
    u32 dm[8];
#pragma unroll
    for (int m = 0; m < 8; ++m) {
      const int k0 = ((2 * m) & 3) + 8 * (m >> 1) + 4 * hh;
      dm[m] = (k0 <= l32 ? 0xffffu : 0u) | (k0 + 1 <= l32 ? 0xffff0000u : 0u);
      asm volatile("" : "+v"(dm[m]));
    }
    u32 adK0 = L_K + (2 * h + hh) * KCH + (32 * qt + l32) * 16, adK1 = L_K + (hh ? 5 : 2 * h + 2) * KCH + (32 * qt + l32) * 16;
    const u32 vrow = 32 * qt + 4 * hh + q4;
    u32 adV = (g & 1) == 0 ? L_V + (3 * h + (pq >> 1)) * VCH + vrow * 16 + (pq & 1) * 8
                           : (pq == 0 ? L_V + 2 * VCH + vrow * 16 + 8 * h : L_K + 5 * KCH + ((4 * hh + (h ? 4 : 12)) & 15) * 16 + 8);
#pragma unroll
    for (int i = 0; i < 3; ++i) {
      const int s = 64 * i + lane, rw = s / 12, cp = s - rw * 12;
      int c = cp - (w & 1) - 2 * ((rw >> 3) & 1); c = c < 0 ? c + 12 : c;
      voffG[i] = rw * 3072 + c * 16;
    }
    asm volatile("" : "+v"(adK0), "+v"(adK1), "+v"(adV), "+v"(voffG[0]), "+v"(voffG[1]), "+v"(voffG[2]));
    vm_wait<1>();
    asm volatile("" : "+v"(qf[0]), "+v"(qf[1]));
    bar();
    asm volatile("" : "+s"(sink));
    STAMP(4);
    const f32x16 zero16 = {0, 0, 0, 0, 0, 0, 0, 0, 0, 0, 0, 0, 0, 0, 0, 0};
    u32x4 kf[9][2];
    constexpr int ord[9] = {0, 8, 1, 2, 3, 4, 5, 6, 7};
#pragma unroll
    for (int n = 0; n < 4; ++n) { kf[ord[n]][0] = lds128(adK0 + 512 * ord[n]); kf[ord[n]][1] = lds128(adK1 + 512 * ord[n]); }
    SB();
    dma_sc1(ws + kvsrc[5], lane * 16, kvdst[5]);
    if (h == 1 && hh == 0) { qf[0][0] = 0; qf[0][1] = 0; }
    if (h == 0) { qf[1][2] = 0; qf[1][3] = 0; }
    if (hh == 1) qf[1] = u32x4{0x3f80u, 0, 0, 0};
    SB();
    f32x16 sc[9];
    float m4[4];
    auto qk = [&](int i, int st) { sc[i] = mfma32(kf[i][st], qf[st], st == 0 ? zero16 : sc[i]); };
    qk(0, 0); qk(8, 0);
    SB();
    dma_sc1(ws + kvsrc[6], lane * 16, kvdst[6]);
    SB();
    qk(0, 1); qk(8, 1);
    SB();
    u32x2 wof;
    if (w < 4) {
      dma_sc1(ws + kvsrc[7], lane * 16, kvdst[7]);
      asm volatile("global_load_dwordx2 %0, %1, %2 sc1" : "=v"(wof) : "v"(r16 * 8), "s"(ws + WS_WOF));
    } else asm volatile("v_mov_b64 %0, 0" : "=v"(wof));
    SB();
#pragma unroll
    for (int n = 2; n < 11; ++n) {
      if (n < 9) { qk(ord[n], 0); SB(); }
      if (n + 2 < 9) { kf[ord[n + 2]][0] = lds128(adK0 + 512 * ord[n + 2]); SB(); }
      if (n == 3) {
#pragma unroll
        for (int r = 6; r < 16; ++r) sc[0][r] = ((r & 3) + 8 * (r >> 2) + 4 * hh <= l32) ? sc[8][r] : sc[0][r];
      } else if (n > 3) {
#pragma unroll
        for (int c = 0; c < 4; ++c) m4[c] = max3(m4[c], sc[n - 3][2 * c], sc[n - 3][2 * c + 1]);
      }
      SB();
      if (n < 9) { qk(ord[n], 1); SB(); }
      if (n + 2 < 9) { kf[ord[n + 2]][1] = lds128(adK1 + 512 * ord[n + 2]); SB(); }
      if (n == 2) {
#pragma unroll
        for (int r = 0; r < 6; ++r) sc[0][r] = ((r & 3) + 8 * (r >> 2) + 4 * hh <= l32) ? sc[8][r] : sc[0][r];
      } else if (n == 3) {
#pragma unroll
        for (int c = 0; c < 4; ++c) m4[c] = max3(max3(sc[0][4 * c], sc[0][4 * c + 1], sc[0][4 * c + 2]), sc[0][4 * c + 3], sc[0][4 * c + 3]);
      } else {
#pragma unroll
        for (int c = 0; c < 4; ++c) m4[c] = max3(m4[c], sc[n - 3][8 + 2 * c], sc[n - 3][9 + 2 * c]);
      }
      SB();
    }
    float mx = __builtin_fmaxf(max3(m4[0], m4[1], m4[2]), m4[3]);
    { auto sw = __builtin_amdgcn_permlane32_swap(__float_as_uint(mx), __float_as_uint(mx), false, false); mx = __builtin_fmaxf(__uint_as_float(sw[0]), __uint_as_float(sw[1])); }
    u32x4 nmf;
    {
      const float nmx = -mx;
      const u32 p0 = __float_as_uint(nmx) & 0xffff0000u;
      const float r1 = nmx - __uint_as_float(p0);
      const u32 p1 = __float_as_uint(r1) & 0xffff0000u;
      const u32 p2 = __float_as_uint(r1 - __uint_as_float(p1));
      nmf = u32x4{hh ? p2 >> 16 : (p0 >> 16) | p1, 0, 0, 0};
    }
    auto submax = [&](int i) { sc[i] = mfma32(ones8, nmf, sc[i]); };
    submax(0); submax(1);
    SB();
    u32x4 rtv[3];
    if (w < 4) {
#pragma unroll
      for (int i = 0; i < 3; ++i) { const u32 l = L_RT + (w + 4 * i) * 1024 + q4 * 128 + g * 32 + pq * 8; rtv[i] = ldstr2(l, l + 512); }
    }
    STAMP(29);
    vm_wait<0>();
    bar();
    STAMP(26);
    if (w < 4) {
      if (WPOS == 2) warm_gu();
      if (j < 12) {
        const int gi = p * 12 + j;
#pragma unroll
        for (int i = 0; i < 3; ++i)
          if (w + 4 * i < 10) st128_sc1(rs, rt_dst(w + 4 * i, 20 + (gi >> 2), gi & 3), rtv[i]);
      }
      asm volatile("" : "+v"(wof));
      if (WDEC && !__all(wof[0] == (u32)tag && wof[1] == (u32)(tag >> 32))) poll_flags(ws, WS_WOF + r16 * 8, tag);
#pragma unroll
      for (int i = 0; i < 20 - 2 * NP; ++i) {
        const u32 o = 20480 + (8 * NP + w * (20 - 2 * NP) + i) * 1024;
        dma_sc1(wt + o, lane * 16, L_WOB + o);
      }
#pragma unroll
      for (int t = 0; t < 5; ++t) if (!XL) gld64(xr[t], xa + (64 * half + 16 * w) * 1280 + slice * 160 + t * 32, r16 * 1280 + g * 8);
      asm volatile("global_load_dwordx2 %0, %1, %2 sc1" : "=v"(xr[5]) : "v"(r16 * 16), "s"(ws + WS_RS + (p * 128 + 64 * half + 16 * w) * 16));
      if (!SW && WPOS == 1 && (WGAT > 9 || j == 15)) warm_gu();
    } else {
#pragma unroll
      for (int t = XL ? 5 : 0; t < 6; ++t) asm volatile("v_mov_b64 %0, 0" : "=v"(xr[t]));
    }
    SB();
    f32x16 o = zero16;
    u32 pw[2][8];
    u32x2 vf[2][4], vx[4];
    auto rdV = [&](int i, int e) { return ldstr(adV + 512 * i + 128 * e); };
    auto pfrag = [&](const u32* q, int st) { return u32x4{q[4 * st], q[4 * st + 1], q[4 * st + 2], q[4 * st + 3]}; };
    u32 p8[8];
#pragma unroll
    for (int n = 0; n < 8; ++n) {
      float ex[16];
#pragma unroll
      for (int e = 0; e < 8; ++e) {
        ex[2 * e] = __builtin_amdgcn_exp2f(sc[n][2 * e]); SB();
        if (e < 4) { vf[n & 1][e] = rdV(n, e); SB(); }
        else if (n == 0) { vx[e - 4] = rdV(8, e - 4); SB(); }
        ex[2 * e + 1] = __builtin_amdgcn_exp2f(sc[n][2 * e + 1]); SB();
        if (e > 0) { pw[n & 1][e - 1] = pack2(ex[2 * e - 2], ex[2 * e - 1]); SB(); }
        if (n >= 1) {
          if (e == 0 && n < 7) { submax(n + 1); SB(); }
          if (e == 3) { o = mfma32(cat(vf[(n - 1) & 1][0], vf[(n - 1) & 1][1]), pfrag(pw[(n - 1) & 1], 0), o); SB(); }
          if (e == 6) { o = mfma32(cat(vf[(n - 1) & 1][2], vf[(n - 1) & 1][3]), pfrag(pw[(n - 1) & 1], 1), o); SB(); }
          if (n == 2 && e == 2) { o = mfma32(cat(vx[0], vx[1]), pfrag(p8, 0), o); SB(); }
          if (n == 2 && e == 5) { o = mfma32(cat(vx[2], vx[3]), pfrag(p8, 1), o); SB(); }
        }
      }
      pw[n & 1][7] = pack2(ex[14], ex[15]);
      if (n == 0) {
#pragma unroll
        for (int m = 0; m < 8; ++m) { p8[m] = pw[0][m] & dm[m]; pw[0][m] &= ~dm[m]; }
      }
      SB();
    }
    o = mfma32(cat(vf[1][0], vf[1][1]), pfrag(pw[1], 0), o);
    o = mfma32(cat(vf[1][2], vf[1][3]), pfrag(pw[1], 1), o);
    const float inv = __builtin_amdgcn_rcpf(o[12]);
    const u32 x0 = pack2(o[0] * inv, o[1] * inv), x1 = pack2(o[2] * inv, o[3] * inv), y0 = pack2(o[4] * inv, o[5] * inv), y1 = pack2(o[6] * inv, o[7] * inv);
    auto s0 = __builtin_amdgcn_permlane32_swap(x0, y0, false, false);
    auto s1 = __builtin_amdgcn_permlane32_swap(x1, y1, false, false);
    const u32 ob = WS_O + (pa * 80 + 5 * ja) * 2048 + (32 * qt + l32) * 16;
    st128_sc1(rs, ob + (3 * h + hh) * 2048, u32x4{s0[0], s1[0], s0[1], s1[1]});
    if (hh == 0) st64_sc1(rs, ob + 2 * 2048 + 8 * h, u32x2{pack2(o[8] * inv, o[9] * inv), pack2(o[10] * inv, o[11] * inv)});
    STAMP(5);
    if (XL) {
#pragma unroll
      for (int t = 0; t < 5; ++t) xr[t] = w < 4 ? *(LAS const u32x2*)(smem + L_XT + (16 * w + r16) * 160 + t * 32 + g * 8) : u32x2{0, 0};
    }
  }
  const char* gsrc = ((w & 1) ? wu : wg) + jg * 192;
  auto stageGBp = [&](int c, int rb, int i) {
    dma(gsrc + (size_t)(c * 64 + rb * 16) * 3072, voffG[i], gb_off(c) + (w & 1) * 12288 + rb * 3072 + i * 1024);
  };
  auto stageGB = [&](int c, int rb) { stageGBp(c, rb, 0); stageGBp(c, rb, 1); stageGBp(c, rb, 2); };
  const int wm4 = w & 3, wk = w >> 2, kh = KSWAP ? wk ^ (slice & 1) : wk;
  u32 voffR = g * 2048 + (64 * half + 16 * wm4 + r16) * 16;
  fl_get = flag_off(1, p, r16);
  u32 fl_get2 = flag_off(2, pg, r16), fl_put = lane == 0 ? flag_off(1, pa, ja) : WS_DENSE + bx * 8;
  asm volatile("" : "+v"(fl_put));
  asm volatile("" : "+v"(voffR), "+v"(fl_get), "+v"(fl_get2));
  asm volatile("" ::: "memory");
  bar();
  if (w < 4) {
#pragma unroll
    for (int i = 0; i < 5; ++i) dma_sc1(wt + 5120 * w + i * 1024, lane * 16, L_WOB + 5120 * w + i * 1024);
    dma_sc1(ws + WS_WB + (w < 3 ? w : 2) * 1024, lane * 16, L_WB + (w < 3 ? w : 2) * 1024);
    vm_wait<6>();
  } else {
    if (X_A) vm_wait<0>(); else {
    stageGB(2, w >> 1); stageGB(2, (w >> 1) - 2);
    vm_wait<6>(); }
  }
  bar0();
  STAMP(6);
  u32x2 hp[5];
  {
    if (w == FW && lane < 2) __hip_atomic_store((GAS u64*)(ws + fl_put), tag, RLX_AGENT);
    u32x4 a[10], b[6][5];
    const u32 ad0 = L_WOB + kh * 10 * 5120 + lane * 16, adm = L_WB + (r16 & 1) * 1280 + kh * 640 + g * 16;
    const bool alt = KROT && (slice & 2);
    auto kso = [&](int s) { return alt ? (s < 6 ? 4 + (s + 3) % 6 : (s - 4) & 3) : (s + 4) % 10; };
    auto rdB = [&](int s, int t) { b[s % 6][t] = lds128(ad0 + kso(s) * 5120 + t * 1024); };
#pragma unroll
    for (int s = 0; s < 6; ++s)
#pragma unroll
      for (int t = 0; t < 5; ++t) rdB(s, t);
    asm volatile("" : "+v"(xr[0]), "+v"(xr[1]), "+v"(xr[2]), "+v"(xr[3]), "+v"(xr[4]), "+v"(xr[5]));
    f32x4 hacc[5], macc;
    { const u32 xm = xr[5][0]; macc = f32x4{__uint_as_float(xm), 0.f, 0.f, 0.f}; }
#pragma unroll
    for (int t = 0; t < 5; ++t) { const u32 a0 = xr[t][0], a1 = xr[t][1]; hacc[t] = f32x4{bflo(a0), bfhi(a0), bflo(a1), bfhi(a1)}; }
    SB();
    if (w == PW) poll_flags(ws, fl_get, tag); else if (w < 4) vm_wait<0>();
    bar();
    STAMP(7);
    const char* osrc = ws + WS_O + (p * 80 + 40 * kh) * 2048;
#pragma unroll
    for (int s = 0; s < 10; ++s) gld_sc1(a[s], osrc + kso(s) * 8192, voffR);
    u32x4 bm[2];
    bm[0] = lds128(adm + kso(0) * 64);
    SB();
#pragma unroll
    for (int s = 0; s < 10; ++s) {
      switch (s) {
        case 0: vm_wait_use<9>(a[0]); break; case 1: vm_wait_use<8>(a[1]); break; case 2: vm_wait_use<7>(a[2]); break;
        case 3: vm_wait_use<6>(a[3]); break; case 4: vm_wait_use<5>(a[4]); break; case 5: vm_wait_use<4>(a[5]); break;
        case 6: vm_wait_use<3>(a[6]); break; case 7: vm_wait_use<2>(a[7]); break; case 8: vm_wait_use<1>(a[8]); break;
        default: vm_wait_use<0>(a[9]); break;
      }
      if (s == 0) STAMP(28);
#pragma unroll
      for (int t = 0; t < 5; ++t) {
        hacc[t] = mfma(b[s % 6][t], a[s], hacc[t]);
        SB();
        if (s + 6 < 10) { rdB(s + 6, t); SB(); }
        if (t == 0 && s < 9) { bm[(s + 1) & 1] = lds128(adm + kso(s + 1) * 64); SB(); }
      }
      macc = mfma(bm[s & 1], a[s], macc);
      SB();
    }
    STAMP(8);
    if (wk == 0) {
#pragma unroll
      for (int t = 0; t < 5; ++t) *(LAS f32x4*)(smem + L_RED + (wm4 * 5 + t) * 1024 + lane * 16) = hacc[t];
      *(LAS float*)(smem + L_RED + 20480 + wm4 * 256 + lane * 4) = macc[0] + macc[1];
    }
    bar();
    if (wk == 1) {
      float s1 = 0.f, s2 = 0.f;
      const float mean = macc[0] + macc[1] + *(LAS const float*)(smem + L_RED + 20480 + wm4 * 256 + lane * 4);
      u32x2 hc[5];
#pragma unroll
      for (int t = 0; t < 5; ++t) {
        const f32x4 v = hacc[t] + *(LAS const f32x4*)(smem + L_RED + (wm4 * 5 + t) * 1024 + lane * 16);
        const u32 lo = pack2(v[0], v[1]), hi = pack2(v[2], v[3]);
        hp[t] = u32x2{lo, hi};
        hc[t] = u32x2{pack2(bflo(lo) - mean, bfhi(lo) - mean), pack2(bflo(hi) - mean, bfhi(hi) - mean)};
      }
      const u32 hb = WS_H + (p * 80 + 10 * slice + 2 * (g & 1) + (g >> 1)) * 2048 + (64 * half + 16 * wm4 + r16) * 16;
      st128_sc1(rs, hb, quad2chunk(hc[0], hc[1]));
      st128_sc1(rs, hb + 4 * 2048, quad2chunk(hc[2], hc[3]));
      const u32x4 last = quad2chunk(hc[4], hc[4]);
      if ((g & 1) == 0) st128_sc1(rs, hb + 8 * 2048, last);
#pragma unroll
      for (int t = 0; t < 5; ++t) { const u32 lo = hc[t][0], hi = hc[t][1]; s1 = dot2(hi, ONES2, dot2(lo, ONES2, s1)); s2 = dot2(hi, hi, dot2(lo, lo, s2)); }
      s1 = gsum(s1); s2 = gsum(s2);
      if (g == 0) st64_sc1(rs, WS_ST + (p * 8 + slice) * 1024 + (64 * half + 16 * wm4 + r16) * 8, u32x2{__float_as_uint(s1), __float_as_uint(s2)});
      if (W3BAR) bar();
      vm_wait<0>();
    } else {
#pragma unroll
      for (int t = 0; t < 5; ++t) hp[t] = u32x2{0, 0};
      if (W3BAR) bar();
      if (!X_B) { stageGB(0, w >> 1); stageGB(0, 2 + (w >> 1)); stageGB(1, w >> 1); stageGB(1, 2 + (w >> 1)); }
    }
  }
  bar();
  STAMP(9);
  if (w == FW && lane == 0) __hip_atomic_store((GAS u64*)(ws + flag_off(2, p, j)), tag, RLX_AGENT);
  if (w == PW) {
    poll_flags(ws, fl_get2, tag);
  } else if (w == 2) {
    GAS u64* a = (GAS u64*)(ws + WS_DENSE + lane * 8);
    for (u32 spins = 0; spins < SPIN_CAP; ++spins) {
      const u64 v0 = __hip_atomic_load(a, RLX_AGENT), v1 = __hip_atomic_load(a + 64, RLX_AGENT), v2 = __hip_atomic_load(a + 128, RLX_AGENT), v3 = __hip_atomic_load(a + 192, RLX_AGENT);
      if (__all(v0 == tag && v1 == tag && v2 == tag && v3 == tag)) break;
    }
    if (bx == 0 && lane == 0) __hip_atomic_store((GAS u64*)(ws + WS_EPOCH), tag, RLX_AGENT);
  } else if (wk == 1) {
    if (!X_C) { stageGB(3, w >> 1); stageGB(3, (w >> 1) - 2); }
  }
  bar();
  STAMP(10);

  const int rotd = DROT == 0 ? 0 : (DMAP ? (((3 * (rr & 3)) >> 1) + (DROT == 2 ? ((6 * (rr >> 2)) >> 3) : 0)) % 6 : (((slice * 6) >> 3) + (DROT == 2 ? ((3 * (rr >> 3)) >> 1) : 0)) % 6);
  auto stageDq = [&](int c, int blk, int i) {
    int kc = c + rotd; kc = kc >= 6 ? kc - 6 : kc;
    dma_sc1(wt + (20 + kc * 8 + blk) * 5120 + i * 1024, lane * 16, (c & 3) * 40960 + blk * 5120 + i * 1024);
  };
  auto stageDp = [&](int c, int i) { stageDq(c, w, i); };
  auto stageD = [&](int c) {
#pragma unroll
    for (int i = 0; i < 5; ++i) stageDp(c, i);
  };

  {
    const int wm = w >> 2, wn = w & 3;
    const char* hsrc = ws + WS_H + (pg * 80 + w) * 2048;
    auto stageGAp = [&](int c, int i) { dma_sc1(hsrc + c * 16384 + i * 1024, lane * 16, ga_off(c) + w * 2048 + i * 1024); };
    auto stageGA = [&](int c) { stageGAp(c, 0); stageGAp(c, 1); };
    stageGA(0);
    dma_sc1(ws + WS_ST + (pg * 8 + w) * 1024, lane * 16, L_ST + w * 1024);
    stageGA(1);
    const u32 adA = g * 2048 + (64 * wm + r16) * 16;
    u32 adB[3];
#pragma unroll
    for (int t = 0; t < 3; ++t) {
      const int mat = t == 0 ? 0 : (t == 1 ? 1 : (pq >> 1)), c = t < 2 ? 3 * wn + (pq >> 1) : 3 * wn + 2;
      int cp = c + mat + 2 * (g & 1); cp = cp >= 12 ? cp - 12 : cp;
      adB[t] = mat * 12288 + (8 * g + q4) * 192 + cp * 16 + (pq & 1) * 8;
    }
    f32x4 acc[4][3];
#pragma unroll
    for (int m = 0; m < 4; ++m)
#pragma unroll
      for (int t = 0; t < 3; ++t) acc[m][t] = f32x4{0, 0, 0, 0};
    u32x4 fa[2][4]; u32x2 fb[2][3][2];
    auto rdp = [&](int c, int s, int i) {
      const u32 sa = ga_off(c) + s * 8192, sb = gb_off(c) + s * 6144;
      if (i < 2) fb[s][0][i] = ldstr(sb + adB[0] + i * 768);
      else if (i == 2) fa[s][0] = lds128(sa + adA);
      else if (i < 7) fb[s][1 + ((i - 3) >> 1)][(i - 3) & 1] = ldstr(sb + adB[1 + ((i - 3) >> 1)] + ((i - 3) & 1) * 768);
      else if (i < 10) fa[s][i - 6] = lds128(sa + adA + (i - 6) * 256);
    };
    auto mm = [&](int s, int c) {
      u32x4 b[3];
#pragma unroll
      for (int t = 0; t < 3; ++t) b[t] = cat(fb[s][t][0], fb[s][t][1]);
      const int cr = s == 0 ? c : c + 1;
#pragma unroll
      for (int i = 0; i < 12; ++i) {
        acc[i / 3][i % 3] = mfma(b[i % 3], fa[s][i / 3], acc[i / 3][i % 3]);
        if (cr < 10) rdp(cr, s ^ 1, i);
        if (s == 1 && (i == 4 || i == 10) && c + 3 < 10) stageGAp(c + 3, i == 10);
        if (s == 0 && i % 4 == 3 && c >= 1 && c + 3 < 10) stageGBp(c + 3, w >> 1, i >> 2);
        if (DWARM && s == 1 && i == 7 && c >= 1 && c <= 4 && (w >> 1) == c - 1)
          warm_sc1(ws + WS_WT + w * WT_SLICE + 102400 + rr * 7680, (lane < 60 ? lane : 59) * 128, L_WARM);
        if (s == 1 && (c == 7 || c == 8) && i % 2 == 1 && i < 10) stageDp(c - 7, i >> 1);
        SB();
      }
    };
    vm_wait<3>();
    bar();
    STAMP(19);
#pragma unroll
    for (int i = 0; i < 10; ++i) rdp(0, 0, i);
    SB();
    stageGA(2);
    SB();
#pragma unroll
    for (int c = 0; c < 10; ++c) {
      if (c == 1) {
        const f32x2 u0 = *(LAS const f32x2*)(smem + L_ST + (2 * g) * 1024 + (16 * w + r16) * 8), u1 = *(LAS const f32x2*)(smem + L_ST + (2 * g + 1) * 1024 + (16 * w + r16) * 8);
        const float s1 = gsum(u0[0] + u1[0]), s2 = gsum(u0[1] + u1[1]);
        const float mean = s1 * (1.f / 640.f), var = __builtin_fmaxf(s2 * (1.f / 640.f) - mean * mean, 0.f);
        if (g == 0) *(LAS float*)(smem + L_MISC + (16 * w + r16) * 4) = __builtin_amdgcn_rcpf(var + LN_EPS);
      }
      mm(0, c);
      if (c < 9) {
        if (c == 0) vm_wait<2>(); else if (c == 1) vm_wait<5>(); else if (c <= 6) vm_wait<8>(); else vm_wait<5>();
        bar();
        if (c == 4) STAMP(20);
        if (c == 7) STAMP(30);
      }
      mm(1, c);
    }
    STAMP(11);
    u32x2 p01[4];
    float r2[4];
#pragma unroll
    for (int m = 0; m < 4; ++m) r2[m] = *(LAS const float*)(smem + L_MISC + (64 * wm + 16 * m + r16) * 4);
    if (wm == 1) { vm_wait<5>(); bar(); }
#pragma unroll
    for (int m = 0; m < 4; ++m) {
      const f32x4 v = acc[m][0] * acc[m][1] * r2[m];
      p01[m] = u32x2{pack2(v[0], v[1]), pack2(v[2], v[3])};
    }
    const u32 hb = WS_HH + (pg * 192 + 12 * jg + 3 * wn) * 2048 + (64 * wm + r16) * 16;
    st128_sc1(rs, hb + (g >> 1) * 2048 + (g & 1) * 256, quad2chunk(p01[0], p01[1]));
    st128_sc1(rs, hb + (g >> 1) * 2048 + (2 + (g & 1)) * 256, quad2chunk(p01[2], p01[3]));
    u32x2 p2[2];
#pragma unroll
    for (int mm2 = 0; mm2 < 2; ++mm2) {
      f32x4 v;
#pragma unroll
      for (int r = 0; r < 4; ++r) {
        auto sw = __builtin_amdgcn_permlane32_swap(__float_as_uint(acc[2 * mm2][2][r]), __float_as_uint(acc[2 * mm2 + 1][2][r]), false, false);
        v[r] = __uint_as_float(sw[0]) * __uint_as_float(sw[1]) * (lane < 32 ? r2[2 * mm2] : r2[2 * mm2 + 1]);
      }
      p2[mm2] = u32x2{pack2(v[0], v[1]), pack2(v[2], v[3])};
    }
    st128_sc1(rs, hb + 2 * 2048 + (2 * (g & 1) + (g >> 1)) * 256, quad2chunk(p2[0], p2[1]));
    STAMP(31);
    if (wm == 0) {
      vm_wait<8>();
      bar();
#pragma unroll
      for (int i = 0; i < 10; ++i) stageDq(2, w + 4 * (i / 5), i % 5);
    }
  }

  {
    u32x4 b[6][5];
    const u32 ad0 = kh * 4 * 5120 + lane * 16;
    auto rdB = [&](int s, int t) { b[s % 6][t] = lds128(ad0 + ((s >> 2) & 3) * 40960 + (s & 3) * 5120 + t * 1024); };
#pragma unroll
    for (int s = 0; s < 4; ++s)
#pragma unroll
      for (int t = 0; t < 5; ++t) rdB(s, t);
    SB();
    if (wk == 0) vm_wait<10>(); else vm_wait<0>();
    bar();
    STAMP(12);
    if (w == FW && lane == 0) __hip_atomic_store((GAS u64*)(ws + flag_off(3, pg, jg)), tag, RLX_AGENT);
    if (wk == 1) {
#pragma unroll
      for (int i = 0; i < 10; ++i) if (!X_D) stageDq(3, w - 4 * (i / 5), i % 5);
    }
#pragma unroll
    for (int s = 4; s < 6; ++s)
#pragma unroll
      for (int t = 0; t < 5; ++t) rdB(s, t);
    f32x4 acc[5];
#pragma unroll
    for (int t = 0; t < 5; ++t) { const u32 a = hp[t][0], b2 = hp[t][1]; acc[t] = f32x4{bflo(a), bfhi(a), bflo(b2), bfhi(b2)}; }
    SB();
    if (w == PW) poll_flags(ws, fl_get + 2 * 65536, tag); else if (wk == 0) vm_wait<0>();
    bar();
    STAMP(13);
    u32x4 a[6][4];
    const char* asrc = ws + WS_HH + (p * 192 + 16 * kh) * 2048;
    auto loadAp = [&](int c, int s) {
      int kc = c + rotd; kc = kc >= 6 ? kc - 6 : kc;
      gld_sc1(a[c][s], asrc + kc * 65536 + s * 8192, voffR);
    };
    auto issue = [&](int n) {
      if (n < 4) loadAp(2, n); else if (n < 9) stageDp(4, n - 4); else if (n < 13) loadAp(3, n - 9); else if (n < 18) stageDp(5, n - 13);
      else if (n < 22) loadAp(4, n - 18); else if (n < 26) loadAp(5, n - 22);
    };
#pragma unroll
    for (int s = 0; s < 4; ++s) loadAp(0, s);
#pragma unroll
    for (int s = 0; s < 4; ++s) loadAp(1, s);
#pragma unroll
    for (int s = 0; s < 24; ++s) {
      if (s == 0) vm_wait<4>();
      if (s == 4) vm_wait<8>();
      if (s == 8) vm_wait<12>();
      if (s == 10) vm_wait<11>();
      if (s == 12) vm_wait<11>();
      if (s == 14) vm_wait<8>();
      if (s == 16) vm_wait<4>();
      if (s == 20) vm_wait<0>();
      if (s % 4 == 0) asm volatile("" : "+v"(a[s >> 2][0]), "+v"(a[s >> 2][1]), "+v"(a[s >> 2][2]), "+v"(a[s >> 2][3]));
      if (s == 6) bar();
      if (s == 10 || s == 14) bar0();
      if (s == 0) STAMP(21);
#pragma unroll
      for (int t = 0; t < 5; ++t) {
        acc[t] = mfma(b[s % 6][t], a[s >> 2][s & 3], acc[t]);
        SB();
        if (s + 6 < 24) { rdB(s + 6, t); SB(); }
        if (t == 1) { issue(2 * s); SB(); }
        if (t == 4) { issue(2 * s + 1); SB(); }
      }
    }
    STAMP(14);
    constexpr u32 L_RED2 = 2 * 40960;
    if (wk == 0) {
#pragma unroll
      for (int t = 0; t < 5; ++t) *(LAS f32x4*)(smem + L_RED2 + (wm4 * 5 + t) * 1024 + lane * 16) = acc[t];
    }
    bar();
    if (wk == 1) {
      u32x2 op[5];
#pragma unroll
      for (int t = 0; t < 5; ++t) {
        const f32x4 v = acc[t] + *(LAS const f32x4*)(smem + L_RED2 + (wm4 * 5 + t) * 1024 + lane * 16);
        op[t] = u32x2{pack2(v[0], v[1]), pack2(v[2], v[3])};
      }
      const __amdgpu_buffer_rsrc_t ro = __builtin_amdgcn_make_buffer_rsrc(out, 0, 2048 * 1280, 0x00020000);
      const u32 oo = (128 * p + 64 * half + 16 * wm4 + r16) * 1280 + slice * 160 + (2 * (g & 1) + (g >> 1)) * 16;
      __builtin_amdgcn_raw_buffer_store_b128(quad2chunk(op[0], op[1]), ro, (int)oo, 0, OUT_AUX);
      __builtin_amdgcn_raw_buffer_store_b128(quad2chunk(op[2], op[3]), ro, (int)oo + 64, 0, OUT_AUX);
      const u32x4 last = quad2chunk(op[4], op[4]);
      if ((g & 1) == 0) __builtin_amdgcn_raw_buffer_store_b128(last, ro, (int)oo + 128, 0, OUT_AUX);
    }
    STAMP(15);
  }
}

extern "C" void kernel_launch(void* const* d_in, const int* in_sizes, int n_in, void* d_out, int out_size, void* d_ws, size_t ws_size, hipStream_t stream) {
  (void)in_sizes; (void)n_in; (void)out_size; (void)ws_size;
  (void)hipFuncSetAttribute((const void*)fused_block, hipFuncAttributeMaxDynamicSharedMemorySize, LDS_BYTES);
  fused_block<<<256, 512, LDS_BYTES, stream>>>((const char*)d_in[0], (const char*)d_in[1], (const char*)d_in[2], (const char*)d_in[4], (const char*)d_in[5],
                                               (const char*)d_in[6], (char*)d_out, (char*)d_ws);
}
```

```cpp
#include <hip/hip_runtime.h>
#include <stdint.h>

typedef unsigned short u16;
typedef unsigned int u32;
typedef unsigned long long u64;
typedef __bf16 bf16x8 __attribute__((ext_vector_type(8)));
typedef __bf16 bf16x2 __attribute__((ext_vector_type(2)));
typedef short s16x4 __attribute__((ext_vector_type(4)));
typedef float f32x4 __attribute__((ext_vector_type(4)));
typedef float f32x2 __attribute__((ext_vector_type(2)));
typedef u32 u32x4 __attribute__((ext_vector_type(4)));
typedef u32 u32x2 __attribute__((ext_vector_type(2)));
typedef u64 u64x2 __attribute__((ext_vector_type(2)));
#define LAS __attribute__((address_space(3)))
#define GAS __attribute__((address_space(1)))
#define DI __device__ __forceinline__
#define RLX_AGENT __ATOMIC_RELAXED, __HIP_MEMORY_SCOPE_AGENT

#define RG 5
#define OUT_AUX 0
#define W3BAR 0
#define DWARM 0
#define GMAP 0
#define DMAP 1
#define DROT 0
#define RSAT 8
#define X_A 0
#define X_B 0
#define X_C 0
#define X_D 0
#define POLLW 0
#define KSWAP 0
#define KROT 0
#define POLLV 8
#define FW 0
#define NEARLY 20
#define ATT_PRIO 0
#define NP 0
#define WPOS 1
#define XFIRST 2
#define XROT 0
#define WDEC 1
#define WDAT 1
#define WGAT 99
#define WGSP 0
#define KA 1
#define WH 3
#define SW 0
#define XL 1
#define XLAT 7
#define S1POS 2
#define CWW 0
#define STAMPS 0

constexpr u32 WS_EPOCH = 0;
constexpr u32 WS_WOF = 1024;
constexpr u32 WS_DENSE = 2048;
constexpr u32 WS_FLAG = 4096;
constexpr u32 WS_QKV = WS_FLAG + 4 * 256 * 256;
constexpr u32 WS_O = WS_QKV + 16 * 240 * 2048;
constexpr u32 WS_H = WS_O + 16 * 80 * 2048;
constexpr u32 WS_HH = WS_H + 16 * 80 * 2048;
constexpr u32 WS_ST = WS_HH + 16 * 192 * 2048;
constexpr u32 WS_RS = WS_ST + 16 * 8 * 1024;
constexpr u32 WS_WB = WS_RS + 2048 * 16;
constexpr u32 WT_SLICE = 68 * 5120;
constexpr u32 WS_WT = WS_WB + 3072;
constexpr u32 WS_END = WS_WT + 8 * WT_SLICE;
constexpr u32 WS_STAMP = WS_END;
constexpr u64 TAG_STEP = 0x9E3779B97F4A7C15ull;

constexpr u32 LDS_BYTES = 163840;
constexpr u32 L_MISC = 161792;
constexpr u32 L_K = 0, KCH = 6144;
constexpr u32 L_V = 6 * KCH, VCH = 6208;
constexpr u32 L_WOB = L_MISC - 102400;
__device__ constexpr u32 gb_off(int c) { return (c & 3) == 2 ? 0 : ((c & 3) == 3 ? 24576 : ((c & 3) == 0 ? 65536 : 106496)); }
__device__ constexpr u32 ga_off(int c) { return c % 3 == 1 ? 49152 : (c % 3 == 2 ? 90112 : 131072); }
constexpr u32 L_ST = 147456;
constexpr u32 L_WB = 49152;
constexpr u32 L_RED = 24576;
constexpr u32 L_WARM = L_MISC + 1792;
constexpr u32 L_XT = L_V + 5 * VCH;
constexpr u32 L_RT = 131072;
constexpr u32 CODE_SPAN = 42240;
constexpr u32 SPIN_CAP = 1u << 15;
constexpr float LN_EPS = 1e-5f;

extern __shared__ __attribute__((aligned(16))) char smem[];

DI u32x4 lds128(u32 off) { return *(LAS const u32x4*)(smem + off); }
DI u32x2 ldstr(u32 off) { return __builtin_bit_cast(u32x2, __builtin_amdgcn_ds_read_tr16_b64_v4i16((LAS s16x4*)(smem + off))); }
DI u32x4 ldstr2(u32 lo, u32 hi) { const u32x2 a = ldstr(lo), b = ldstr(hi); return u32x4{a[0], a[1], b[0], b[1]}; }
DI f32x4 mfma(u32x4 a, u32x4 b, f32x4 c) { return __builtin_amdgcn_mfma_f32_16x16x32_bf16(__builtin_bit_cast(bf16x8, a), __builtin_bit_cast(bf16x8, b), c, 0, 0, 0); }
typedef float f32x16 __attribute__((ext_vector_type(16)));
DI f32x16 mfma32(u32x4 a, u32x4 b, f32x16 c) { return __builtin_amdgcn_mfma_f32_32x32x16_bf16(__builtin_bit_cast(bf16x8, a), __builtin_bit_cast(bf16x8, b), c, 0, 0, 0); }
DI float max3(float a, float b, float c) { return __builtin_fmaxf(__builtin_fmaxf(a, b), c); }
DI f32x4 mfma16(u32x2 a, u32x2 b, f32x4 c) { return __builtin_amdgcn_mfma_f32_16x16x16bf16_1k(__builtin_bit_cast(s16x4, a), __builtin_bit_cast(s16x4, b), c, 0, 0, 0); }
DI float dot2(u32 a, u32 b, float c) { return __builtin_amdgcn_fdot2_f32_bf16(__builtin_bit_cast(bf16x2, a), __builtin_bit_cast(bf16x2, b), c, false); }
DI u32 pack2(float lo, float hi) { const f32x2 v = {lo, hi}; return __builtin_bit_cast(u32, __builtin_convertvector(v, bf16x2)); }
DI float bflo(u32 v) { return __uint_as_float(v << 16); }
DI float bfhi(u32 v) { return __uint_as_float(v & 0xffff0000u); }
constexpr u32 ONES2 = 0x3f803f80u;
DI float sum8(u32x4 a, float s) { const u32 a0 = a[0], a1 = a[1], a2 = a[2], a3 = a[3]; return dot2(a3, ONES2, dot2(a2, ONES2, dot2(a1, ONES2, dot2(a0, ONES2, s)))); }
DI float sq8(u32x4 a, float s) { const u32 a0 = a[0], a1 = a[1], a2 = a[2], a3 = a[3]; return dot2(a3, a3, dot2(a2, a2, dot2(a1, a1, dot2(a0, a0, s)))); }
DI float gsum(float x) {
  auto a = __builtin_amdgcn_permlane16_swap(__float_as_uint(x), __float_as_uint(x), false, false);
  x = __uint_as_float(a[0]) + __uint_as_float(a[1]);
  auto b = __builtin_amdgcn_permlane32_swap(__float_as_uint(x), __float_as_uint(x), false, false);
  return __uint_as_float(b[0]) + __uint_as_float(b[1]);
}
DI float gmax(float x) {
  auto a = __builtin_amdgcn_permlane16_swap(__float_as_uint(x), __float_as_uint(x), false, false);
  x = __builtin_fmaxf(__uint_as_float(a[0]), __uint_as_float(a[1]));
  auto b = __builtin_amdgcn_permlane32_swap(__float_as_uint(x), __float_as_uint(x), false, false);
  return __builtin_fmaxf(__uint_as_float(b[0]), __uint_as_float(b[1]));
}
DI u32x4 quad2chunk(u32x2 A, u32x2 B) {
  const u32 a0 = A[0], a1 = A[1], b0 = B[0], b1 = B[1];
  auto s0 = __builtin_amdgcn_permlane16_swap(a0, b0, false, false);
  auto s1 = __builtin_amdgcn_permlane16_swap(a1, b1, false, false);
  return u32x4{s0[0], s1[0], s0[1], s1[1]};
}
DI void dma(const void* sbase, u32 voff, u32 lds) {
  asm volatile("s_mov_b32 m0, %2\n\ts_nop 0\n\tglobal_load_lds_dwordx4 %0, %1" ::"v"(voff), "s"(sbase), "s"(lds));
}
DI void dma_sc1(const void* sbase, u32 voff, u32 lds) {
  asm volatile("s_mov_b32 m0, %2\n\ts_nop 0\n\tglobal_load_lds_dwordx4 %0, %1 sc1" ::"v"(voff), "s"(sbase), "s"(lds));
}
DI void gld_sc1(u32x4& dst, const void* sbase, u32 voff) {
  asm volatile("global_load_dwordx4 %0, %1, %2 sc1" : "=v"(dst) : "v"(voff), "s"(sbase));
}
DI void gld(u32x4& dst, const void* sbase, u32 voff) {
  asm volatile("global_load_dwordx4 %0, %1, %2" : "=v"(dst) : "v"(voff), "s"(sbase));
}
DI void gld64(u32x2& dst, const void* sbase, u32 voff) {
  asm volatile("global_load_dwordx2 %0, %1, %2" : "=v"(dst) : "v"(voff), "s"(sbase));
}
template <int N> DI void vm_wait() { asm volatile("s_waitcnt vmcnt(%0)" ::"n"(N) : "memory"); }
template <int N> DI void vm_wait_use(u32x4& r) { asm volatile("s_waitcnt vmcnt(%1)" : "+v"(r) : "n"(N)); }
#define INL __attribute__((always_inline))
#define SB() __builtin_amdgcn_sched_barrier(0)
DI void warm(const void* sbase, u32 voff, u32 lds) {
  asm volatile("s_mov_b32 m0, %2\n\ts_nop 0\n\tglobal_load_lds_dword %0, %1" ::"v"(voff), "s"(sbase), "s"(lds));
}
DI void warm_sc1(const void* sbase, u32 voff, u32 lds) {
  asm volatile("s_mov_b32 m0, %2\n\ts_nop 0\n\tglobal_load_lds_dword %0, %1 sc1" ::"v"(voff), "s"(sbase), "s"(lds));
}
#define SL(o) "s_load_dword %0, %1, " #o "\n\t"
DI void swarm15(u32& sink, const void* base) {
  asm volatile(SL(0) SL(128) SL(256) SL(3072) SL(3200) SL(3328) SL(6144) SL(6272) SL(6400) SL(9216) SL(9344) SL(9472) SL(12288) SL(12416) SL(12544) : "=&s"(sink) : "s"(base));
}
DI void bar0() { asm volatile("s_barrier" ::: "memory"); }
DI u32x4 cat(u32x2 a, u32x2 b) { return u32x4{a[0], a[1], b[0], b[1]}; }
DI void bar() { asm volatile("s_waitcnt lgkmcnt(0)\n\ts_barrier" ::: "memory"); }
DI void st128_sc1(__amdgpu_buffer_rsrc_t rs, u32 off, u32x4 v) { __builtin_amdgcn_raw_buffer_store_b128(v, rs, (int)off, 0, 16); }
DI void st64_sc1(__amdgpu_buffer_rsrc_t rs, u32 off, u32x2 v) { __builtin_amdgcn_raw_buffer_store_b64(v, rs, (int)off, 0, 16); }

DI void poll_flags(const char* ws, u32 off, u64 tag) {
  GAS u64* a = (GAS u64*)(ws + off);
  __builtin_amdgcn_s_sleep(POLLV);
  for (u32 spins = 0; spins < SPIN_CAP; ++spins) {
    const u64 v = __hip_atomic_load(a, RLX_AGENT);
    if (__all(v == tag)) break;
    if (POLLW) __builtin_amdgcn_s_sleep(POLLW);
  }
  __builtin_amdgcn_fence(__ATOMIC_ACQUIRE, "wavefront");
}
DI u32 flag_off(int phase, int p, int j) { return WS_FLAG + (u32)((phase * 256 + p * 16 + j) * 256); }

#define STAMP(i) do {} while (0)

__global__ __launch_bounds__(512, 2) void fused_block(const char*, const char*, const char* __restrict__ wo,
                                                      const char* __restrict__ wg, const char* __restrict__ wu, const char* __restrict__ wd,
                                                      char* __restrict__ out, char*) {
  u64 h0_t; asm volatile("s_memrealtime %0\n\ts_waitcnt lgkmcnt(0)" : "=s"(h0_t));
  u64x2 ka; u64 kw;
  int tid = threadIdx.x, bx = blockIdx.x;
  asm volatile("s_load_dwordx4 %0, %3, 0\n\ts_load_dwordx2 %1, %3, 0x38" : "=&s"(ka), "=&s"(kw), "+s"(bx) : "s"(__builtin_amdgcn_kernarg_segment_ptr()));
  asm volatile("" : "+v"(tid));
  const char *x, *wqkv; char* ws;
  __builtin_assume(bx >= 0 && bx < 256);
  const int w = (__builtin_amdgcn_readfirstlane(tid) >> 6) & 7;
  const int p = ((bx & 7) >> 1) * 4 + (bx >> 6), j = (bx & 1) * 8 + ((bx >> 3) & 7);
  const int rs_ = ((bx >> 3) & 7) + (bx >> 6), rot = rs_ - 5 * ((rs_ * 52) >> 8);
  __builtin_assume(rot >= 0 && rot < 5);
  u32 voffA[2], voffB[2];
  {
    const int lane = tid & 63, r16 = lane & 15, g = lane >> 4;
#pragma unroll
    for (int i = 0; i < 2; ++i) {
      const int ra = 16 * w + 8 * i + (lane >> 3);
      voffA[i] = ra * 1280 + (((lane & 7) ^ ((ra >> 1) & 7)) << 4);
      const int rb = 8 * w + 4 * i + g, f = (rb & 3) | (((rb >> 3) & 1) << 2);
      voffB[i] = rb * 3840 + (((((r16 >> 1) ^ f) << 1) | (r16 & 1)) << 4);
    }
    const u32 oa = p * 163840 + rot * 128, ob = j * 256 + rot * 245760;
    u32 a0 = voffA[0] + oa, a1 = voffA[1] + oa, b0 = voffB[0] + ob, b1 = voffB[1] + ob, l0 = w * 2048;
    u32 l1 = l0 + 1024, l2 = l0 + 16384, l3 = l0 + 17408;
    asm volatile("" : "+v"(a0), "+v"(a1), "+v"(b0), "+v"(b1));
    asm volatile("s_waitcnt lgkmcnt(0)" : "+s"(ka), "+s"(kw), "+s"(l0), "+s"(l1), "+s"(l2), "+s"(l3));
    x = (const char*)ka[0]; wqkv = (const char*)ka[1];
    STAMP(22);
    if (__builtin_expect(j < 15, 1)) { dma(x, a0, l0); dma(x, a1, l1); dma(wqkv, b0, l2); dma(wqkv, b1, l3); }
    asm volatile("" : "+s"(bx), "+s"(ka), "+s"(kw));
    asm volatile("" : "+v"(tid));
    __builtin_assume(bx >= 0 && bx < 256 && tid >= 0 && tid < 512);
    x = (const char*)ka[0]; wqkv = (const char*)ka[1]; ws = (char*)kw;
  }
  const int lane = tid & 63, r16 = lane & 15, g = lane >> 4, q4 = r16 >> 2, pq = r16 & 3;
  const int xcd = bx & 7, rr = bx >> 3;
  const int pa = rr >> 1, ja = 2 * xcd + (rr & 1);
  const int pg = 8 * (xcd >> 2) + (rr >> 2), jg = 4 * (xcd & 3) + (rr & 3);
  const int half = (rr >> 2) & 1, slice = 4 * (xcd & 1) + (rr & 3);
  if (w == CWW) {
    u64 pc; asm volatile("s_getpc_b64 %0" : "=s"(pc));
    const u32 o = rr * 1408 + (lane < 11 ? lane : 10) * 128;
    warm((const char*)(pc & ~127ull), o < CODE_SPAN ? o : CODE_SPAN, L_WARM);
  }
  constexpr int PW = 3;
  const __amdgpu_buffer_rsrc_t rs = __builtin_amdgcn_make_buffer_rsrc(ws, 0, WS_END, 0x00020000);
  u64 tag;
  auto load_epoch = [&](u32x2& e) { const u32 z = 0; asm volatile("global_load_dwordx2 %0, %1, %2 sc1" : "=v"(e) : "v"(z), "s"(ws + WS_EPOCH)); };
  auto make_tag = [&](u32x2& e) {
    asm volatile("" : "+v"(e));
    const u32 e0 = e[0], e1 = e[1];
    tag = (((u64)(u32)__builtin_amdgcn_readfirstlane(e1) << 32) | (u32)__builtin_amdgcn_readfirstlane(e0)) + TAG_STEP;
  };
  const char* xa = x + (size_t)p * 128 * 1280;
  const u32x4 ones8 = {ONES2, ONES2, ONES2, ONES2};
  const char* wt = ws + WS_WT + slice * WT_SLICE;
  auto rt_dst = [&](int piece, int blk, int gq) {
    const int tile = 4 * piece + g, sl = tile / 5, t = tile - sl * 5;
    return WS_WT + sl * WT_SLICE + blk * 5120 + t * 1024 + gq * 256 + r16 * 16;
  };
  auto warm_gu = [&]() INL {
    if (GMAP) {
      const int l3 = lane < 30 ? lane : 29, rw = l3 / 3;
      warm(((w & 1) ? wu : wg) + (size_t)(rr * 20 + (w >> 1) * 10) * 3072 + xcd * 384, rw * 3072 + (l3 - rw * 3) * 128, L_WARM);
      return;
    }
    if (WH == 1) {
      const int l6 = lane < 30 ? lane : 29, rw = l6 / 6;
      warm(((xcd & 4) ? wu : wg) + (size_t)(rr * 20 + (w & 3) * 5) * 3072 + (xcd & 3) * 768, rw * 3072 + (l6 - rw * 6) * 128, L_WARM);
      return;
    }
    if (WH == 2) {
      const int l6 = lane < 15 ? lane : 14, rw = l6 / 3;
      warm(((xcd & 4) ? wu : wg) + (size_t)(rr * 20 + (w & 3) * 5) * 3072 + (xcd & 3) * 768, rw * 3072 + (l6 - rw * 3) * 256, L_WARM);
      return;
    }
    if (WH == 3) {
      const int l6 = lane < 60 ? lane : 59, rw = l6 / 6;
      if ((w & 3) < 2) warm(((xcd & 4) ? wu : wg) + (size_t)(rr * 20 + (w & 1) * 10) * 3072 + (xcd & 3) * 768, rw * 3072 + (l6 - rw * 6) * 128, L_WARM);
      return;
    }
    const int l6 = lane < 60 ? lane : 59, rw = l6 / 6;
    warm(((w & 1) ? wu : wg) + (size_t)(rr * 20 + ((w >> 1) & 1) * 10) * 3072 + (xcd & 3) * 768, rw * 3072 + (l6 - rw * 6) * 128, L_WARM);
  };
  u32 kvsrc[8], kvdst[8], fl_get;
  auto setup = [&]() INL {
#pragma unroll
    for (int i = 0; i < 8; ++i) {
      const int id = w + 8 * i, kv = id >= 30, r = id - 30 * kv, c = r / 6, r2 = r - 6 * c;
      int pp = pa - 2 + (r2 >> 1); pp = pp < 0 ? 0 : pp;
      kvsrc[i] = WS_QKV + (pp * 240 + 80 * (1 + kv) + 5 * ja + c) * 2048 + (r2 & 1) * 1024;
      kvdst[i] = (kv ? L_V + c * VCH : L_K + c * KCH) + r2 * 1024;
      asm volatile("" : "+s"(kvsrc[i]), "+s"(kvdst[i]));
    }
    const int e = r16 < 14 ? r16 : 0, sec = e < 2 ? 0 : (e < 8 ? 1 : 2), i = e < 2 ? e : (e - 2) % 6;
    int pp = e < 2 ? pa : pa - (i >> 1); pp = pp < 0 ? 0 : pp;
    fl_get = (!WDEC && (g & 1)) ? flag_off(0, r16, 15) : flag_off(0, pp, (80 * sec + 5 * ja + 4 * (i & 1)) >> 4);
    asm volatile("" : "+v"(fl_get));
  };

  auto ld_xt = [&]() INL {
    const int rw = lane / 10;
    if (XLAT == 10) {
      const char* s4 = xa + (64 * half + 16 * (w & 3)) * 1280 + slice * 160;
      if (w >= 4 && lane < 40) {
#pragma unroll
        for (int i = 0; i < 4; ++i) dma(s4 + i * 5120, rw * 1280 + (lane - 10 * rw) * 16, L_XT + (w & 3) * 2560 + i * 640);
      }
      return;
    }
    const char* src = xa + (64 * half + 8 * w) * 1280 + slice * 160;
    if (lane < 40) {
      dma(src, rw * 1280 + (lane - 10 * rw) * 16, L_XT + w * 1280);
      dma(src + 4 * 1280, rw * 1280 + (lane - 10 * rw) * 16, L_XT + w * 1280 + 640);
    }
    SB();
  };
  if (j < 15) {
    const int wm = w & 1, wn = w >> 1;
    const char* wb = wqkv + j * 256;
    auto stage = [&](int c, int piece) {
      int kc = c % RG + rot; kc = (kc >= RG ? kc - RG : kc) + (c / RG) * RG;
      const u32 slot = (c & 3) * 32768;
      const char* a = xa + kc * 128; const char* b = wb + (size_t)kc * 64 * 3840;
      if (piece < 0 || piece == 0) dma(a, voffA[0], slot + w * 2048);
      if (piece < 0 || piece == 1) dma(a, voffA[1], slot + w * 2048 + 1024);
      if (piece < 0 || piece == 2) dma(b, voffB[0], slot + 16384 + w * 2048);
      if (piece < 0 || piece == 3) dma(b, voffB[1], slot + 16384 + w * 2048 + 1024);
    };
    STAMP(23);
    u32x2 epoch;
    load_epoch(epoch);
    if (S1POS == 0 || S1POS == 4) stage(1, -1);
    if (S1POS == 1) { stage(1, 0); stage(1, 2); }
    if (S1POS == 3) { stage(1, -1); stage(2, 0); stage(2, 1); }
    SB();
    setup();
    STAMP(24);
    if (S1POS == 2) stage(1, -1);
    if (S1POS == 1) { stage(1, 1); stage(1, 3); }
    if (S1POS == 4) { stage(2, 0); stage(2, 1); }
    STAMP(25);
    u32 adA[2], adB[2];
#pragma unroll
    for (int ks = 0; ks < 2; ++ks) adA[ks] = (64 * wm + r16) * 128 + (((4 * ks + g) ^ ((r16 >> 1) & 7)) << 4);
#pragma unroll
    for (int n = 0; n < 2; ++n) adB[n] = 16384 + (8 * g + q4) * 256 + (((2 * wn + (n ^ wm)) ^ (q4 | ((g & 1) << 2))) << 5) + pq * 8;
    f32x4 acc[4][2], csum = {0, 0, 0, 0};
#pragma unroll
    for (int m = 0; m < 4; ++m) { acc[m][0] = f32x4{0, 0, 0, 0}; acc[m][1] = f32x4{0, 0, 0, 0}; }
    u32x4 fa[2][4]; u32x2 fb[2][2][2];
    u32x4 rsg[4];
    auto ld_rs = [&]() {
#pragma unroll
      for (int m = 0; m < 4; ++m) gld_sc1(rsg[m], ws + WS_RS + (p * 128 + 64 * wm + 16 * m) * 16, r16 * 16);
    };
    auto rdp = [&](int c, int ks, int i) {
      const u32 slot = (c & 3) * 32768;
      if (i == 0 || i == 1) fb[ks][0][i] = ldstr(slot + adB[0] + ks * 8192 + i * 1024);
      else if (i == 2) fa[ks][0] = lds128(slot + adA[ks]);
      else if (i == 3 || i == 4) fb[ks][1][i - 3] = ldstr(slot + adB[1] + ks * 8192 + (i - 3) * 1024);
      else if (i < 8) fa[ks][i - 4] = lds128(slot + adA[ks] + (i - 4) * 2048);
    };
    auto mm = [&](int ks, int cr, int cn) {
      const u32x4 b0 = cat(fb[ks][0][0], fb[ks][0][1]), b1 = cat(fb[ks][1][0], fb[ks][1][1]);
#pragma unroll
      for (int i = 0; i < 9; ++i) {
        if (i > 0) acc[(i - 1) >> 1][(i - 1) & 1] = mfma(((i - 1) & 1) ? b1 : b0, fa[ks][(i - 1) >> 1], acc[(i - 1) >> 1][(i - 1) & 1]);
        else csum = mfma(b0, ones8, csum);
        if (cr == 10 && i == 3) {
          if (r16 == 0) *(LAS f32x4*)(smem + L_MISC + 1024 + (32 * wn + 16 * wm + 4 * g) * 4) = csum;
          asm volatile("" ::: "memory");
          if (lane == 0) *(volatile LAS u32*)(smem + L_MISC + w * 4) = 1;
        }
        if (cr < 10) rdp(cr, ks ^ 1, i);
        if (cn < 10 && i == 4) stage(cn, 2 * (ks ^ 1));
        if (cn < 10 && i == 8) stage(cn, 2 * (ks ^ 1) + 1);
        SB();
      }
    };
    auto ld_wd = [&]() INL {
      if (j < 12 && w < 5) {
        dma(wd + (p * 12 + j) * 10240 + w * 256, (lane >> 3) * 1280 + (lane & 7) * 16, L_RT + w * 2048);
        dma(wd + (p * 12 + j) * 10240 + w * 256 + 128, (lane >> 3) * 1280 + (lane & 7) * 16, L_RT + w * 2048 + 1024);
      }
      SB();
    };
    if (tid < 8) *(LAS u32*)(smem + L_MISC + tid * 4) = 0;
    if (S1POS >= 3) vm_wait<6>(); else vm_wait<4>();
    make_tag(epoch);
    bar();
    STAMP(16);
#pragma unroll
    for (int i = 0; i < 8; ++i) rdp(0, 0, i);
    SB();
    if (WDAT == -1) ld_wd();
    if (S1POS < 3) { stage(2, 0); stage(2, 1); }
    SB();
#pragma unroll
    for (int c = 0; c < 10; ++c) {
      mm(0, c, c + 2);
      if (c < 9) {
        {
          constexpr int base[9] = {4, 4, 4, 4, 4, 4, 4, 4, 0};
          const int n = base[c] + (c == RSAT + 1 ? 4 : 0);
          if (XL && c == XLAT + 1) { if (n == 0) vm_wait<2>(); else vm_wait<6>(); } else
          if (WGAT < 9 && c >= WGAT + 1 && c <= WGAT + 1 + 3 * WGSP && w >= 4 && c == WGAT + 1 + (w - 4) * WGSP) { if (n == 0) vm_wait<1>(); else vm_wait<5>(); }
          else if (c == WDAT + 1 && j < 12 && w < 5) { if (n == 0) vm_wait<2>(); else if (n == 4) vm_wait<6>(); else vm_wait<10>(); }
          else { if (n == 0) vm_wait<0>(); else if (n == 4) vm_wait<4>(); else vm_wait<8>(); }
        }
        bar0();
        if (c == RSAT) {
          ld_rs(); SB();
        }
        if (c == WDAT) ld_wd();
        if (XL && c == XLAT) ld_xt();
        if (WGAT < 9 && c >= WGAT && c <= WGAT + 3 * WGSP && w >= 4 && c == WGAT + (w - 4) * WGSP) { warm_gu(); SB(); }
      }
      mm(1, c + 1, c + 3);
    }
    STAMP(1);
    if (XL && XLAT == 8) vm_wait<2>(); else vm_wait<0>();
    for (u32 spins = 0; spins < SPIN_CAP; ++spins) {
      asm volatile("" : "+v"(rsg[0]), "+v"(rsg[1]), "+v"(rsg[2]), "+v"(rsg[3]));
      bool ok = true;
#pragma unroll
      for (int m = 0; m < 4; ++m) ok = ok && rsg[m][1] == (u32)tag && rsg[m][3] == (u32)(tag >> 32);
      if (__all(ok)) break;
      ld_rs();
      vm_wait<0>();
    }
    asm volatile("" : "+v"(rsg[0]), "+v"(rsg[1]), "+v"(rsg[2]), "+v"(rsg[3]));
    STAMP(27);
    while (*(volatile LAS u32*)(smem + L_MISC + (w ^ 1) * 4) == 0) {}
    asm volatile("" ::: "memory");
    STAMP(17);
    const float qs = j < 5 ? 0.22360679774997896f * 1.4426950408889634f : 1.f;
    f32x4 cs[2];
#pragma unroll
    for (int n = 0; n < 2; ++n) cs[n] = n == 0 ? csum : *(LAS const f32x4*)(smem + L_MISC + 1024 + (32 * wn + 16 * (1 ^ wm) + 4 * g) * 4);
    const u32 qb = WS_QKV + (p * 240 + 16 * j + 4 * wn + 2 * ((g & 1) ^ wm) + (g >> 1)) * 2048 + (64 * wm + r16) * 16;
#pragma unroll
    for (int m = 0; m < 4; ++m) {
      const u32 um = rsg[m][0], ur = rsg[m][2];
      const float mean = __uint_as_float(um), sc = __uint_as_float(ur) * qs;
      u32x2 pk[2];
#pragma unroll
      for (int n = 0; n < 2; ++n) {
        const f32x4 v = (acc[m][n] - mean * cs[n]) * sc;
        pk[n] = u32x2{pack2(v[0], v[1]), pack2(v[2], v[3])};
      }
      st128_sc1(rs, qb + m * 256, quad2chunk(pk[0], pk[1]));
    }
    asm volatile("" ::: "memory");
    if (XL && XLAT >= 9) ld_xt();
  } else {
    u32x2 epoch;
    load_epoch(epoch);
    auto ld_wo = [&]() INL {
#pragma unroll
      for (int i = 0; i < 7; ++i) { const int id = w + 8 * i < 50 ? w + 8 * i : 49; dma(wo + p * 51200 + id * 1024, lane * 16, id * 1024); }
    };
    if (!XFIRST) ld_wo();
    u32x4 xv[2][10];
#pragma unroll
    for (int t = 0; t < 10; ++t)
#pragma unroll
      for (int s2 = 0; s2 < 2; ++s2) gld(xv[s2][t], xa + (16 * w + 8 * s2) * 1280 + ((t + XROT) % 10) * 128, (lane >> 3) * 1280 + (lane & 7) * 16);
    if (XFIRST == 1) ld_wo();
    float s1[2] = {0.f, 0.f}, sq[2] = {0.f, 0.f};
#pragma unroll
    for (int t = 0; t < 10; ++t) {
      if (t == 0) vm_wait<10 + 7 * (XFIRST == 1)>();
      if (t == 5) vm_wait<4 + 7 * (XFIRST == 1)>();
      if (t == 8) vm_wait<7 * (XFIRST == 1)>();
#pragma unroll
      for (int s2 = 0; s2 < 2; ++s2) { asm volatile("" : "+v"(xv[s2][t])); s1[s2] = sum8(xv[s2][t], s1[s2]); sq[s2] = sq8(xv[s2][t], sq[s2]); }
    }
    make_tag(epoch);
#pragma unroll
    for (int o = 1; o < 8; o <<= 1) { s1[0] += __shfl_xor(s1[0], o); sq[0] += __shfl_xor(sq[0], o); s1[1] += __shfl_xor(s1[1], o); sq[1] += __shfl_xor(sq[1], o); }
    {
      const float a1 = (lane & 1) ? s1[1] : s1[0], a2 = (lane & 1) ? sq[1] : sq[0];
      const float mean = a1 * (1.f / 640.f), var = __builtin_fmaxf(a2 * (1.f / 640.f) - mean * mean, 0.f);
      if ((lane & 7) < 2)
        st128_sc1(rs, WS_RS + (p * 128 + 16 * w + 8 * (lane & 1) + (lane >> 3)) * 16, u32x4{__float_as_uint(mean), (u32)tag, __float_as_uint(__builtin_amdgcn_rsqf(var + LN_EPS)), (u32)(tag >> 32)});
    }
    STAMP(16);
    asm volatile("" ::: "memory");
    if (XFIRST == 2) ld_wo();
    if (XL) ld_xt();
    setup();
    if (XFIRST == 2) vm_wait<0>(); else vm_wait<1>();
    bar();
    {
#pragma unroll
      for (int i = 0; i < 7; ++i) {
        const int id = w + 8 * i, gr = id / 10;
        if (id < 50) {
          const u32 l = gr * 10240 + q4 * 1280 + (id - 10 * gr) * 128 + g * 32 + pq * 8;
          st128_sc1(rs, rt_dst(id - 10 * gr, (p * 5 + gr) >> 2, (p * 5 + gr) & 3), ldstr2(l, l + 5120));
        }
      }
      if (w >= 3) {
        float a1 = 0.f;
#pragma unroll
        for (int t = 0; t < 10; ++t) a1 = sum8(lds128((w - 3) * 10240 + (lane >> 3) * 1280 + t * 128 + (lane & 7) * 16), a1);
#pragma unroll
        for (int o = 1; o < 8; o <<= 1) a1 += __shfl_xor(a1, o);
        a1 *= (1.f / 640.f);
        const u32 hi = pack2(a1, 0.f), lo = pack2(a1 - bflo(hi), 0.f);
        if ((lane & 7) < 2) __builtin_amdgcn_raw_buffer_store_b16((u16)((lane & 1) ? lo : hi), rs, (int)(WS_WB + (lane & 1) * 1280 + ((p * 5 + w - 3) * 8 + (lane >> 3)) * 2), 0, 16);
      }
    }
  }
  STAMP(18);
  u32 sink = 0;
  if (SW) {
    asm volatile("s_waitcnt lgkmcnt(0)" ::: "memory");
    swarm15(sink, ((xcd & 4) ? wu : wg) + (size_t)(rr * 20 + (w >> 1) * 5) * 3072 + (xcd & 3) * 768 + (w & 1) * 384);
  }
  if (XL && XLAT == 9 && j < 15) vm_wait<2>(); else if (XL && XLAT == 10 && j < 15 && w >= 4) vm_wait<4>(); else vm_wait<0>();
  if (SW) bar0(); else { bar(); STAMP(2); }
  if (w == FW && lane < (j == 15 ? 2 : 1)) __hip_atomic_store((GAS u64*)(ws + (lane == 0 ? flag_off(0, p, j) : WS_WOF + p * 8)), tag, RLX_AGENT);
  asm volatile("" ::: "memory");
  if (w != PW && w != 7) {
    const int kr = (w < PW ? w : w - 1) * 64 + lane;
    const bool valid = 128 * (pa - 2) + kr >= 0;
    *(LAS u32x4*)(smem + L_K + 5 * KCH + kr * 16) = u32x4{valid ? 0u : 0xf149u, 0u, ONES2, ONES2};
  }
  if (w == PW) poll_flags(ws, fl_get, tag);
  if (SW) bar0(); else { bar(); STAMP(3); }

  u32x2 xr[6];
  u32 voffG[3];
  {
    const int h = w & 1, qt = w >> 1, l32 = lane & 31, hh = lane >> 5;
    u32x4 qf[2];
    gld_sc1(qf[0], ws + WS_QKV + (pa * 240 + 5 * ja + 2 * h) * 2048 + qt * 512, hh * 2048 + l32 * 16);
    gld_sc1(qf[1], ws + WS_QKV + (pa * 240 + 5 * ja + 2 * h + 2) * 2048 + qt * 512, l32 * 16);
#pragma unroll
    for (int i = 0; i < 5; ++i) dma_sc1(ws + kvsrc[i], lane * 16, kvdst[i]);
    u32 dm[8];
#pragma unroll
    for (int m = 0; m < 8; ++m) {
      const int k0 = ((2 * m) & 3) + 8 * (m >> 1) + 4 * hh;
      dm[m] = (k0 <= l32 ? 0xffffu : 0u) | (k0 + 1 <= l32 ? 0xffff0000u : 0u);
      asm volatile("" : "+v"(dm[m]));
    }
    u32 adK0 = L_K + (2 * h + hh) * KCH + (32 * qt + l32) * 16, adK1 = L_K + (hh ? 5 : 2 * h + 2) * KCH + (32 * qt + l32) * 16;
    const u32 vrow = 32 * qt + 4 * hh + q4;
    u32 adV = (g & 1) == 0 ? L_V + (3 * h + (pq >> 1)) * VCH + vrow * 16 + (pq & 1) * 8
                           : (pq == 0 ? L_V + 2 * VCH + vrow * 16 + 8 * h : L_K + 5 * KCH + ((4 * hh + (h ? 4 : 12)) & 15) * 16 + 8);
#pragma unroll
    for (int i = 0; i < 3; ++i) {
      const int s = 64 * i + lane, rw = s / 12, cp = s - rw * 12;
      int c = cp - (w & 1) - 2 * ((rw >> 3) & 1); c = c < 0 ? c + 12 : c;
      voffG[i] = rw * 3072 + c * 16;
    }
    asm volatile("" : "+v"(adK0), "+v"(adK1), "+v"(adV), "+v"(voffG[0]), "+v"(voffG[1]), "+v"(voffG[2]));
    vm_wait<1>();
    asm volatile("" : "+v"(qf[0]), "+v"(qf[1]));
    bar();
    asm volatile("" : "+s"(sink));
    STAMP(4);
    const f32x16 zero16 = {0, 0, 0, 0, 0, 0, 0, 0, 0, 0, 0, 0, 0, 0, 0, 0};
    u32x4 kf[9][2];
    constexpr int ord[9] = {0, 8, 1, 2, 3, 4, 5, 6, 7};
#pragma unroll
    for (int n = 0; n < 4; ++n) { kf[ord[n]][0] = lds128(adK0 + 512 * ord[n]); kf[ord[n]][1] = lds128(adK1 + 512 * ord[n]); }
    SB();
    dma_sc1(ws + kvsrc[5], lane * 16, kvdst[5]);
    if (h == 1 && hh == 0) { qf[0][0] = 0; qf[0][1] = 0; }
    if (h == 0) { qf[1][2] = 0; qf[1][3] = 0; }
    if (hh == 1) qf[1] = u32x4{0x3f80u, 0, 0, 0};
    SB();
    f32x16 sc[9];
    float m4[4];
    auto qk = [&](int i, int st) { sc[i] = mfma32(kf[i][st], qf[st], st == 0 ? zero16 : sc[i]); };
    qk(0, 0); qk(8, 0);
    SB();
    dma_sc1(ws + kvsrc[6], lane * 16, kvdst[6]);
    SB();
    qk(0, 1); qk(8, 1);
    SB();
    u32x2 wof;
    if (w < 4) {
      dma_sc1(ws + kvsrc[7], lane * 16, kvdst[7]);
      asm volatile("global_load_dwordx2 %0, %1, %2 sc1" : "=v"(wof) : "v"(r16 * 8), "s"(ws + WS_WOF));
    } else asm volatile("v_mov_b64 %0, 0" : "=v"(wof));
    SB();
#pragma unroll
    for (int n = 2; n < 11; ++n) {
      if (n < 9) { qk(ord[n], 0); SB(); }
      if (n + 2 < 9) { kf[ord[n + 2]][0] = lds128(adK0 + 512 * ord[n + 2]); SB(); }
      if (n == 3) {
#pragma unroll
        for (int r = 6; r < 16; ++r) sc[0][r] = ((r & 3) + 8 * (r >> 2) + 4 * hh <= l32) ? sc[8][r] : sc[0][r];
      } else if (n > 3) {
#pragma unroll
        for (int c = 0; c < 4; ++c) m4[c] = max3(m4[c], sc[n - 3][2 * c], sc[n - 3][2 * c + 1]);
      }
      SB();
      if (n < 9) { qk(ord[n], 1); SB(); }
      if (n + 2 < 9) { kf[ord[n + 2]][1] = lds128(adK1 + 512 * ord[n + 2]); SB(); }
      if (n == 2) {
#pragma unroll
        for (int r = 0; r < 6; ++r) sc[0][r] = ((r & 3) + 8 * (r >> 2) + 4 * hh <= l32) ? sc[8][r] : sc[0][r];
      } else if (n == 3) {
#pragma unroll
        for (int c = 0; c < 4; ++c) m4[c] = max3(max3(sc[0][4 * c], sc[0][4 * c + 1], sc[0][4 * c + 2]), sc[0][4 * c + 3], sc[0][4 * c + 3]);
      } else {
#pragma unroll
        for (int c = 0; c < 4; ++c) m4[c] = max3(m4[c], sc[n - 3][8 + 2 * c], sc[n - 3][9 + 2 * c]);
      }
      SB();
    }
    float mx = __builtin_fmaxf(max3(m4[0], m4[1], m4[2]), m4[3]);
    { auto sw = __builtin_amdgcn_permlane32_swap(__float_as_uint(mx), __float_as_uint(mx), false, false); mx = __builtin_fmaxf(__uint_as_float(sw[0]), __uint_as_float(sw[1])); }
    u32x4 nmf;
    {
      const float nmx = -mx;
      const u32 p0 = __float_as_uint(nmx) & 0xffff0000u;
      const float r1 = nmx - __uint_as_float(p0);
      const u32 p1 = __float_as_uint(r1) & 0xffff0000u;
      const u32 p2 = __float_as_uint(r1 - __uint_as_float(p1));
      nmf = u32x4{hh ? p2 >> 16 : (p0 >> 16) | p1, 0, 0, 0};
    }
    auto submax = [&](int i) { sc[i] = mfma32(ones8, nmf, sc[i]); };
    submax(0); submax(1);
    SB();
    u32x4 rtv[3];
    if (w < 4) {
#pragma unroll
      for (int i = 0; i < 3; ++i) { const u32 l = L_RT + (w + 4 * i) * 1024 + q4 * 128 + g * 32 + pq * 8; rtv[i] = ldstr2(l, l + 512); }
    }
    STAMP(29);
    vm_wait<0>();
    bar();
    STAMP(26);
    if (w < 4) {
      if (WPOS == 2) warm_gu();
      if (j < 12) {
        const int gi = p * 12 + j;
#pragma unroll
        for (int i = 0; i < 3; ++i)
          if (w + 4 * i < 10) st128_sc1(rs, rt_dst(w + 4 * i, 20 + (gi >> 2), gi & 3), rtv[i]);
      }
      asm volatile("" : "+v"(wof));
      if (WDEC && !__all(wof[0] == (u32)tag && wof[1] == (u32)(tag >> 32))) poll_flags(ws, WS_WOF + r16 * 8, tag);
#pragma unroll
      for (int i = 0; i < 20 - 2 * NP; ++i) {
        const u32 o = 20480 + (8 * NP + w * (20 - 2 * NP) + i) * 1024;
        dma_sc1(wt + o, lane * 16, L_WOB + o);
      }
#pragma unroll
      for (int t = 0; t < 5; ++t) if (!XL) gld64(xr[t], xa + (64 * half + 16 * w) * 1280 + slice * 160 + t * 32, r16 * 1280 + g * 8);
      asm volatile("global_load_dwordx2 %0, %1, %2 sc1" : "=v"(xr[5]) : "v"(r16 * 16), "s"(ws + WS_RS + (p * 128 + 64 * half + 16 * w) * 16));
      if (!SW && WPOS == 1 && (WGAT > 9 || j == 15)) warm_gu();
    } else {
#pragma unroll
      for (int t = XL ? 5 : 0; t < 6; ++t) asm volatile("v_mov_b64 %0, 0" : "=v"(xr[t]));
    }
    SB();
    f32x16 o = zero16;
    u32 pw[2][8];
    u32x2 vf[2][4], vx[4];
    auto rdV = [&](int i, int e) { return ldstr(adV + 512 * i + 128 * e); };
    auto pfrag = [&](const u32* q, int st) { return u32x4{q[4 * st], q[4 * st + 1], q[4 * st + 2], q[4 * st + 3]}; };
    u32 p8[8];
#pragma unroll
    for (int n = 0; n < 8; ++n) {
      float ex[16];
#pragma unroll
      for (int e = 0; e < 8; ++e) {
        ex[2 * e] = __builtin_amdgcn_exp2f(sc[n][2 * e]); SB();
        if (e < 4) { vf[n & 1][e] = rdV(n, e); SB(); }
        else if (n == 0) { vx[e - 4] = rdV(8, e - 4); SB(); }
        ex[2 * e + 1] = __builtin_amdgcn_exp2f(sc[n][2 * e + 1]); SB();
        if (e > 0) { pw[n & 1][e - 1] = pack2(ex[2 * e - 2], ex[2 * e - 1]); SB(); }
        if (n >= 1) {
          if (e == 0 && n < 7) { submax(n + 1); SB(); }
          if (e == 3) { o = mfma32(cat(vf[(n - 1) & 1][0], vf[(n - 1) & 1][1]), pfrag(pw[(n - 1) & 1], 0), o); SB(); }
          if (e == 6) { o = mfma32(cat(vf[(n - 1) & 1][2], vf[(n - 1) & 1][3]), pfrag(pw[(n - 1) & 1], 1), o); SB(); }
          if (n == 2 && e == 2) { o = mfma32(cat(vx[0], vx[1]), pfrag(p8, 0), o); SB(); }
          if (n == 2 && e == 5) { o = mfma32(cat(vx[2], vx[3]), pfrag(p8, 1), o); SB(); }
        }
      }
      pw[n & 1][7] = pack2(ex[14], ex[15]);
      if (n == 0) {
#pragma unroll
        for (int m = 0; m < 8; ++m) { p8[m] = pw[0][m] & dm[m]; pw[0][m] &= ~dm[m]; }
      }
      SB();
    }
    o = mfma32(cat(vf[1][0], vf[1][1]), pfrag(pw[1], 0), o);
    o = mfma32(cat(vf[1][2], vf[1][3]), pfrag(pw[1], 1), o);
    const float inv = __builtin_amdgcn_rcpf(o[12]);
    const u32 x0 = pack2(o[0] * inv, o[1] * inv), x1 = pack2(o[2] * inv, o[3] * inv), y0 = pack2(o[4] * inv, o[5] * inv), y1 = pack2(o[6] * inv, o[7] * inv);
    auto s0 = __builtin_amdgcn_permlane32_swap(x0, y0, false, false);
    auto s1 = __builtin_amdgcn_permlane32_swap(x1, y1, false, false);
    const u32 ob = WS_O + (pa * 80 + 5 * ja) * 2048 + (32 * qt + l32) * 16;
    st128_sc1(rs, ob + (3 * h + hh) * 2048, u32x4{s0[0], s1[0], s0[1], s1[1]});
    if (hh == 0) st64_sc1(rs, ob + 2 * 2048 + 8 * h, u32x2{pack2(o[8] * inv, o[9] * inv), pack2(o[10] * inv, o[11] * inv)});
    STAMP(5);
    if (XL) {
#pragma unroll
      for (int t = 0; t < 5; ++t) xr[t] = w < 4 ? *(LAS const u32x2*)(smem + L_XT + (16 * w + r16) * 160 + t * 32 + g * 8) : u32x2{0, 0};
    }
  }
  const char* gsrc = ((w & 1) ? wu : wg) + jg * 192;
  auto stageGBp = [&](int c, int rb, int i) {
    dma(gsrc + (size_t)(c * 64 + rb * 16) * 3072, voffG[i], gb_off(c) + (w & 1) * 12288 + rb * 3072 + i * 1024);
  };
  auto stageGB = [&](int c, int rb) { stageGBp(c, rb, 0); stageGBp(c, rb, 1); stageGBp(c, rb, 2); };
  const int wm4 = w & 3, wk = w >> 2, kh = KSWAP ? wk ^ (slice & 1) : wk;
  u32 voffR = g * 2048 + (64 * half + 16 * wm4 + r16) * 16;
  fl_get = flag_off(1, p, r16);
  u32 fl_get2 = flag_off(2, pg, r16), fl_put = lane == 0 ? flag_off(1, pa, ja) : WS_DENSE + bx * 8;
  asm volatile("" : "+v"(fl_put));
  asm volatile("" : "+v"(voffR), "+v"(fl_get), "+v"(fl_get2));
  asm volatile("" ::: "memory");
  bar();
  if (w < 4) {
#pragma unroll
    for (int i = 0; i < 5; ++i) dma_sc1(wt + 5120 * w + i * 1024, lane * 16, L_WOB + 5120 * w + i * 1024);
    dma_sc1(ws + WS_WB + (w < 3 ? w : 2) * 1024, lane * 16, L_WB + (w < 3 ? w : 2) * 1024);
    vm_wait<6>();
  } else {
    if (X_A) vm_wait<0>(); else {
    stageGB(2, w >> 1); stageGB(2, (w >> 1) - 2);
    vm_wait<6>(); }
  }
  bar0();
  STAMP(6);
  u32x2 hp[5];
  {
    if (w == FW && lane < 2) __hip_atomic_store((GAS u64*)(ws + fl_put), tag, RLX_AGENT);
    u32x4 a[10], b[6][5];
    u32 ad0 = L_WOB + kh * 10 * 5120 + lane * 16, adm = L_WB + (r16 & 1) * 1280 + kh * 640 + g * 16;
    asm volatile("" : "+v"(ad0), "+v"(adm));
    const bool alt = KROT && (slice & 2);
    auto kso = [&](int s) { return alt ? (s < 6 ? 4 + (s + 3) % 6 : (s - 4) & 3) : (s + 4) % 10; };
    auto rdB = [&](int s, int t) { b[s % 6][t] = lds128(ad0 + kso(s) * 5120 + t * 1024); };
#pragma unroll
    for (int s = 0; s < 6; ++s)
#pragma unroll
      for (int t = 0; t < 5; ++t) rdB(s, t);
    asm volatile("" : "+v"(xr[0]), "+v"(xr[1]), "+v"(xr[2]), "+v"(xr[3]), "+v"(xr[4]), "+v"(xr[5]));
    f32x4 hacc[5], macc;
    { const u32 xm = xr[5][0]; macc = f32x4{__uint_as_float(xm), 0.f, 0.f, 0.f}; }
#pragma unroll
    for (int t = 0; t < 5; ++t) { const u32 a0 = xr[t][0], a1 = xr[t][1]; hacc[t] = f32x4{bflo(a0), bfhi(a0), bflo(a1), bfhi(a1)}; }
    SB();
    if (w == PW) poll_flags(ws, fl_get, tag); else if (w < 4) vm_wait<0>();
    bar();
    STAMP(7);
    const char* osrc = ws + WS_O + (p * 80 + 40 * kh) * 2048;
#pragma unroll
    for (int s = 0; s < 10; ++s) gld_sc1(a[s], osrc + kso(s) * 8192, voffR);
    u32x4 bm[2];
    bm[0] = lds128(adm + kso(0) * 64);
    SB();
#pragma unroll
    for (int s = 0; s < 10; ++s) {
      switch (s) {
        case 0: vm_wait_use<9>(a[0]); break; case 1: vm_wait_use<8>(a[1]); break; case 2: vm_wait_use<7>(a[2]); break;
        case 3: vm_wait_use<6>(a[3]); break; case 4: vm_wait_use<5>(a[4]); break; case 5: vm_wait_use<4>(a[5]); break;
        case 6: vm_wait_use<3>(a[6]); break; case 7: vm_wait_use<2>(a[7]); break; case 8: vm_wait_use<1>(a[8]); break;
        default: vm_wait_use<0>(a[9]); break;
      }
      if (s == 0) STAMP(28);
#pragma unroll
      for (int t = 0; t < 5; ++t) {
        hacc[t] = mfma(b[s % 6][t], a[s], hacc[t]);
        SB();
        if (s + 6 < 10) { rdB(s + 6, t); SB(); }
        if (t == 0 && s < 9) { bm[(s + 1) & 1] = lds128(adm + kso(s + 1) * 64); SB(); }
      }
      macc = mfma(bm[s & 1], a[s], macc);
      SB();
    }
    STAMP(8);
    if (wk == 0) {
#pragma unroll
      for (int t = 0; t < 5; ++t) *(LAS f32x4*)(smem + L_RED + (wm4 * 5 + t) * 1024 + lane * 16) = hacc[t];
      *(LAS float*)(smem + L_RED + 20480 + wm4 * 256 + lane * 4) = macc[0] + macc[1];
    }
    bar();
    if (wk == 1) {
      float s1 = 0.f, s2 = 0.f;
      const float mean = macc[0] + macc[1] + *(LAS const float*)(smem + L_RED + 20480 + wm4 * 256 + lane * 4);
      u32x2 hc[5];
#pragma unroll
      for (int t = 0; t < 5; ++t) {
        const f32x4 v = hacc[t] + *(LAS const f32x4*)(smem + L_RED + (wm4 * 5 + t) * 1024 + lane * 16);
        const u32 lo = pack2(v[0], v[1]), hi = pack2(v[2], v[3]);
        hp[t] = u32x2{lo, hi};
        hc[t] = u32x2{pack2(bflo(lo) - mean, bfhi(lo) - mean), pack2(bflo(hi) - mean, bfhi(hi) - mean)};
      }
      const u32 hb = WS_H + (p * 80 + 10 * slice + 2 * (g & 1) + (g >> 1)) * 2048 + (64 * half + 16 * wm4 + r16) * 16;
      st128_sc1(rs, hb, quad2chunk(hc[0], hc[1]));
      st128_sc1(rs, hb + 4 * 2048, quad2chunk(hc[2], hc[3]));
      const u32x4 last = quad2chunk(hc[4], hc[4]);
      if ((g & 1) == 0) st128_sc1(rs, hb + 8 * 2048, last);
#pragma unroll
      for (int t = 0; t < 5; ++t) { const u32 lo = hc[t][0], hi = hc[t][1]; s1 = dot2(hi, ONES2, dot2(lo, ONES2, s1)); s2 = dot2(hi, hi, dot2(lo, lo, s2)); }
      s1 = gsum(s1); s2 = gsum(s2);
      if (g == 0) st64_sc1(rs, WS_ST + (p * 8 + slice) * 1024 + (64 * half + 16 * wm4 + r16) * 8, u32x2{__float_as_uint(s1), __float_as_uint(s2)});
      if (W3BAR) bar();
      vm_wait<0>();
    } else {
#pragma unroll
      for (int t = 0; t < 5; ++t) hp[t] = u32x2{0, 0};
      if (W3BAR) bar();
      if (!X_B) { stageGB(0, w >> 1); stageGB(0, 2 + (w >> 1)); stageGB(1, w >> 1); stageGB(1, 2 + (w >> 1)); }
    }
  }
  bar();
  STAMP(9);
  if (w == FW && lane == 0) __hip_atomic_store((GAS u64*)(ws + flag_off(2, p, j)), tag, RLX_AGENT);
  if (w == PW) {
    poll_flags(ws, fl_get2, tag);
  } else if (w == 2) {
    GAS u64* a = (GAS u64*)(ws + WS_DENSE + lane * 8);
    for (u32 spins = 0; spins < SPIN_CAP; ++spins) {
      const u64 v0 = __hip_atomic_load(a, RLX_AGENT), v1 = __hip_atomic_load(a + 64, RLX_AGENT), v2 = __hip_atomic_load(a + 128, RLX_AGENT), v3 = __hip_atomic_load(a + 192, RLX_AGENT);
      if (__all(v0 == tag && v1 == tag && v2 == tag && v3 == tag)) break;
    }
    if (bx == 0 && lane == 0) __hip_atomic_store((GAS u64*)(ws + WS_EPOCH), tag, RLX_AGENT);
  } else if (wk == 1) {
    if (!X_C) { stageGB(3, w >> 1); stageGB(3, (w >> 1) - 2); }
  }
  bar();
  STAMP(10);

  const int rotd = DROT == 0 ? 0 : (DMAP ? (((3 * (rr & 3)) >> 1) + (DROT == 2 ? ((6 * (rr >> 2)) >> 3) : 0)) % 6 : (((slice * 6) >> 3) + (DROT == 2 ? ((3 * (rr >> 3)) >> 1) : 0)) % 6);
  auto stageDq = [&](int c, int blk, int i) {
    int kc = c + rotd; kc = kc >= 6 ? kc - 6 : kc;
    dma_sc1(wt + (20 + kc * 8 + blk) * 5120 + i * 1024, lane * 16, (c & 3) * 40960 + blk * 5120 + i * 1024);
  };
  auto stageDp = [&](int c, int i) { stageDq(c, w, i); };
  auto stageD = [&](int c) {
#pragma unroll
    for (int i = 0; i < 5; ++i) stageDp(c, i);
  };

  {
    const int wm = w >> 2, wn = w & 3;
    const char* hsrc = ws + WS_H + (pg * 80 + w) * 2048;
    auto stageGAp = [&](int c, int i) { dma_sc1(hsrc + c * 16384 + i * 1024, lane * 16, ga_off(c) + w * 2048 + i * 1024); };
    auto stageGA = [&](int c) { stageGAp(c, 0); stageGAp(c, 1); };
    stageGA(0);
    dma_sc1(ws + WS_ST + (pg * 8 + w) * 1024, lane * 16, L_ST + w * 1024);
    stageGA(1);
    const u32 adA = g * 2048 + (64 * wm + r16) * 16;
    u32 adB[3];
#pragma unroll
    for (int t = 0; t < 3; ++t) {
      const int mat = t == 0 ? 0 : (t == 1 ? 1 : (pq >> 1)), c = t < 2 ? 3 * wn + (pq >> 1) : 3 * wn + 2;
      int cp = c + mat + 2 * (g & 1); cp = cp >= 12 ? cp - 12 : cp;
      adB[t] = mat * 12288 + (8 * g + q4) * 192 + cp * 16 + (pq & 1) * 8;
    }
    f32x4 acc[4][3];
#pragma unroll
    for (int m = 0; m < 4; ++m)
#pragma unroll
      for (int t = 0; t < 3; ++t) acc[m][t] = f32x4{0, 0, 0, 0};
    u32x4 fa[2][4]; u32x2 fb[2][3][2];
    auto rdp = [&](int c, int s, int i) {
      const u32 sa = ga_off(c) + s * 8192, sb = gb_off(c) + s * 6144;
      if (i < 2) fb[s][0][i] = ldstr(sb + adB[0] + i * 768);
      else if (i == 2) fa[s][0] = lds128(sa + adA);
      else if (i < 7) fb[s][1 + ((i - 3) >> 1)][(i - 3) & 1] = ldstr(sb + adB[1 + ((i - 3) >> 1)] + ((i - 3) & 1) * 768);
      else if (i < 10) fa[s][i - 6] = lds128(sa + adA + (i - 6) * 256);
    };
    auto mm = [&](int s, int c) {
      u32x4 b[3];
#pragma unroll
      for (int t = 0; t < 3; ++t) b[t] = cat(fb[s][t][0], fb[s][t][1]);
      const int cr = s == 0 ? c : c + 1;
#pragma unroll
      for (int i = 0; i < 12; ++i) {
        acc[i / 3][i % 3] = mfma(b[i % 3], fa[s][i / 3], acc[i / 3][i % 3]);
        if (cr < 10) rdp(cr, s ^ 1, i);
        if (s == 1 && (i == 4 || i == 10) && c + 3 < 10) stageGAp(c + 3, i == 10);
        if (s == 0 && i % 4 == 3 && c >= 1 && c + 3 < 10) stageGBp(c + 3, w >> 1, i >> 2);
        if (DWARM && s == 1 && i == 7 && c >= 1 && c <= 4 && (w >> 1) == c - 1)
          warm_sc1(ws + WS_WT + w * WT_SLICE + 102400 + rr * 7680, (lane < 60 ? lane : 59) * 128, L_WARM);
        if (s == 1 && (c == 7 || c == 8) && i % 2 == 1 && i < 10) stageDp(c - 7, i >> 1);
        SB();
      }
    };
    vm_wait<3>();
    bar();
    STAMP(19);
#pragma unroll
    for (int i = 0; i < 10; ++i) rdp(0, 0, i);
    SB();
    stageGA(2);
    SB();
#pragma unroll
    for (int c = 0; c < 10; ++c) {
      if (c == 1) {
        const f32x2 u0 = *(LAS const f32x2*)(smem + L_ST + (2 * g) * 1024 + (16 * w + r16) * 8), u1 = *(LAS const f32x2*)(smem + L_ST + (2 * g + 1) * 1024 + (16 * w + r16) * 8);
        const float s1 = gsum(u0[0] + u1[0]), s2 = gsum(u0[1] + u1[1]);
        const float mean = s1 * (1.f / 640.f), var = __builtin_fmaxf(s2 * (1.f / 640.f) - mean * mean, 0.f);
        if (g == 0) *(LAS float*)(smem + L_MISC + (16 * w + r16) * 4) = __builtin_amdgcn_rcpf(var + LN_EPS);
      }
      mm(0, c);
      if (c < 9) {
        if (c == 0) vm_wait<2>(); else if (c == 1) vm_wait<5>(); else if (c <= 6) vm_wait<8>(); else vm_wait<5>();
        bar();
        if (c == 4) STAMP(20);
        if (c == 7) STAMP(30);
      }
      mm(1, c);
    }
    STAMP(11);
    u32x2 p01[4];
    float r2[4];
#pragma unroll
    for (int m = 0; m < 4; ++m) r2[m] = *(LAS const float*)(smem + L_MISC + (64 * wm + 16 * m + r16) * 4);
    if (wm == 1) { vm_wait<5>(); bar(); }
#pragma unroll
    for (int m = 0; m < 4; ++m) {
      const f32x4 v = acc[m][0] * acc[m][1] * r2[m];
      p01[m] = u32x2{pack2(v[0], v[1]), pack2(v[2], v[3])};
    }
    const u32 hb = WS_HH + (pg * 192 + 12 * jg + 3 * wn) * 2048 + (64 * wm + r16) * 16;
    st128_sc1(rs, hb + (g >> 1) * 2048 + (g & 1) * 256, quad2chunk(p01[0], p01[1]));
    st128_sc1(rs, hb + (g >> 1) * 2048 + (2 + (g & 1)) * 256, quad2chunk(p01[2], p01[3]));
    u32x2 p2[2];
#pragma unroll
    for (int mm2 = 0; mm2 < 2; ++mm2) {
      f32x4 v;
#pragma unroll
      for (int r = 0; r < 4; ++r) {
        auto sw = __builtin_amdgcn_permlane32_swap(__float_as_uint(acc[2 * mm2][2][r]), __float_as_uint(acc[2 * mm2 + 1][2][r]), false, false);
        v[r] = __uint_as_float(sw[0]) * __uint_as_float(sw[1]) * (lane < 32 ? r2[2 * mm2] : r2[2 * mm2 + 1]);
      }
      p2[mm2] = u32x2{pack2(v[0], v[1]), pack2(v[2], v[3])};
    }
    st128_sc1(rs, hb + 2 * 2048 + (2 * (g & 1) + (g >> 1)) * 256, quad2chunk(p2[0], p2[1]));
    STAMP(31);
    if (wm == 0) {
      vm_wait<8>();
      bar();
#pragma unroll
      for (int i = 0; i < 10; ++i) stageDq(2, w + 4 * (i / 5), i % 5);
    }
  }

  {
    u32x4 b[6][5];
    u32 ad0 = kh * 4 * 5120 + lane * 16, ad2 = ad0 + 81920;
    asm volatile("" : "+v"(ad0), "+v"(ad2));
    auto rdB = [&](int s, int t) { b[s % 6][t] = lds128(((s & 8) ? ad2 : ad0) + ((s >> 2) & 1) * 40960 + (s & 3) * 5120 + t * 1024); };
#pragma unroll
    for (int s = 0; s < 4; ++s)
#pragma unroll
      for (int t = 0; t < 5; ++t) rdB(s, t);
    SB();
    if (wk == 0) vm_wait<10>(); else vm_wait<0>();
    bar();
    STAMP(12);
    if (w == FW && lane == 0) __hip_atomic_store((GAS u64*)(ws + flag_off(3, pg, jg)), tag, RLX_AGENT);
    if (wk == 1) {
#pragma unroll
      for (int i = 0; i < 10; ++i) if (!X_D) stageDq(3, w - 4 * (i / 5), i % 5);
    }
#pragma unroll
    for (int s = 4; s < 6; ++s)
#pragma unroll
      for (int t = 0; t < 5; ++t) rdB(s, t);
    f32x4 acc[5];
#pragma unroll
    for (int t = 0; t < 5; ++t) { const u32 a = hp[t][0], b2 = hp[t][1]; acc[t] = f32x4{bflo(a), bfhi(a), bflo(b2), bfhi(b2)}; }
    SB();
    if (w == PW) poll_flags(ws, fl_get + 2 * 65536, tag); else if (wk == 0) vm_wait<0>();
    bar();
    STAMP(13);
    u32x4 a[6][4];
    const char* asrc = ws + WS_HH + (p * 192 + 16 * kh) * 2048;
    auto loadAp = [&](int c, int s) {
      int kc = c + rotd; kc = kc >= 6 ? kc - 6 : kc;
      gld_sc1(a[c][s], asrc + kc * 65536 + s * 8192, voffR);
    };
    auto issue = [&](int n) {
      if (n < 4) loadAp(2, n); else if (n < 9) stageDp(4, n - 4); else if (n < 13) loadAp(3, n - 9); else if (n < 18) stageDp(5, n - 13);
      else if (n < 22) loadAp(4, n - 18); else if (n < 26) loadAp(5, n - 22);
    };
#pragma unroll
    for (int s = 0; s < 4; ++s) loadAp(0, s);
#pragma unroll
    for (int s = 0; s < 4; ++s) loadAp(1, s);
#pragma unroll
    for (int s = 0; s < 24; ++s) {
      if (s == 0) vm_wait<4>();
      if (s == 4) vm_wait<8>();
      if (s == 8) vm_wait<12>();
      if (s == 10) vm_wait<11>();
      if (s == 12) vm_wait<11>();
      if (s == 14) vm_wait<8>();
      if (s == 16) vm_wait<4>();
      if (s == 20) vm_wait<0>();
      if (s % 4 == 0) asm volatile("" : "+v"(a[s >> 2][0]), "+v"(a[s >> 2][1]), "+v"(a[s >> 2][2]), "+v"(a[s >> 2][3]));
      if (s == 6) bar();
      if (s == 10 || s == 14) bar0();
      if (s == 0) STAMP(21);
#pragma unroll
      for (int t = 0; t < 5; ++t) {
        acc[t] = mfma(b[s % 6][t], a[s >> 2][s & 3], acc[t]);
        SB();
        if (s + 6 < 24) { rdB(s + 6, t); SB(); }
        if (t == 1) { issue(2 * s); SB(); }
        if (t == 4) { issue(2 * s + 1); SB(); }
      }
    }
    STAMP(14);
    constexpr u32 L_RED2 = 2 * 40960;
    if (wk == 0) {
#pragma unroll
      for (int t = 0; t < 5; ++t) *(LAS f32x4*)(smem + L_RED2 + (wm4 * 5 + t) * 1024 + lane * 16) = acc[t];
    }
    bar();
    if (wk == 1) {
      u32x2 op[5];
#pragma unroll
      for (int t = 0; t < 5; ++t) {
        const f32x4 v = acc[t] + *(LAS const f32x4*)(smem + L_RED2 + (wm4 * 5 + t) * 1024 + lane * 16);
        op[t] = u32x2{pack2(v[0], v[1]), pack2(v[2], v[3])};
      }
      const __amdgpu_buffer_rsrc_t ro = __builtin_amdgcn_make_buffer_rsrc(out, 0, 2048 * 1280, 0x00020000);
      const u32 oo = (128 * p + 64 * half + 16 * wm4 + r16) * 1280 + slice * 160 + (2 * (g & 1) + (g >> 1)) * 16;
      __builtin_amdgcn_raw_buffer_store_b128(quad2chunk(op[0], op[1]), ro, (int)oo, 0, OUT_AUX);
      __builtin_amdgcn_raw_buffer_store_b128(quad2chunk(op[2], op[3]), ro, (int)oo + 64, 0, OUT_AUX);
      const u32x4 last = quad2chunk(op[4], op[4]);
      if ((g & 1) == 0) __builtin_amdgcn_raw_buffer_store_b128(last, ro, (int)oo + 128, 0, OUT_AUX);
    }
    STAMP(15);
  }
  if (xcd == 0) { const u64 end = h0_t + 4500; for (int q = 0; q < 100000; ++q) { u64 t; asm volatile("s_memrealtime %0\n\ts_waitcnt lgkmcnt(0)" : "=s"(t)); if (t >= end) break; } }
}

extern "C" void kernel_launch(void* const* d_in, const int* in_sizes, int n_in, void* d_out, int out_size, void* d_ws, size_t ws_size, hipStream_t stream) {
  (void)in_sizes; (void)n_in; (void)out_size; (void)ws_size;
  (void)hipFuncSetAttribute((const void*)fused_block, hipFuncAttributeMaxDynamicSharedMemorySize, LDS_BYTES);
  fused_block<<<256, 512, LDS_BYTES, stream>>>((const char*)d_in[0], (const char*)d_in[1], (const char*)d_in[2], (const char*)d_in[4], (const char*)d_in[5],
                                               (const char*)d_in[6], (char*)d_out, (char*)d_ws);
}
```

```cpp
#include <hip/hip_runtime.h>
#include <stdint.h>

typedef unsigned short u16;
typedef unsigned int u32;
typedef unsigned long long u64;
typedef __bf16 bf16x8 __attribute__((ext_vector_type(8)));
typedef __bf16 bf16x2 __attribute__((ext_vector_type(2)));
typedef short s16x4 __attribute__((ext_vector_type(4)));
typedef float f32x4 __attribute__((ext_vector_type(4)));
typedef float f32x2 __attribute__((ext_vector_type(2)));
typedef u32 u32x4 __attribute__((ext_vector_type(4)));
typedef u32 u32x2 __attribute__((ext_vector_type(2)));
typedef u64 u64x2 __attribute__((ext_vector_type(2)));
#define LAS __attribute__((address_space(3)))
#define GAS __attribute__((address_space(1)))
#define DI __device__ __forceinline__
#define RLX_AGENT __ATOMIC_RELAXED, __HIP_MEMORY_SCOPE_AGENT

constexpr int RG = 5;
constexpr int RSAT = 8, WDAT = 1, XLAT = 7;
constexpr int POLLV = 8, PV2 = 12;
constexpr int FW = 0;
#define STAMPS 0

constexpr u32 WS_EPOCH = 0;
constexpr u32 WS_WOF = 1024;
constexpr u32 WS_DENSE = 2048;
constexpr u32 WS_FLAG = 4096;
constexpr u32 WS_QKV = WS_FLAG + 4 * 256 * 256;
constexpr u32 WS_O = WS_QKV + 16 * 240 * 2048;
constexpr u32 WS_H = WS_O + 16 * 80 * 2048;
constexpr u32 WS_HH = WS_H + 16 * 80 * 2048;
constexpr u32 WS_ST = WS_HH + 16 * 192 * 2048;
constexpr u32 WS_RS = WS_ST + 16 * 8 * 1024;
constexpr u32 WS_WB = WS_RS + 2048 * 16;
constexpr u32 WT_SLICE = 68 * 5120;
constexpr u32 WS_WT = WS_WB + 3072;
constexpr u32 WS_END = WS_WT + 8 * WT_SLICE;
constexpr u32 WS_STAMP = WS_END;
constexpr u64 TAG_STEP = 0x9E3779B97F4A7C15ull;

constexpr u32 LDS_BYTES = 163840;
constexpr u32 L_MISC = 161792;
constexpr u32 L_K = 0, KCH = 6144;
constexpr u32 L_V = 6 * KCH, VCH = 6208;
constexpr u32 L_WOB = L_MISC - 102400;
__device__ constexpr u32 gb_off(int c) { return (c & 3) == 2 ? 0 : ((c & 3) == 3 ? 24576 : ((c & 3) == 0 ? 65536 : 106496)); }
__device__ constexpr u32 ga_off(int c) { return c % 3 == 1 ? 49152 : (c % 3 == 2 ? 90112 : 131072); }
constexpr u32 L_ST = 147456;
constexpr u32 L_WB = 49152;
constexpr u32 L_RED = 24576;
constexpr u32 L_WARM = L_MISC + 1792;
constexpr u32 L_XT = L_V + 5 * VCH;
constexpr u32 L_RT = 131072;
constexpr u32 CODE_SPAN = 42240;
constexpr u32 SPIN_CAP = 1u << 15;
constexpr float LN_EPS = 1e-5f;

extern __shared__ __attribute__((aligned(16))) char smem[];

DI u32x4 lds128(u32 off) { return *(LAS const u32x4*)(smem + off); }
DI u32x2 ldstr(u32 off) { return __builtin_bit_cast(u32x2, __builtin_amdgcn_ds_read_tr16_b64_v4i16((LAS s16x4*)(smem + off))); }
DI u32x4 ldstr2(u32 lo, u32 hi) { const u32x2 a = ldstr(lo), b = ldstr(hi); return u32x4{a[0], a[1], b[0], b[1]}; }
DI f32x4 mfma(u32x4 a, u32x4 b, f32x4 c) { return __builtin_amdgcn_mfma_f32_16x16x32_bf16(__builtin_bit_cast(bf16x8, a), __builtin_bit_cast(bf16x8, b), c, 0, 0, 0); }
typedef float f32x16 __attribute__((ext_vector_type(16)));
DI f32x16 mfma32(u32x4 a, u32x4 b, f32x16 c) { return __builtin_amdgcn_mfma_f32_32x32x16_bf16(__builtin_bit_cast(bf16x8, a), __builtin_bit_cast(bf16x8, b), c, 0, 0, 0); }
DI float max3(float a, float b, float c) { return __builtin_fmaxf(__builtin_fmaxf(a, b), c); }
DI float dot2(u32 a, u32 b, float c) { return __builtin_amdgcn_fdot2_f32_bf16(__builtin_bit_cast(bf16x2, a), __builtin_bit_cast(bf16x2, b), c, false); }
DI u32 pack2(float lo, float hi) { const f32x2 v = {lo, hi}; return __builtin_bit_cast(u32, __builtin_convertvector(v, bf16x2)); }
DI float bflo(u32 v) { return __uint_as_float(v << 16); }
DI float bfhi(u32 v) { return __uint_as_float(v & 0xffff0000u); }
constexpr u32 ONES2 = 0x3f803f80u;
DI float sum8(u32x4 a, float s) { const u32 a0 = a[0], a1 = a[1], a2 = a[2], a3 = a[3]; return dot2(a3, ONES2, dot2(a2, ONES2, dot2(a1, ONES2, dot2(a0, ONES2, s)))); }
DI float sq8(u32x4 a, float s) { const u32 a0 = a[0], a1 = a[1], a2 = a[2], a3 = a[3]; return dot2(a3, a3, dot2(a2, a2, dot2(a1, a1, dot2(a0, a0, s)))); }
DI float gsum(float x) {
  auto a = __builtin_amdgcn_permlane16_swap(__float_as_uint(x), __float_as_uint(x), false, false);
  x = __uint_as_float(a[0]) + __uint_as_float(a[1]);
  auto b = __builtin_amdgcn_permlane32_swap(__float_as_uint(x), __float_as_uint(x), false, false);
  return __uint_as_float(b[0]) + __uint_as_float(b[1]);
}
DI u32x4 quad2chunk(u32x2 A, u32x2 B) {
  const u32 a0 = A[0], a1 = A[1], b0 = B[0], b1 = B[1];
  auto s0 = __builtin_amdgcn_permlane16_swap(a0, b0, false, false);
  auto s1 = __builtin_amdgcn_permlane16_swap(a1, b1, false, false);
  return u32x4{s0[0], s1[0], s0[1], s1[1]};
}
DI void dma(const void* sbase, u32 voff, u32 lds) {
  asm volatile("s_mov_b32 m0, %2\n\ts_nop 0\n\tglobal_load_lds_dwordx4 %0, %1" ::"v"(voff), "s"(sbase), "s"(lds));
}
DI void dma_sc1(const void* sbase, u32 voff, u32 lds) {
  asm volatile("s_mov_b32 m0, %2\n\ts_nop 0\n\tglobal_load_lds_dwordx4 %0, %1 sc1" ::"v"(voff), "s"(sbase), "s"(lds));
}
DI void gld_sc1(u32x4& dst, const void* sbase, u32 voff) {
  asm volatile("global_load_dwordx4 %0, %1, %2 sc1" : "=v"(dst) : "v"(voff), "s"(sbase));
}
DI void gld(u32x4& dst, const void* sbase, u32 voff) {
  asm volatile("global_load_dwordx4 %0, %1, %2" : "=v"(dst) : "v"(voff), "s"(sbase));
}
template <int N> DI void vm_wait() { asm volatile("s_waitcnt vmcnt(%0)" ::"n"(N) : "memory"); }
template <int N> DI void vm_wait_use(u32x4& r) { asm volatile("s_waitcnt vmcnt(%1)" : "+v"(r) : "n"(N)); }
#define INL __attribute__((always_inline))
#define SB() __builtin_amdgcn_sched_barrier(0)
DI void warm(const void* sbase, u32 voff, u32 lds) {
  asm volatile("s_mov_b32 m0, %2\n\ts_nop 0\n\tglobal_load_lds_dword %0, %1" ::"v"(voff), "s"(sbase), "s"(lds));
}
DI void bar0() { asm volatile("s_barrier" ::: "memory"); }
DI u32x4 cat(u32x2 a, u32x2 b) { return u32x4{a[0], a[1], b[0], b[1]}; }
DI void bar() { asm volatile("s_waitcnt lgkmcnt(0)\n\ts_barrier" ::: "memory"); }
DI void st128_sc1(__amdgpu_buffer_rsrc_t rs, u32 off, u32x4 v) { __builtin_amdgcn_raw_buffer_store_b128(v, rs, (int)off, 0, 16); }
DI void st64_sc1(__amdgpu_buffer_rsrc_t rs, u32 off, u32x2 v) { __builtin_amdgcn_raw_buffer_store_b64(v, rs, (int)off, 0, 16); }

template <int DELAY = POLLV> DI void poll_flags(const char* ws, u32 off, u64 tag) {
  GAS u64* a = (GAS u64*)(ws + off);
  __builtin_amdgcn_s_sleep(DELAY);
  for (u32 spins = 0; spins < SPIN_CAP; ++spins) {
    const u64 v = __hip_atomic_load(a, RLX_AGENT);
    if (__all(v == tag)) break;
  }
  __builtin_amdgcn_fence(__ATOMIC_ACQUIRE, "wavefront");
}
DI u32 flag_off(int phase, int p, int j) { return WS_FLAG + (u32)((phase * 256 + p * 16 + j) * 256); }

#define STAMP(i) do {} while (0)

__global__ __launch_bounds__(512, 2) void fused_block(const char*, const char*, const char* __restrict__ wo,
                                                      const char* __restrict__ wg, const char* __restrict__ wu, const char* __restrict__ wd,
                                                      char* __restrict__ out, char*) {
  u64x2 ka; u64 kw;
  int tid = threadIdx.x, bx = blockIdx.x;
  asm volatile("s_load_dwordx4 %0, %3, 0\n\ts_load_dwordx2 %1, %3, 0x38" : "=&s"(ka), "=&s"(kw), "+s"(bx) : "s"(__builtin_amdgcn_kernarg_segment_ptr()));
  asm volatile("" : "+v"(tid));
  const char *x, *wqkv; char* ws;
  __builtin_assume(bx >= 0 && bx < 256);
  const int w = (__builtin_amdgcn_readfirstlane(tid) >> 6) & 7;
  const int p = ((bx & 7) >> 1) * 4 + (bx >> 6), j = (bx & 1) * 8 + ((bx >> 3) & 7);
  const int rs_ = ((bx >> 3) & 7) + (bx >> 6), rot = rs_ - 5 * ((rs_ * 52) >> 8);
  __builtin_assume(rot >= 0 && rot < 5);
  u32 voffA[2], voffB[2];
  {
    const int lane = tid & 63, r16 = lane & 15, g = lane >> 4;
#pragma unroll
    for (int i = 0; i < 2; ++i) {
      const int ra = 16 * w + 8 * i + (lane >> 3);
      voffA[i] = ra * 1280 + (((lane & 7) ^ ((ra >> 1) & 7)) << 4);
      const int rb = 8 * w + 4 * i + g, f = (rb & 3) | (((rb >> 3) & 1) << 2);
      voffB[i] = rb * 3840 + (((((r16 >> 1) ^ f) << 1) | (r16 & 1)) << 4);
    }
    const u32 oa = p * 163840 + rot * 128, ob = j * 256 + rot * 245760;
    u32 a0 = voffA[0] + oa, a1 = voffA[1] + oa, b0 = voffB[0] + ob, b1 = voffB[1] + ob, l0 = w * 2048;
    u32 l1 = l0 + 1024, l2 = l0 + 16384, l3 = l0 + 17408;
    asm volatile("" : "+v"(a0), "+v"(a1), "+v"(b0), "+v"(b1));
    asm volatile("s_waitcnt lgkmcnt(0)" : "+s"(ka), "+s"(kw), "+s"(l0), "+s"(l1), "+s"(l2), "+s"(l3));
    x = (const char*)ka[0]; wqkv = (const char*)ka[1];
    STAMP(22);
    if (__builtin_expect(j < 15, 1)) { dma(x, a0, l0); dma(x, a1, l1); dma(wqkv, b0, l2); dma(wqkv, b1, l3); }
    asm volatile("" : "+s"(bx), "+s"(ka), "+s"(kw));
    asm volatile("" : "+v"(tid));
    __builtin_assume(bx >= 0 && bx < 256 && tid >= 0 && tid < 512);
    x = (const char*)ka[0]; wqkv = (const char*)ka[1]; ws = (char*)kw;
  }
  const int lane = tid & 63, r16 = lane & 15, g = lane >> 4, q4 = r16 >> 2, pq = r16 & 3;
  const int xcd = bx & 7, rr = bx >> 3;
  const int pa = rr >> 1, ja = 2 * xcd + (rr & 1);
  const int pg = 8 * (xcd >> 2) + (rr >> 2), jg = 4 * (xcd & 3) + (rr & 3);
  const int half = (rr >> 2) & 1, slice = 4 * (xcd & 1) + (rr & 3);
  if (w == 0) {
    u64 pc; asm volatile("s_getpc_b64 %0" : "=s"(pc));
    const u32 o = rr * 1408 + (lane < 11 ? lane : 10) * 128;
    warm((const char*)(pc & ~127ull), o < CODE_SPAN ? o : CODE_SPAN, L_WARM);
  }
  constexpr int PW = 3;
  const __amdgpu_buffer_rsrc_t rs = __builtin_amdgcn_make_buffer_rsrc(ws, 0, WS_END, 0x00020000);
  u64 tag;
  auto load_epoch = [&](u32x2& e) { const u32 z = 0; asm volatile("global_load_dwordx2 %0, %1, %2 sc1" : "=v"(e) : "v"(z), "s"(ws + WS_EPOCH)); };
  auto make_tag = [&](u32x2& e) {
    asm volatile("" : "+v"(e));
    const u32 e0 = e[0], e1 = e[1];
    tag = (((u64)(u32)__builtin_amdgcn_readfirstlane(e1) << 32) | (u32)__builtin_amdgcn_readfirstlane(e0)) + TAG_STEP;
  };
  const char* xa = x + (size_t)p * 128 * 1280;
  const u32x4 ones8 = {ONES2, ONES2, ONES2, ONES2};
  const char* wt = ws + WS_WT + slice * WT_SLICE;
  auto rt_dst = [&](int piece, int blk, int gq) {
    const int tile = 4 * piece + g, sl = tile / 5, t = tile - sl * 5;
    return WS_WT + sl * WT_SLICE + blk * 5120 + t * 1024 + gq * 256 + r16 * 16;
  };
  auto warm_gu = [&]() INL {
    const int l6 = lane < 60 ? lane : 59, rw = l6 / 6;
    if ((w & 3) < 2) warm(((xcd & 4) ? wu : wg) + (size_t)(rr * 20 + (w & 1) * 10) * 3072 + (xcd & 3) * 768, rw * 3072 + (l6 - rw * 6) * 128, L_WARM);
  };
  u32 kvsrc[8], kvdst[8], fl_get;
  auto setup = [&]() INL {
#pragma unroll
    for (int i = 0; i < 8; ++i) {
      const int id = w + 8 * i, kv = id >= 30, r = id - 30 * kv, c = r / 6, r2 = r - 6 * c;
      int pp = pa - 2 + (r2 >> 1); pp = pp < 0 ? 0 : pp;
      kvsrc[i] = WS_QKV + (pp * 240 + 80 * (1 + kv) + 5 * ja + c) * 2048 + (r2 & 1) * 1024;
      kvdst[i] = (kv ? L_V + c * VCH : L_K + c * KCH) + r2 * 1024;
      asm volatile("" : "+s"(kvsrc[i]), "+s"(kvdst[i]));
    }
    const int e = r16 < 14 ? r16 : 0, sec = e < 2 ? 0 : (e < 8 ? 1 : 2), i = e < 2 ? e : (e - 2) % 6;
    int pp = e < 2 ? pa : pa - (i >> 1); pp = pp < 0 ? 0 : pp;
    fl_get = flag_off(0, pp, (80 * sec + 5 * ja + 4 * (i & 1)) >> 4);
    asm volatile("" : "+v"(fl_get));
  };

  auto ld_xt = [&]() INL {
    const int rw = lane / 10;
    const char* src = xa + (64 * half + 8 * w) * 1280 + slice * 160;
    if (lane < 40) {
      dma(src, rw * 1280 + (lane - 10 * rw) * 16, L_XT + w * 1280);
      dma(src + 4 * 1280, rw * 1280 + (lane - 10 * rw) * 16, L_XT + w * 1280 + 640);
    }
    SB();
  };
  if (j < 15) {
    const int wm = w & 1, wn = w >> 1;
    const char* wb = wqkv + j * 256;
    auto stage = [&](int c, int piece) {
      int kc = c % RG + rot; kc = (kc >= RG ? kc - RG : kc) + (c / RG) * RG;
      const u32 slot = (c & 3) * 32768;
      const char* a = xa + kc * 128; const char* b = wb + (size_t)kc * 64 * 3840;
      if (piece < 0 || piece == 0) dma(a, voffA[0], slot + w * 2048);
      if (piece < 0 || piece == 1) dma(a, voffA[1], slot + w * 2048 + 1024);
      if (piece < 0 || piece == 2) dma(b, voffB[0], slot + 16384 + w * 2048);
      if (piece < 0 || piece == 3) dma(b, voffB[1], slot + 16384 + w * 2048 + 1024);
    };
    STAMP(23);
    u32x2 epoch;
    load_epoch(epoch);
    SB();
    setup();
    STAMP(24);
    stage(1, -1);
    STAMP(25);
    u32 adA[2], adB[2];
#pragma unroll
    for (int ks = 0; ks < 2; ++ks) adA[ks] = (64 * wm + r16) * 128 + (((4 * ks + g) ^ ((r16 >> 1) & 7)) << 4);
#pragma unroll
    for (int n = 0; n < 2; ++n) adB[n] = 16384 + (8 * g + q4) * 256 + (((2 * wn + (n ^ wm)) ^ (q4 | ((g & 1) << 2))) << 5) + pq * 8;
    f32x4 acc[4][2], csum = {0, 0, 0, 0};
#pragma unroll
    for (int m = 0; m < 4; ++m) { acc[m][0] = f32x4{0, 0, 0, 0}; acc[m][1] = f32x4{0, 0, 0, 0}; }
    u32x4 fa[2][4]; u32x2 fb[2][2][2];
    u32x4 rsg[4];
    auto ld_rs = [&]() {
#pragma unroll
      for (int m = 0; m < 4; ++m) gld_sc1(rsg[m], ws + WS_RS + (p * 128 + 64 * wm + 16 * m) * 16, r16 * 16);
    };
    auto rdp = [&](int c, int ks, int i) {
      const u32 slot = (c & 3) * 32768;
      if (i == 0 || i == 1) fb[ks][0][i] = ldstr(slot + adB[0] + ks * 8192 + i * 1024);
      else if (i == 2) fa[ks][0] = lds128(slot + adA[ks]);
      else if (i == 3 || i == 4) fb[ks][1][i - 3] = ldstr(slot + adB[1] + ks * 8192 + (i - 3) * 1024);
      else if (i < 8) fa[ks][i - 4] = lds128(slot + adA[ks] + (i - 4) * 2048);
    };
    auto mm = [&](int ks, int cr, int cn) {
      const u32x4 b0 = cat(fb[ks][0][0], fb[ks][0][1]), b1 = cat(fb[ks][1][0], fb[ks][1][1]);
#pragma unroll
      for (int i = 0; i < 9; ++i) {
        if (i > 0) acc[(i - 1) >> 1][(i - 1) & 1] = mfma(((i - 1) & 1) ? b1 : b0, fa[ks][(i - 1) >> 1], acc[(i - 1) >> 1][(i - 1) & 1]);
        else csum = mfma(b0, ones8, csum);
        if (cr == 10 && i == 3) {
          if (r16 == 0) *(LAS f32x4*)(smem + L_MISC + 1024 + (32 * wn + 16 * wm + 4 * g) * 4) = csum;
          asm volatile("" ::: "memory");
          if (lane == 0) *(volatile LAS u32*)(smem + L_MISC + w * 4) = 1;
        }
        if (cr < 10) rdp(cr, ks ^ 1, i);
        if (cn < 10 && i == 4) stage(cn, 2 * (ks ^ 1));
        if (cn < 10 && i == 8) stage(cn, 2 * (ks ^ 1) + 1);
        SB();
      }
    };
    auto ld_wd = [&]() INL {
      if (j < 12 && w < 5) {
        dma(wd + (p * 12 + j) * 10240 + w * 256, (lane >> 3) * 1280 + (lane & 7) * 16, L_RT + w * 2048);
        dma(wd + (p * 12 + j) * 10240 + w * 256 + 128, (lane >> 3) * 1280 + (lane & 7) * 16, L_RT + w * 2048 + 1024);
      }
      SB();
    };
    if (tid < 8) *(LAS u32*)(smem + L_MISC + tid * 4) = 0;
    vm_wait<4>();
    make_tag(epoch);
    bar();
    STAMP(16);
#pragma unroll
    for (int i = 0; i < 8; ++i) rdp(0, 0, i);
    SB();
    stage(2, 0); stage(2, 1);
    SB();
#pragma unroll
    for (int c = 0; c < 10; ++c) {
      mm(0, c, c + 2);
      if (c < 9) {
        if (c == XLAT + 1) vm_wait<2>(); else if (c == WDAT + 1 && j < 12 && w < 5) vm_wait<6>(); else vm_wait<4>();
        bar0();
        if (c == RSAT) {
          ld_rs(); SB();
        }
        if (c == WDAT) ld_wd();
        if (c == XLAT) ld_xt();
      }
      mm(1, c + 1, c + 3);
    }
    STAMP(1);
    vm_wait<0>();
    for (u32 spins = 0; spins < SPIN_CAP; ++spins) {
      asm volatile("" : "+v"(rsg[0]), "+v"(rsg[1]), "+v"(rsg[2]), "+v"(rsg[3]));
      bool ok = true;
#pragma unroll
      for (int m = 0; m < 4; ++m) ok = ok && rsg[m][1] == (u32)tag && rsg[m][3] == (u32)(tag >> 32);
      if (__all(ok)) break;
      ld_rs();
      vm_wait<0>();
    }
    asm volatile("" : "+v"(rsg[0]), "+v"(rsg[1]), "+v"(rsg[2]), "+v"(rsg[3]));
    STAMP(27);
    while (*(volatile LAS u32*)(smem + L_MISC + (w ^ 1) * 4) == 0) {}
    asm volatile("" ::: "memory");
    STAMP(17);
    const float qs = j < 5 ? 0.22360679774997896f * 1.4426950408889634f : 1.f;
    f32x4 cs[2];
#pragma unroll
    for (int n = 0; n < 2; ++n) cs[n] = n == 0 ? csum : *(LAS const f32x4*)(smem + L_MISC + 1024 + (32 * wn + 16 * (1 ^ wm) + 4 * g) * 4);
    const u32 qb = WS_QKV + (p * 240 + 16 * j + 4 * wn + 2 * ((g & 1) ^ wm) + (g >> 1)) * 2048 + (64 * wm + r16) * 16;
#pragma unroll
    for (int m = 0; m < 4; ++m) {
      const u32 um = rsg[m][0], ur = rsg[m][2];
      const float mean = __uint_as_float(um), sc = __uint_as_float(ur) * qs;
      u32x2 pk[2];
#pragma unroll
      for (int n = 0; n < 2; ++n) {
        const f32x4 v = (acc[m][n] - mean * cs[n]) * sc;
        pk[n] = u32x2{pack2(v[0], v[1]), pack2(v[2], v[3])};
      }
      st128_sc1(rs, qb + m * 256, quad2chunk(pk[0], pk[1]));
    }
  } else {
    u32x2 epoch;
    load_epoch(epoch);
    auto ld_wo = [&]() INL {
#pragma unroll
      for (int i = 0; i < 7; ++i) if (w + 8 * i < 50) dma(wo + p * 51200 + (w + 8 * i) * 1024, lane * 16, (w + 8 * i) * 1024);
    };
    u32x4 xv[2][10];
#pragma unroll
    for (int t = 0; t < 10; ++t)
#pragma unroll
      for (int s2 = 0; s2 < 2; ++s2) gld(xv[s2][t], xa + (16 * w + 8 * s2) * 1280 + t * 128, (lane >> 3) * 1280 + (lane & 7) * 16);
    setup();
    const int wi = w < PW ? w : w - 1;
    u32 rdst[8], rsrc[8];
#pragma unroll
    for (int i = 0; i < 8; ++i) {
      const int id = wi + 7 * i, gr = id / 10;
      rsrc[i] = gr * 10240 + q4 * 1280 + (id - 10 * gr) * 128 + g * 32 + pq * 8;
      rdst[i] = rt_dst(id - 10 * gr, (p * 5 + gr) >> 2, (p * 5 + gr) & 3);
      asm volatile("" : "+v"(rsrc[i]), "+v"(rdst[i]));
    }
    if (tid == 0) *(LAS u32*)(smem + L_MISC) = 0;
    float s1[2] = {0.f, 0.f}, sq[2] = {0.f, 0.f};
#pragma unroll
    for (int t = 0; t < 10; ++t) {
      if (t == 0) { vm_wait<10>(); ld_wo(); }
      if (t == 5) vm_wait<10>();
      if (t == 8) vm_wait<6>();
#pragma unroll
      for (int s2 = 0; s2 < 2; ++s2) { asm volatile("" : "+v"(xv[s2][t])); s1[s2] = sum8(xv[s2][t], s1[s2]); sq[s2] = sq8(xv[s2][t], sq[s2]); }
    }
    make_tag(epoch);
#pragma unroll
    for (int o = 1; o < 8; o <<= 1) { s1[0] += __shfl_xor(s1[0], o); sq[0] += __shfl_xor(sq[0], o); s1[1] += __shfl_xor(s1[1], o); sq[1] += __shfl_xor(sq[1], o); }
    {
      const float a1 = (lane & 1) ? s1[1] : s1[0], a2 = (lane & 1) ? sq[1] : sq[0];
      const float mean = a1 * (1.f / 640.f), var = __builtin_fmaxf(a2 * (1.f / 640.f) - mean * mean, 0.f);
      if ((lane & 7) < 2)
        st128_sc1(rs, WS_RS + (p * 128 + 16 * w + 8 * (lane & 1) + (lane >> 3)) * 16, u32x4{__float_as_uint(mean), (u32)tag, __float_as_uint(__builtin_amdgcn_rsqf(var + LN_EPS)), (u32)(tag >> 32)});
    }
    STAMP(16);
    asm volatile("" ::: "memory");
    vm_wait<1>();
    bar();
    {
      auto relay = [&](int i) INL { if (wi + 7 * i < 50 && w != PW) st128_sc1(rs, rdst[i], ldstr2(rsrc[i], rsrc[i] + 5120)); };
#pragma unroll
      for (int i = 0; i < 4; ++i) relay(i);
      asm volatile("" ::: "memory");
      if (wi >= 2 && w != PW) {
        float a1 = 0.f;
#pragma unroll
        for (int t = 0; t < 10; ++t) a1 = sum8(lds128((wi - 2) * 10240 + (lane >> 3) * 1280 + t * 128 + (lane & 7) * 16), a1);
#pragma unroll
        for (int o = 1; o < 8; o <<= 1) a1 += __shfl_xor(a1, o);
        a1 *= (1.f / 640.f);
        const u32 hi = pack2(a1, 0.f), lo = pack2(a1 - bflo(hi), 0.f);
        if ((lane & 7) < 2) __builtin_amdgcn_raw_buffer_store_b16((u16)((lane & 1) ? lo : hi), rs, (int)(WS_WB + (lane & 1) * 1280 + ((p * 5 + wi - 2) * 8 + (lane >> 3)) * 2), 0, 16);
      }
      asm volatile("" ::: "memory");
#pragma unroll
      for (int i = 4; i < 8; ++i) relay(i);
      asm volatile("" ::: "memory");
      ld_xt();
    }
  }
  STAMP(18);
  int j2 = j;
  asm volatile("" : "+s"(j2));
  if (j2 < 15) vm_wait<0>();
  bar();
  STAMP(2);
  if (j2 < 15 && w == FW && lane == 0) __hip_atomic_store((GAS u64*)(ws + flag_off(0, p, j)), tag, RLX_AGENT);
  asm volatile("" ::: "memory");
  if (w != PW && w != 7) {
    const int kr = (w < PW ? w : w - 1) * 64 + lane;
    const bool valid = 128 * (pa - 2) + kr >= 0;
    *(LAS u32x4*)(smem + L_K + 5 * KCH + kr * 16) = u32x4{valid ? 0u : 0xf149u, 0u, ONES2, ONES2};
  }
  if (w == PW) poll_flags(ws, fl_get, tag);
  else if (j2 == 15) {
    vm_wait<2>();
    if (lane == 0 && __hip_atomic_fetch_add((LAS u32*)(smem + L_MISC), 1u, __ATOMIC_RELAXED, __HIP_MEMORY_SCOPE_WORKGROUP) == 6)
      __hip_atomic_store((GAS u64*)(ws + WS_WOF + p * 8), tag, RLX_AGENT);
    asm volatile("" ::: "memory");
  }
  bar();
  STAMP(3);

  u32x2 xr[6];
  u32 voffG[3];
  {
    const int h = w & 1, qt = w >> 1, l32 = lane & 31, hh = lane >> 5;
    u32x4 qf[2];
    gld_sc1(qf[0], ws + WS_QKV + (pa * 240 + 5 * ja + 2 * h) * 2048 + qt * 512, hh * 2048 + l32 * 16);
    gld_sc1(qf[1], ws + WS_QKV + (pa * 240 + 5 * ja + 2 * h + 2) * 2048 + qt * 512, l32 * 16);
#pragma unroll
    for (int i = 0; i < 5; ++i) dma_sc1(ws + kvsrc[i], lane * 16, kvdst[i]);
    u32 dm[8];
#pragma unroll
    for (int m = 0; m < 8; ++m) {
      const int k0 = ((2 * m) & 3) + 8 * (m >> 1) + 4 * hh;
      dm[m] = (k0 <= l32 ? 0xffffu : 0u) | (k0 + 1 <= l32 ? 0xffff0000u : 0u);
      asm volatile("" : "+v"(dm[m]));
    }
    u32 adK0 = L_K + (2 * h + hh) * KCH + (32 * qt + l32) * 16, adK1 = L_K + (hh ? 5 : 2 * h + 2) * KCH + (32 * qt + l32) * 16;
    const u32 vrow = 32 * qt + 4 * hh + q4;
    u32 adV = (g & 1) == 0 ? L_V + (3 * h + (pq >> 1)) * VCH + vrow * 16 + (pq & 1) * 8
                           : (pq == 0 ? L_V + 2 * VCH + vrow * 16 + 8 * h : L_K + 5 * KCH + ((4 * hh + (h ? 4 : 12)) & 15) * 16 + 8);
#pragma unroll
    for (int i = 0; i < 3; ++i) {
      const int s = 64 * i + lane, rw = s / 12, cp = s - rw * 12;
      int c = cp - (w & 1) - 2 * ((rw >> 3) & 1); c = c < 0 ? c + 12 : c;
      voffG[i] = rw * 3072 + c * 16;
    }
    asm volatile("" : "+v"(adK0), "+v"(adK1), "+v"(adV), "+v"(voffG[0]), "+v"(voffG[1]), "+v"(voffG[2]));
    vm_wait<1>();
    asm volatile("" : "+v"(qf[0]), "+v"(qf[1]));
    bar();
    STAMP(4);
    const f32x16 zero16 = {0, 0, 0, 0, 0, 0, 0, 0, 0, 0, 0, 0, 0, 0, 0, 0};
    u32x4 kf[9][2];
    constexpr int ord[9] = {0, 8, 1, 2, 3, 4, 5, 6, 7};
#pragma unroll
    for (int n = 0; n < 4; ++n) { kf[ord[n]][0] = lds128(adK0 + 512 * ord[n]); kf[ord[n]][1] = lds128(adK1 + 512 * ord[n]); }
    SB();
    dma_sc1(ws + kvsrc[5], lane * 16, kvdst[5]);
    if (h == 1 && hh == 0) { qf[0][0] = 0; qf[0][1] = 0; }
    if (h == 0) { qf[1][2] = 0; qf[1][3] = 0; }
    if (hh == 1) qf[1] = u32x4{0x3f80u, 0, 0, 0};
    SB();
    f32x16 sc[9];
    float m4[4];
    auto qk = [&](int i, int st) { sc[i] = mfma32(kf[i][st], qf[st], st == 0 ? zero16 : sc[i]); };
    qk(0, 0); qk(8, 0);
    SB();
    dma_sc1(ws + kvsrc[6], lane * 16, kvdst[6]);
    SB();
    qk(0, 1); qk(8, 1);
    SB();
    u32x2 wof;
    if (w < 4) {
      dma_sc1(ws + kvsrc[7], lane * 16, kvdst[7]);
      asm volatile("global_load_dwordx2 %0, %1, %2 sc1" : "=v"(wof) : "v"(r16 * 8), "s"(ws + WS_WOF));
    } else asm volatile("v_mov_b64 %0, 0" : "=v"(wof));
    SB();
#pragma unroll
    for (int n = 2; n < 11; ++n) {
      if (n < 9) { qk(ord[n], 0); SB(); }
      if (n + 2 < 9) { kf[ord[n + 2]][0] = lds128(adK0 + 512 * ord[n + 2]); SB(); }
      if (n == 3) {
#pragma unroll
        for (int r = 6; r < 16; ++r) sc[0][r] = ((r & 3) + 8 * (r >> 2) + 4 * hh <= l32) ? sc[8][r] : sc[0][r];
      } else if (n > 3) {
#pragma unroll
        for (int c = 0; c < 4; ++c) m4[c] = max3(m4[c], sc[n - 3][2 * c], sc[n - 3][2 * c + 1]);
      }
      SB();
      if (n < 9) { qk(ord[n], 1); SB(); }
      if (n + 2 < 9) { kf[ord[n + 2]][1] = lds128(adK1 + 512 * ord[n + 2]); SB(); }
      if (n == 2) {
#pragma unroll
        for (int r = 0; r < 6; ++r) sc[0][r] = ((r & 3) + 8 * (r >> 2) + 4 * hh <= l32) ? sc[8][r] : sc[0][r];
      } else if (n == 3) {
#pragma unroll
        for (int c = 0; c < 4; ++c) m4[c] = max3(max3(sc[0][4 * c], sc[0][4 * c + 1], sc[0][4 * c + 2]), sc[0][4 * c + 3], sc[0][4 * c + 3]);
      } else {
#pragma unroll
        for (int c = 0; c < 4; ++c) m4[c] = max3(m4[c], sc[n - 3][8 + 2 * c], sc[n - 3][9 + 2 * c]);
      }
      SB();
    }
    float mx = __builtin_fmaxf(max3(m4[0], m4[1], m4[2]), m4[3]);
    { auto sw = __builtin_amdgcn_permlane32_swap(__float_as_uint(mx), __float_as_uint(mx), false, false); mx = __builtin_fmaxf(__uint_as_float(sw[0]), __uint_as_float(sw[1])); }
    u32x4 nmf;
    {
      const float nmx = -mx;
      const u32 p0 = __float_as_uint(nmx) & 0xffff0000u;
      const float r1 = nmx - __uint_as_float(p0);
      const u32 p1 = __float_as_uint(r1) & 0xffff0000u;
      const u32 p2 = __float_as_uint(r1 - __uint_as_float(p1));
      nmf = u32x4{hh ? p2 >> 16 : (p0 >> 16) | p1, 0, 0, 0};
    }
    auto submax = [&](int i) { sc[i] = mfma32(ones8, nmf, sc[i]); };
    submax(0); submax(1);
    SB();
    u32x4 rtv[3];
    if (w < 4) {
#pragma unroll
      for (int i = 0; i < 3; ++i) { const u32 l = L_RT + (w + 4 * i) * 1024 + q4 * 128 + g * 32 + pq * 8; rtv[i] = ldstr2(l, l + 512); }
    }
    STAMP(29);
    vm_wait<0>();
    bar();
    STAMP(26);
    if (w < 4) {
      if (j < 12) {
        const int gi = p * 12 + j;
#pragma unroll
        for (int i = 0; i < 3; ++i)
          if (w + 4 * i < 10) st128_sc1(rs, rt_dst(w + 4 * i, 20 + (gi >> 2), gi & 3), rtv[i]);
      }
      asm volatile("" : "+v"(wof));
      if (!__all(wof[0] == (u32)tag && wof[1] == (u32)(tag >> 32))) poll_flags<0>(ws, WS_WOF + r16 * 8, tag);
#pragma unroll
      for (int i = 0; i < 20; ++i) {
        const u32 o = 20480 + (w * 20 + i) * 1024;
        dma_sc1(wt + o, lane * 16, L_WOB + o);
      }
      asm volatile("global_load_dwordx2 %0, %1, %2 sc1" : "=v"(xr[5]) : "v"(r16 * 16), "s"(ws + WS_RS + (p * 128 + 64 * half + 16 * w) * 16));
      warm_gu();
    } else {
      asm volatile("v_mov_b64 %0, 0" : "=v"(xr[5]));
    }
    SB();
    f32x16 o = zero16;
    u32 pw[2][8];
    u32x2 vf[2][4], vx[4];
    auto rdV = [&](int i, int e) { return ldstr(adV + 512 * i + 128 * e); };
    auto pfrag = [&](const u32* q, int st) { return u32x4{q[4 * st], q[4 * st + 1], q[4 * st + 2], q[4 * st + 3]}; };
    u32 p8[8];
#pragma unroll
    for (int n = 0; n < 8; ++n) {
      float ex[16];
#pragma unroll
      for (int e = 0; e < 8; ++e) {
        ex[2 * e] = __builtin_amdgcn_exp2f(sc[n][2 * e]); SB();
        if (e < 4) { vf[n & 1][e] = rdV(n, e); SB(); }
        else if (n == 0) { vx[e - 4] = rdV(8, e - 4); SB(); }
        ex[2 * e + 1] = __builtin_amdgcn_exp2f(sc[n][2 * e + 1]); SB();
        if (e > 0) { pw[n & 1][e - 1] = pack2(ex[2 * e - 2], ex[2 * e - 1]); SB(); }
        if (n >= 1) {
          if (e == 0 && n < 7) { submax(n + 1); SB(); }
          if (e == 3) { o = mfma32(cat(vf[(n - 1) & 1][0], vf[(n - 1) & 1][1]), pfrag(pw[(n - 1) & 1], 0), o); SB(); }
          if (e == 6) { o = mfma32(cat(vf[(n - 1) & 1][2], vf[(n - 1) & 1][3]), pfrag(pw[(n - 1) & 1], 1), o); SB(); }
          if (n == 2 && e == 2) { o = mfma32(cat(vx[0], vx[1]), pfrag(p8, 0), o); SB(); }
          if (n == 2 && e == 5) { o = mfma32(cat(vx[2], vx[3]), pfrag(p8, 1), o); SB(); }
        }
      }
      pw[n & 1][7] = pack2(ex[14], ex[15]);
      if (n == 0) {
#pragma unroll
        for (int m = 0; m < 8; ++m) { p8[m] = pw[0][m] & dm[m]; pw[0][m] &= ~dm[m]; }
      }
      SB();
    }
    o = mfma32(cat(vf[1][0], vf[1][1]), pfrag(pw[1], 0), o);
    o = mfma32(cat(vf[1][2], vf[1][3]), pfrag(pw[1], 1), o);
    const float inv = __builtin_amdgcn_rcpf(o[12]);
    const u32 x0 = pack2(o[0] * inv, o[1] * inv), x1 = pack2(o[2] * inv, o[3] * inv), y0 = pack2(o[4] * inv, o[5] * inv), y1 = pack2(o[6] * inv, o[7] * inv);
    auto s0 = __builtin_amdgcn_permlane32_swap(x0, y0, false, false);
    auto s1 = __builtin_amdgcn_permlane32_swap(x1, y1, false, false);
    const u32 ob = WS_O + (pa * 80 + 5 * ja) * 2048 + (32 * qt + l32) * 16;
    st128_sc1(rs, ob + (3 * h + hh) * 2048, u32x4{s0[0], s1[0], s0[1], s1[1]});
    if (hh == 0) st64_sc1(rs, ob + 2 * 2048 + 8 * h, u32x2{pack2(o[8] * inv, o[9] * inv), pack2(o[10] * inv, o[11] * inv)});
    STAMP(5);
    {
#pragma unroll
      for (int t = 0; t < 5; ++t) xr[t] = w < 4 ? *(LAS const u32x2*)(smem + L_XT + (16 * w + r16) * 160 + t * 32 + g * 8) : u32x2{0, 0};
    }
  }
  const char* gsrc = ((w & 1) ? wu : wg) + jg * 192;
  auto stageGBp = [&](int c, int rb, int i) {
    dma(gsrc + (size_t)(c * 64 + rb * 16) * 3072, voffG[i], gb_off(c) + (w & 1) * 12288 + rb * 3072 + i * 1024);
  };
  auto stageGB = [&](int c, int rb) { stageGBp(c, rb, 0); stageGBp(c, rb, 1); stageGBp(c, rb, 2); };
  const int wm4 = w & 3, wk = w >> 2, kh = wk;
  u32 voffR = g * 2048 + (64 * half + 16 * wm4 + r16) * 16;
  fl_get = flag_off(1, p, r16);
  u32 fl_get2 = flag_off(2, pg, r16), fl_put = lane == 0 ? flag_off(1, pa, ja) : WS_DENSE + bx * 8;
  asm volatile("" : "+v"(fl_put));
  asm volatile("" : "+v"(voffR), "+v"(fl_get), "+v"(fl_get2));
  asm volatile("" ::: "memory");
  auto late_wo = [&](int q) INL {
#pragma unroll
    for (int i = 0; i < 5; ++i) dma_sc1(wt + 5120 * q + i * 1024, lane * 16, L_WOB + 5120 * q + i * 1024);
    dma_sc1(ws + WS_WB + (q < 3 ? q : 2) * 1024, lane * 16, L_WB + (q < 3 ? q : 2) * 1024);
  };
  vm_wait<0>();
  bar();
  STAMP(6);
  u32x2 hp[5];
  {
    if (w == FW && lane < 2) __hip_atomic_store((GAS u64*)(ws + fl_put), tag, RLX_AGENT);
    if (w >= 4) late_wo(w - 4);
    u32x4 a[10], b[6][5];
    u32 ad0 = L_WOB + kh * 10 * 5120 + lane * 16, adm = L_WB + (r16 & 1) * 1280 + kh * 640 + g * 16;
    asm volatile("" : "+v"(ad0), "+v"(adm));
    auto kso = [&](int s) { return (s + 4) % 10; };
    auto rdB = [&](int s, int t) { b[s % 6][t] = lds128(ad0 + kso(s) * 5120 + t * 1024); };
#pragma unroll
    for (int s = 0; s < 6; ++s)
#pragma unroll
      for (int t = 0; t < 5; ++t) rdB(s, t);
    asm volatile("" : "+v"(xr[0]), "+v"(xr[1]), "+v"(xr[2]), "+v"(xr[3]), "+v"(xr[4]), "+v"(xr[5]));
    f32x4 hacc[5], macc;
    { const u32 xm = xr[5][0]; macc = f32x4{__uint_as_float(xm), 0.f, 0.f, 0.f}; }
#pragma unroll
    for (int t = 0; t < 5; ++t) { const u32 a0 = xr[t][0], a1 = xr[t][1]; hacc[t] = f32x4{bflo(a0), bfhi(a0), bflo(a1), bfhi(a1)}; }
    SB();
    if (w == PW) poll_flags(ws, fl_get, tag); else if (w >= 4) vm_wait<0>();
    bar();
    STAMP(7);
    const char* osrc = ws + WS_O + (p * 80 + 40 * kh) * 2048;
#pragma unroll
    for (int s = 0; s < 10; ++s) gld_sc1(a[s], osrc + kso(s) * 8192, voffR);
    u32x4 bm[2];
    bm[0] = lds128(adm + kso(0) * 64);
    SB();
#pragma unroll
    for (int s = 0; s < 10; ++s) {
      switch (s) {
        case 0: vm_wait_use<9>(a[0]); break; case 1: vm_wait_use<8>(a[1]); break; case 2: vm_wait_use<7>(a[2]); break;
        case 3: vm_wait_use<6>(a[3]); break; case 4: vm_wait_use<5>(a[4]); break; case 5: vm_wait_use<4>(a[5]); break;
        case 6: vm_wait_use<3>(a[6]); break; case 7: vm_wait_use<2>(a[7]); break; case 8: vm_wait_use<1>(a[8]); break;
        default: vm_wait_use<0>(a[9]); break;
      }
      if (s == 0) STAMP(28);
#pragma unroll
      for (int t = 0; t < 5; ++t) {
        hacc[t] = mfma(b[s % 6][t], a[s], hacc[t]);
        SB();
        if (s + 6 < 10) { rdB(s + 6, t); SB(); }
        if (t == 0 && s < 9) { bm[(s + 1) & 1] = lds128(adm + kso(s + 1) * 64); SB(); }
      }
      macc = mfma(bm[s & 1], a[s], macc);
      SB();
    }
    STAMP(8);
    if (wk == 0) {
#pragma unroll
      for (int t = 0; t < 5; ++t) *(LAS f32x4*)(smem + L_RED + (wm4 * 5 + t) * 1024 + lane * 16) = hacc[t];
      *(LAS float*)(smem + L_RED + 20480 + wm4 * 256 + lane * 4) = macc[0] + macc[1];
      stageGB(2, w >> 1); stageGB(2, 2 + (w >> 1));
    }
    bar();
    if (wk == 1) {
      float s1 = 0.f, s2 = 0.f;
      const float mean = macc[0] + macc[1] + *(LAS const float*)(smem + L_RED + 20480 + wm4 * 256 + lane * 4);
      u32x2 hc[5];
#pragma unroll
      for (int t = 0; t < 5; ++t) {
        const f32x4 v = hacc[t] + *(LAS const f32x4*)(smem + L_RED + (wm4 * 5 + t) * 1024 + lane * 16);
        const u32 lo = pack2(v[0], v[1]), hi = pack2(v[2], v[3]);
        hp[t] = u32x2{lo, hi};
        hc[t] = u32x2{pack2(bflo(lo) - mean, bfhi(lo) - mean), pack2(bflo(hi) - mean, bfhi(hi) - mean)};
      }
      const u32 hb = WS_H + (p * 80 + 10 * slice + 2 * (g & 1) + (g >> 1)) * 2048 + (64 * half + 16 * wm4 + r16) * 16;
      st128_sc1(rs, hb, quad2chunk(hc[0], hc[1]));
      st128_sc1(rs, hb + 4 * 2048, quad2chunk(hc[2], hc[3]));
      const u32x4 last = quad2chunk(hc[4], hc[4]);
      if ((g & 1) == 0) st128_sc1(rs, hb + 8 * 2048, last);
#pragma unroll
      for (int t = 0; t < 5; ++t) { const u32 lo = hc[t][0], hi = hc[t][1]; s1 = dot2(hi, ONES2, dot2(lo, ONES2, s1)); s2 = dot2(hi, hi, dot2(lo, lo, s2)); }
      s1 = gsum(s1); s2 = gsum(s2);
      if (g == 0) st64_sc1(rs, WS_ST + (p * 8 + slice) * 1024 + (64 * half + 16 * wm4 + r16) * 8, u32x2{__float_as_uint(s1), __float_as_uint(s2)});
      vm_wait<0>();
    } else {
#pragma unroll
      for (int t = 0; t < 5; ++t) hp[t] = u32x2{0, 0};
      stageGB(0, w >> 1); stageGB(0, 2 + (w >> 1));
    }
  }
  bar();
  STAMP(9);
  if (w == FW && lane == 0) __hip_atomic_store((GAS u64*)(ws + flag_off(2, p, j)), tag, RLX_AGENT);
  if (w == PW) {
    poll_flags<PV2>(ws, fl_get2, tag);
  } else if (w == 2) {
    GAS u64* a = (GAS u64*)(ws + WS_DENSE + lane * 8);
    for (u32 spins = 0; spins < SPIN_CAP; ++spins) {
      const u64 v0 = __hip_atomic_load(a, RLX_AGENT), v1 = __hip_atomic_load(a + 64, RLX_AGENT), v2 = __hip_atomic_load(a + 128, RLX_AGENT), v3 = __hip_atomic_load(a + 192, RLX_AGENT);
      if (__all(v0 == tag && v1 == tag && v2 == tag && v3 == tag)) break;
    }
    if (bx == 0 && lane == 0) __hip_atomic_store((GAS u64*)(ws + WS_EPOCH), tag, RLX_AGENT);
  } else if (wk == 1) {
    stageGB(1, w >> 1); stageGB(1, (w >> 1) - 2);
  }
  bar();
  STAMP(10);

  auto stageDq = [&](int c, int blk, int i) {
    const int kc = c;
    dma_sc1(wt + (20 + kc * 8 + blk) * 5120 + i * 1024, lane * 16, (c & 3) * 40960 + blk * 5120 + i * 1024);
  };
  auto stageDp = [&](int c, int i) { stageDq(c, w, i); };

  {
    const int wm = w >> 2, wn = w & 3;
    const char* hsrc = ws + WS_H + (pg * 80 + w) * 2048;
    auto stageGAp = [&](int c, int i) { dma_sc1(hsrc + c * 16384 + i * 1024, lane * 16, ga_off(c) + w * 2048 + i * 1024); };
    auto stageGA = [&](int c) { stageGAp(c, 0); stageGAp(c, 1); };
    stageGA(0);
    dma_sc1(ws + WS_ST + (pg * 8 + w) * 1024, lane * 16, L_ST + w * 1024);
    stageGA(1);
    const u32 adA = g * 2048 + (64 * wm + r16) * 16;
    u32 adB[3];
#pragma unroll
    for (int t = 0; t < 3; ++t) {
      const int mat = t == 0 ? 0 : (t == 1 ? 1 : (pq >> 1)), c = t < 2 ? 3 * wn + (pq >> 1) : 3 * wn + 2;
      int cp = c + mat + 2 * (g & 1); cp = cp >= 12 ? cp - 12 : cp;
      adB[t] = mat * 12288 + (8 * g + q4) * 192 + cp * 16 + (pq & 1) * 8;
    }
    f32x4 acc[4][3];
#pragma unroll
    for (int m = 0; m < 4; ++m)
#pragma unroll
      for (int t = 0; t < 3; ++t) acc[m][t] = f32x4{0, 0, 0, 0};
    u32x4 fa[2][4]; u32x2 fb[2][3][2];
    auto rdp = [&](int c, int s, int i) {
      const u32 sa = ga_off(c) + s * 8192, sb = gb_off(c) + s * 6144;
      if (i < 2) fb[s][0][i] = ldstr(sb + adB[0] + i * 768);
      else if (i == 2) fa[s][0] = lds128(sa + adA);
      else if (i < 7) fb[s][1 + ((i - 3) >> 1)][(i - 3) & 1] = ldstr(sb + adB[1 + ((i - 3) >> 1)] + ((i - 3) & 1) * 768);
      else if (i < 10) fa[s][i - 6] = lds128(sa + adA + (i - 6) * 256);
    };
    auto mm = [&](int s, int c) {
      u32x4 b[3];
#pragma unroll
      for (int t = 0; t < 3; ++t) b[t] = cat(fb[s][t][0], fb[s][t][1]);
      const int cr = s == 0 ? c : c + 1;
#pragma unroll
      for (int i = 0; i < 12; ++i) {
        acc[i / 3][i % 3] = mfma(b[i % 3], fa[s][i / 3], acc[i / 3][i % 3]);
        if (cr < 10) rdp(cr, s ^ 1, i);
        if (s == 1 && (i == 4 || i == 10) && c + 3 < 10) stageGAp(c + 3, i == 10);
        if (s == 0 && i % 4 == 3 && c >= 1 && c + 3 < 10) stageGBp(c + 3, w >> 1, i >> 2);
        if (s == 1 && (c == 7 || c == 8) && i % 2 == 1 && i < 10) stageDp(c - 7, i >> 1);
        SB();
      }
    };
    vm_wait<3>();
    bar();
    STAMP(19);
#pragma unroll
    for (int i = 0; i < 10; ++i) rdp(0, 0, i);
    SB();
    stageGA(2);
    stageGB(3, w >> 1);
    SB();
#pragma unroll
    for (int c = 0; c < 10; ++c) {
      if (c == 1) {
        const f32x2 u0 = *(LAS const f32x2*)(smem + L_ST + (2 * g) * 1024 + (16 * w + r16) * 8), u1 = *(LAS const f32x2*)(smem + L_ST + (2 * g + 1) * 1024 + (16 * w + r16) * 8);
        const float s1 = gsum(u0[0] + u1[0]), s2 = gsum(u0[1] + u1[1]);
        const float mean = s1 * (1.f / 640.f), var = __builtin_fmaxf(s2 * (1.f / 640.f) - mean * mean, 0.f);
        if (g == 0) *(LAS float*)(smem + L_MISC + (16 * w + r16) * 4) = __builtin_amdgcn_rcpf(var + LN_EPS);
      }
      mm(0, c);
      if (c < 9) {
        if (c == 0) vm_wait<5>(); else if (c <= 6) vm_wait<8>(); else vm_wait<5>();
        bar();
        if (c == 4) STAMP(20);
        if (c == 7) STAMP(30);
      }
      mm(1, c);
    }
    STAMP(11);
    u32x2 p01[4];
    float r2[4];
#pragma unroll
    for (int m = 0; m < 4; ++m) r2[m] = *(LAS const float*)(smem + L_MISC + (64 * wm + 16 * m + r16) * 4);
    if (wm == 1) { vm_wait<5>(); bar(); }
#pragma unroll
    for (int m = 0; m < 4; ++m) {
      const f32x4 v = acc[m][0] * acc[m][1] * r2[m];
      p01[m] = u32x2{pack2(v[0], v[1]), pack2(v[2], v[3])};
    }
    const u32 hb = WS_HH + (pg * 192 + 12 * jg + 3 * wn) * 2048 + (64 * wm + r16) * 16;
    st128_sc1(rs, hb + (g >> 1) * 2048 + (g & 1) * 256, quad2chunk(p01[0], p01[1]));
    st128_sc1(rs, hb + (g >> 1) * 2048 + (2 + (g & 1)) * 256, quad2chunk(p01[2], p01[3]));
    u32x2 p2[2];
#pragma unroll
    for (int mm2 = 0; mm2 < 2; ++mm2) {
      f32x4 v;
#pragma unroll
      for (int r = 0; r < 4; ++r) {
        auto sw = __builtin_amdgcn_permlane32_swap(__float_as_uint(acc[2 * mm2][2][r]), __float_as_uint(acc[2 * mm2 + 1][2][r]), false, false);
        v[r] = __uint_as_float(sw[0]) * __uint_as_float(sw[1]) * (lane < 32 ? r2[2 * mm2] : r2[2 * mm2 + 1]);
      }
      p2[mm2] = u32x2{pack2(v[0], v[1]), pack2(v[2], v[3])};
    }
    st128_sc1(rs, hb + 2 * 2048 + (2 * (g & 1) + (g >> 1)) * 256, quad2chunk(p2[0], p2[1]));
    STAMP(31);
    if (wm == 0) {
      vm_wait<8>();
      bar();
    }
  }

  {
    u32x4 b[6][5];
    u32 ad0 = kh * 4 * 5120 + lane * 16, ad2 = ad0 + 81920;
    asm volatile("" : "+v"(ad0), "+v"(ad2));
    auto rdB = [&](int s, int t) { b[s % 6][t] = lds128(((s & 8) ? ad2 : ad0) + ((s >> 2) & 1) * 40960 + (s & 3) * 5120 + t * 1024); };
#pragma unroll
    for (int s = 0; s < 4; ++s)
#pragma unroll
      for (int t = 0; t < 5; ++t) rdB(s, t);
    SB();
    vm_wait<0>();
    bar();
    STAMP(12);
    if (w == FW && lane == 0) __hip_atomic_store((GAS u64*)(ws + flag_off(3, pg, jg)), tag, RLX_AGENT);
    if (wk == 1) {
#pragma unroll
      for (int i = 0; i < 20; ++i) stageDq(2 + i / 10, w - 4 * ((i / 5) & 1), i % 5);
    }
#pragma unroll
    for (int s = 4; s < 6; ++s)
#pragma unroll
      for (int t = 0; t < 5; ++t) rdB(s, t);
    f32x4 acc[5];
#pragma unroll
    for (int t = 0; t < 5; ++t) { const u32 a = hp[t][0], b2 = hp[t][1]; acc[t] = f32x4{bflo(a), bfhi(a), bflo(b2), bfhi(b2)}; }
    SB();
    if (w == PW) poll_flags<PV2>(ws, fl_get + 2 * 65536, tag); else vm_wait<10>();
    bar();
    STAMP(13);
    u32x4 a[6][4];
    const char* asrc = ws + WS_HH + (p * 192 + 16 * kh) * 2048;
    auto loadAp = [&](int c, int s) {
      const int kc = c;
      gld_sc1(a[c][s], asrc + kc * 65536 + s * 8192, voffR);
    };
    auto issue = [&](int n) {
      if (n < 4) loadAp(2, n); else if (n < 9) stageDp(4, n - 4); else if (n < 13) loadAp(3, n - 9); else if (n < 18) stageDp(5, n - 13);
      else if (n < 22) loadAp(4, n - 18); else if (n < 26) loadAp(5, n - 22);
    };
#pragma unroll
    for (int s = 0; s < 4; ++s) loadAp(0, s);
#pragma unroll
    for (int s = 0; s < 4; ++s) loadAp(1, s);
#pragma unroll
    for (int s = 0; s < 24; ++s) {
      if (s == 0) vm_wait<4>();
      if (s == 4) vm_wait<8>();
      if (s == 8) vm_wait<12>();
      if (s == 10) vm_wait<11>();
      if (s == 12) vm_wait<11>();
      if (s == 14) vm_wait<8>();
      if (s == 16) vm_wait<4>();
      if (s == 20) vm_wait<0>();
      if (s % 4 == 0) asm volatile("" : "+v"(a[s >> 2][0]), "+v"(a[s >> 2][1]), "+v"(a[s >> 2][2]), "+v"(a[s >> 2][3]));
      if (s == 6) bar();
      if (s == 10 || s == 14) bar0();
      if (s == 0) STAMP(21);
#pragma unroll
      for (int t = 0; t < 5; ++t) {
        acc[t] = mfma(b[s % 6][t], a[s >> 2][s & 3], acc[t]);
        SB();
        if (s + 6 < 24) { rdB(s + 6, t); SB(); }
        if (t == 1) { issue(2 * s); SB(); }
        if (t == 4) { issue(2 * s + 1); SB(); }
      }
    }
    STAMP(14);
    constexpr u32 L_RED2 = 2 * 40960;
    if (wk == 0) {
#pragma unroll
      for (int t = 0; t < 5; ++t) *(LAS f32x4*)(smem + L_RED2 + (wm4 * 5 + t) * 1024 + lane * 16) = acc[t];
    }
    bar();
    if (wk == 1) {
      u32x2 op[5];
#pragma unroll
      for (int t = 0; t < 5; ++t) {
        const f32x4 v = acc[t] + *(LAS const f32x4*)(smem + L_RED2 + (wm4 * 5 + t) * 1024 + lane * 16);
        op[t] = u32x2{pack2(v[0], v[1]), pack2(v[2], v[3])};
      }
      const __amdgpu_buffer_rsrc_t ro = __builtin_amdgcn_make_buffer_rsrc(out, 0, 2048 * 1280, 0x00020000);
      const u32 oo = (128 * p + 64 * half + 16 * wm4 + r16) * 1280 + slice * 160 + (2 * (g & 1) + (g >> 1)) * 16;
      __builtin_amdgcn_raw_buffer_store_b128(quad2chunk(op[0], op[1]), ro, (int)oo, 0, 0);
      __builtin_amdgcn_raw_buffer_store_b128(quad2chunk(op[2], op[3]), ro, (int)oo + 64, 0, 0);
      const u32x4 last = quad2chunk(op[4], op[4]);
      if ((g & 1) == 0) __builtin_amdgcn_raw_buffer_store_b128(last, ro, (int)oo + 128, 0, 0);
    }
    STAMP(15);
  }
}

extern "C" void kernel_launch(void* const* d_in, const int* in_sizes, int n_in, void* d_out, int out_size, void* d_ws, size_t ws_size, hipStream_t stream) {
  (void)in_sizes; (void)n_in; (void)out_size; (void)ws_size;
  (void)hipFuncSetAttribute((const void*)fused_block, hipFuncAttributeMaxDynamicSharedMemorySize, LDS_BYTES);
  fused_block<<<256, 512, LDS_BYTES, stream>>>((const char*)d_in[0], (const char*)d_in[1], (const char*)d_in[2], (const char*)d_in[4], (const char*)d_in[5],
                                               (const char*)d_in[6], (char*)d_out, (char*)d_ws);
}
```
